```python
import torch

def _softmax(x, axis=-1):
    return torch.softmax(x, dim=axis)
def _gelu(x):
    return torch.nn.functional.gelu(x, approximate="tanh")
def _silu(x):
    return torch.nn.functional.silu(x)
def _sigmoid(x):
    return torch.sigmoid(x)
def _layernorm(x, axis=-1, eps=1e-5):
    m = x.mean(dim=axis, keepdim=True)
    v = x.var(dim=axis, keepdim=True, unbiased=False)
    return (x - m) / torch.sqrt(v + eps)
def _scatter_add(tgt, idx, src, axis=0):
    return tgt.clone().index_add_(axis, idx, src)
def _segment_sum(src, seg, n):
    out = torch.zeros((n,) + src.shape[1:], dtype=src.dtype)
    return out.index_add_(0, seg, src)
def _pad(x, pw):
    flat = tuple(v for pair in reversed(pw) for v in pair)
    return torch.nn.functional.pad(x, flat)

kernel_name = 'gated_mix__ssm_conv_s5__ssm_conv_s4__prompt'

def setup_inputs():

    _w = (lambda a: (lambda t: t.to(torch.bfloat16) if t.is_floating_point() else t)(torch.as_tensor(a)))
    def _A_setup():
        B, S, D = 1, 2048, 640
        P = D
        Lambda_re = -0.5 * (lambda _x: torch.exp(torch.as_tensor(_x)))((lambda *s: torch.randn(*s, dtype=torch.bfloat16) * 0.2)(P))
        Lambda_im = (lambda *s: torch.randn(*s, dtype=torch.bfloat16) * 0.4)(P) * 1.0
        B_re = (lambda *s: torch.randn(*s, dtype=torch.bfloat16) * 0.2)(P, D) * (D ** -0.5)
        B_im = (lambda *s: torch.randn(*s, dtype=torch.bfloat16) * 0.2)(P, D) * (D ** -0.5)
        C_re = (lambda *s: torch.randn(*s, dtype=torch.bfloat16) * 0.2)(D, P) * (P ** -0.5)
        C_im = (lambda *s: torch.randn(*s, dtype=torch.bfloat16) * 0.2)(D, P) * (P ** -0.5)
        log_dt = (lambda *s: torch.randn(*s, dtype=torch.bfloat16) * 0.2)(P) * 0.3 - 2.0
        Dskip = (lambda *s: torch.randn(*s, dtype=torch.bfloat16) * 0.05)(D)
        return {
            "x": _w((lambda *s: torch.randn(*s, dtype=torch.bfloat16) * 0.05)(B, S, D)),
            "Lambda_re": _w(Lambda_re),
            "Lambda_im": _w(Lambda_im),
            "B_re": _w(B_re),
            "B_im": _w(B_im),
            "C_re": _w(C_re),
            "C_im": _w(C_im),
            "log_dt": _w(log_dt),
            "Dskip": _w(Dskip),
        }
    _A_in = _A_setup()

    def _B_setup():
        B, L, D, N = 1, 2048, 640, 128
        A_re = -(lambda _x: torch.exp(torch.as_tensor(_x)))((lambda *s: torch.randn(*s, dtype=torch.bfloat16) * 3.2)(D, N))
        A_im = torch.pi * (lambda *s: torch.randn(*s, dtype=torch.bfloat16) * 0.05)(D, N)
        B_re = (lambda *s: torch.randn(*s, dtype=torch.bfloat16) * 0.1)(D, N)
        B_im = (lambda *s: torch.randn(*s, dtype=torch.bfloat16) * 0.1)(D, N)
        C_re = (lambda *s: torch.randn(*s, dtype=torch.bfloat16) * 0.1)(D, N)
        C_im = (lambda *s: torch.randn(*s, dtype=torch.bfloat16) * 0.1)(D, N)
        log_dt = (lambda *s: torch.randn(*s, dtype=torch.bfloat16) * 0.05)(D) * 0.5 - 4.0
        Dskip = (lambda *s: torch.randn(*s, dtype=torch.bfloat16) * 0.05)(D)
        return {
            "u": _w((lambda *s: torch.randn(*s, dtype=torch.bfloat16) * 0.05)(B, L, D)),
            "A_re": _w(A_re),
            "A_im": _w(A_im),
            "B_re": _w(B_re),
            "B_im": _w(B_im),
            "C_re": _w(C_re),
            "C_im": _w(C_im),
            "log_dt": _w(log_dt),
            "Dskip": _w(Dskip),
        }
    _B_in = _B_setup()

    return {
        "A_x": _A_in["x"],
        "A_Lambda_re": _A_in["Lambda_re"],
        "A_Lambda_im": _A_in["Lambda_im"],
        "A_B_re": _A_in["B_re"],
        "A_B_im": _A_in["B_im"],
        "A_C_re": _A_in["C_re"],
        "A_C_im": _A_in["C_im"],
        "A_log_dt": _A_in["log_dt"],
        "A_Dskip": _A_in["Dskip"],
        "B_u": _B_in["u"],
        "B_A_re": _B_in["A_re"],
        "B_A_im": _B_in["A_im"],
        "B_B_re": _B_in["B_re"],
        "B_B_im": _B_in["B_im"],
        "B_C_re": _B_in["C_re"],
        "B_C_im": _B_in["C_im"],
        "B_log_dt": _B_in["log_dt"],
        "B_Dskip": _B_in["Dskip"],
        "gate_w": _w((lambda *s: torch.randn(*s, dtype=torch.bfloat16) * 0.05)(640) * 16.0),
    }

def reference(A_x, A_Lambda_re, A_Lambda_im, A_B_re, A_B_im, A_C_re, A_C_im, A_log_dt, A_Dskip, B_u, B_A_re, B_A_im, B_B_re, B_B_im, B_C_re, B_C_im, B_log_dt, B_Dskip, gate_w):
    def _A_ref(x, Lambda_re, Lambda_im, B_re, B_im, C_re, C_im, log_dt, Dskip):
        Bsz, L, H = x.shape
        Lam = Lambda_re + 1j * Lambda_im
        Bmat = B_re + 1j * B_im
        Cmat = C_re + 1j * C_im
        dt = torch.exp(log_dt)
        Lam_bar = torch.exp(dt * Lam)
        B_bar = ((Lam_bar - 1.0) / Lam)[:, None] * Bmat
        Bu = torch.einsum("bld,pd->blp", x + 0j, B_bar)
        h = torch.zeros_like(Bu[:, 0, :])
        hs = []
        for t in range(L):
            h = Lam_bar[None, :] * h + Bu[:, t, :]
            hs.append(h)
        hseq = torch.stack(hs, 1)
        y = 2.0 * torch.einsum("blp,dp->bld", hseq, Cmat).real + x * Dskip[None, None, :]
        return y

    def _B_ref(u, A_re, A_im, B_re, B_im, C_re, C_im, log_dt, Dskip):
        Bsz, L, D = u.shape
        dt = torch.exp(log_dt)[:, None]
        A = A_re + 1j * A_im
        Bc = B_re + 1j * B_im
        Cc = C_re + 1j * C_im
        logdA = dt * A
        dB = dt * Bc
        l = (lambda x, dt: x.to(dt))(torch.arange(L), A_re.dtype)
        P = torch.exp(logdA[:, :, None] * l[None, None, :])
        K = torch.einsum("dn,dnl,dn->dl", Cc, P, dB).real
        L2 = 2 * L
        Kp = _pad(K, ((0, 0), (0, L)))
        up = (lambda x, *d: x.permute(*d))(u, 0, 2, 1)
        up = _pad(up, ((0, 0), (0, 0), (0, L)))
        Kf = (lambda x, axes=(-2,-1): torch.fft.rfft2(x.float(), dim=axes))(Kp[:, None, :], axes=(-2, -1))
        Uf = (lambda x, axes=(-2,-1): torch.fft.rfft2(x.float(), dim=axes))(up[:, :, None, :], axes=(-2, -1))
        Yf = Uf * Kf[None, :, :, :]
        y = (lambda x, s, axes=(-2,-1): torch.fft.irfft2(x, s=s, dim=axes).to(torch.bfloat16))(Yf, (1, L2), axes=(-2, -1))[:, :, 0, :L]
        y = (lambda x, *d: x.permute(*d))(y, 0, 2, 1) + u * Dskip[None, None, :]
        return y

    x = A_x
    g = _sigmoid((x * gate_w).sum(-1, keepdims=True))
    a = _A_ref(x, A_Lambda_re, A_Lambda_im, A_B_re, A_B_im, A_C_re, A_C_im, A_log_dt, A_Dskip)
    b = _B_ref(x, B_A_re, B_A_im, B_B_re, B_B_im, B_C_re, B_C_im, B_log_dt, B_Dskip)
    return g * a + (1 - g) * b
```

```cpp
#include <hip/hip_runtime.h>
#include <hip/hip_bf16.h>
#include <stdint.h>
typedef unsigned short u16;
__device__ __forceinline__ float bf2f(u16 v){ return __uint_as_float(((unsigned)v)<<16); }
__device__ __forceinline__ float rbf(float f){ return __bfloat162float(__float2bfloat16(f)); }
constexpr int L=2048, D=640, P=640, N=128;

__global__ void k_bu(const u16* x,const u16* Bre,const u16* Bim,const u16* Lre,const u16* Lim,const u16* ldt,float* bu){
  int p = blockIdx.x*64+threadIdx.x; int t = blockIdx.y;
  float sr=0, si=0;
  for(int d=0;d<D;d++){ float xv=bf2f(x[t*D+d]); sr+=xv*bf2f(Bre[p*D+d]); si+=xv*bf2f(Bim[p*D+d]); }
  float dt=rbf(expf(bf2f(ldt[p]))); float lr=bf2f(Lre[p]), li=bf2f(Lim[p]);
  float m=expf(dt*lr); float ar=m*cosf(dt*li), ai=m*sinf(dt*li);
  float nr=ar-1.f, ni=ai; float den=lr*lr+li*li;
  float cr=(nr*lr+ni*li)/den, ci=(ni*lr-nr*li)/den;
  bu[(t*P+p)*2]=cr*sr-ci*si; bu[(t*P+p)*2+1]=cr*si+ci*sr;
}
__global__ void k_scan(const u16* Lre,const u16* Lim,const u16* ldt,float* bu){
  int p = blockIdx.x*64+threadIdx.x;
  float dt=rbf(expf(bf2f(ldt[p]))); float lr=bf2f(Lre[p]), li=bf2f(Lim[p]);
  float m=expf(dt*lr); float ar=m*cosf(dt*li), ai=m*sinf(dt*li);
  float hr=0,hi=0;
  for(int t=0;t<L;t++){ float br=bu[(t*P+p)*2], bi=bu[(t*P+p)*2+1];
    float nr=ar*hr-ai*hi+br, ni=ar*hi+ai*hr+bi; hr=nr;hi=ni; bu[(t*P+p)*2]=hr; bu[(t*P+p)*2+1]=hi; }
}
__global__ void k_kgen(const u16* Are,const u16* Aim,const u16* Bre,const u16* Bim,const u16* Cre,const u16* Cim,const u16* ldt,float* K){
  int l = blockIdx.x*64+threadIdx.x; int d=blockIdx.y;
  float dt=rbf(expf(bf2f(ldt[d]))); float lb=rbf((float)l); float s=0;
  for(int n=0;n<N;n++){ int i=d*N+n;
    float ar=dt*bf2f(Are[i]), ai=dt*bf2f(Aim[i]); float br=dt*bf2f(Bre[i]), bi=dt*bf2f(Bim[i]); float cr=bf2f(Cre[i]), ci=bf2f(Cim[i]);
    float wr=cr*br-ci*bi, wi=cr*bi+ci*br; float m=expf(ar*lb); float c=cosf(ai*lb), sn=sinf(ai*lb);
    s += m*(wr*c-wi*sn); }
  K[d*L+l]=s;
}
__global__ void k_final(const u16* x,const float* h,const u16* Cre,const u16* Cim,const u16* DA,const u16* DB,const float* K,const u16* gw,float* out){
  int d = blockIdx.x*64+threadIdx.x; int t=blockIdx.y;
  float s=0; for(int k=0;k<D;k++) s+=rbf(bf2f(x[t*D+k])*bf2f(gw[k]));
  s=rbf(s); float g=rbf(1.f/(1.f+expf(-s))); float og=rbf(1.f-g);
  float y=0; for(int p=0;p<P;p++) y+=h[(t*P+p)*2]*bf2f(Cre[d*P+p])-h[(t*P+p)*2+1]*bf2f(Cim[d*P+p]);
  float xv=bf2f(x[t*D+d]);
  float a=2.f*y+rbf(xv*bf2f(DA[d]));
  float c=0; for(int l=0;l<=t;l++) c+=K[d*L+l]*bf2f(x[(t-l)*D+d]);
  float b=rbf(rbf(c)+rbf(xv*bf2f(DB[d])));
  out[t*D+d]=g*a+rbf(og*b);
}
extern "C" void kernel_launch(void* const* d_in, const int* in_sizes, int n_in, void* d_out, int out_size, void* d_ws, size_t ws_size, hipStream_t stream){
  const u16* x=(const u16*)d_in[0];
  float* bu=(float*)d_ws; float* K=bu+L*P*2;
  k_bu<<<dim3(P/64,L),64,0,stream>>>(x,(const u16*)d_in[3],(const u16*)d_in[4],(const u16*)d_in[1],(const u16*)d_in[2],(const u16*)d_in[7],bu);
  k_scan<<<P/64,64,0,stream>>>((const u16*)d_in[1],(const u16*)d_in[2],(const u16*)d_in[7],bu);
  k_kgen<<<dim3(L/64,D),64,0,stream>>>((const u16*)d_in[10],(const u16*)d_in[11],(const u16*)d_in[12],(const u16*)d_in[13],(const u16*)d_in[14],(const u16*)d_in[15],(const u16*)d_in[16],K);
  k_final<<<dim3(D/64,L),64,0,stream>>>(x,bu,(const u16*)d_in[5],(const u16*)d_in[6],(const u16*)d_in[8],(const u16*)d_in[17],K,(const u16*)d_in[18],(float*)d_out);
}
```

```cpp
#include <hip/hip_runtime.h>
#include <stdint.h>

typedef unsigned short u16;
typedef unsigned int u32;
typedef unsigned long long u64;
typedef __bf16 bf16x8 __attribute__((ext_vector_type(8)));
typedef short s16x4 __attribute__((ext_vector_type(4)));
typedef float f32x4 __attribute__((ext_vector_type(4)));
typedef float f32x2 __attribute__((ext_vector_type(2)));
typedef u32 u32x4 __attribute__((ext_vector_type(4)));
typedef u32 u32x2 __attribute__((ext_vector_type(2)));
typedef __bf16 bf16x2_t __attribute__((ext_vector_type(2)));
typedef __amdgpu_buffer_rsrc_t rsrc_t;
#define LAS __attribute__((address_space(3)))
#define GAS __attribute__((address_space(1)))
#define DEV __device__ __forceinline__
#define INL __attribute__((always_inline))
#if defined(__HIP_DEVICE_COMPILE__)
#define NOPK __attribute__((target("no-packed-fp32-ops")))
#else
#define NOPK
#endif

constexpr u32 WS_E = 0;
constexpr u32 WS_FH = 4096;
constexpr u32 WS_FB = WS_FH + 1024 * 128;
constexpr u32 WS_SG = WS_FB + 640 * 128;
constexpr u32 WS_GG = WS_SG + 16 * 640 * 16;
constexpr u32 WS_XT = WS_GG + 2048 * 8;
constexpr u32 WS_BT = WS_XT + 640 * 1024 * 8;
constexpr u32 WS_H = WS_BT + 640 * 2048 * 2;
constexpr u32 WS_END = WS_H + 128 * 40 * 1024;
constexpr u32 WS_DIAG = (WS_END + 4095) & ~4095u;

constexpr u32 STG = 26624;
constexpr u32 L_GW = 79872;
constexpr u32 L_CNT = 162944;
constexpr u32 C_FULL = L_CNT, C_FREE = L_CNT + 4  , C_PRO = L_CNT + 16, C_BAR = L_CNT + 20,
              C_CFULL = L_CNT + 28, C_C0 = L_CNT + 32, C_BDONE = L_CNT + 36, C_TAG = L_CNT + 40, L_TAG = L_CNT + 64;
constexpr u32 L_B0 = 81920;
constexpr u32 BSLOT = 27008;
constexpr u32 L_TAB = 2 * STG;
constexpr u32 L_ERAW = 71680;
constexpr u32 L_HT = 71680;
constexpr u32 L_WS = 76800;
constexpr u32 L_HP = 78080;
constexpr u32 L_IMG = 81920;
constexpr u32 L_BI = 0;
constexpr u32 L_BRAW = 16384;
constexpr u32 UCOPY = 4640, UPAD = 384, KOFF = 4 * UCOPY, KCOPY = 4224, KPAD = 112;
constexpr u32 KC_OFF = 16640;
constexpr u32 TROW = 288;

constexpr int SPIN_CAP = 1 << 16;

template <bool B> struct BoolC { static constexpr bool value = B; };

struct Params {
  const u16 *x, *Lre, *Lim, *Bre, *Bim, *Cre, *Cim, *ldtA, *DA;
  const u16 *Are, *Aim, *sBre, *sBim, *sCre, *sCim, *ldtB, *DB, *gw;
  float* out;
  char* ws;
};

DEV bool wave_all(bool p) { return __builtin_amdgcn_ballot_w64(!p) == 0; }
DEV float bf2f(u32 v) { return __builtin_bit_cast(float, v << 16); }
DEV float bflo(u32 v) { return __builtin_bit_cast(float, v << 16); }
DEV float bfhi(u32 v) { return __builtin_bit_cast(float, v & 0xffff0000u); }
DEV u32 pk(float a, float b) { bf16x2_t v = {(__bf16)a, (__bf16)b}; return __builtin_bit_cast(u32, v); }
DEV float rbf(float a) { return bflo(pk(a, a)); }
DEV u32 f2u(float a) { return __builtin_bit_cast(u32, a); }
DEV float u2f(u32 a) { return __builtin_bit_cast(float, a); }
DEV float swp(float x) { return u2f(__builtin_amdgcn_update_dpp(0, f2u(x), 0xB1, 0xf, 0xf, true)); }
DEV f32x2 rot(f32x2 z) { return f32x2{-z.y, z.x}; }
DEV f32x2 cmul(f32x2 c, f32x2 z, f32x2 zr) { return __builtin_elementwise_fma(f32x2{c.y, c.y}, zr, f32x2{c.x, c.x} * z); }
DEV f32x2 cmul(f32x2 c, f32x2 z) { return cmul(c, z, rot(z)); }
DEV f32x2 cpowk(float a, float th, float k) {
  const float L2E = 1.44269504088896f, IH = 0.15915494f, IL = 6.4206382e-9f;
  float m = __builtin_amdgcn_exp2f(k * a * L2E);
  float x = k * th;
  float hi = x * IH;
  float lo = __builtin_fmaf(x, IL, __builtin_fmaf(x, IH, -hi));
  float r = __builtin_amdgcn_fractf(hi) + lo;
  return f32x2{m * __builtin_amdgcn_cosf(r), m * __builtin_amdgcn_sinf(r)};
}
template <typename T> DEV T lds_ld(u32 a) { typedef T __attribute__((may_alias)) TA; return *(const LAS TA*)(size_t)a; }
template <typename T> DEV void lds_st(u32 a, T v) { typedef T __attribute__((may_alias)) TA; *(LAS TA*)(size_t)a = v; }
DEV s16x4 lds_tr(u32 a) { return __builtin_amdgcn_ds_read_tr16_b64_v4i16((LAS s16x4*)(size_t)a); }
DEV f32x4 mfma(u32x4 a, u32x4 b, f32x4 c) {
  return __builtin_amdgcn_mfma_f32_16x16x32_bf16(__builtin_bit_cast(bf16x8, a), __builtin_bit_cast(bf16x8, b), c, 0, 0, 0);
}
DEV rsrc_t mksrd(const void* p, u32 n) { return __builtin_amdgcn_make_buffer_rsrc((void*)p, (short)0, (int)n, 0x00020000); }
DEV void dma16(rsrc_t rs, u32 voff, u32 soff, u32 lds) {
  asm volatile("s_mov_b32 m0, %0\n\ts_nop 0\n\tbuffer_load_dwordx4 %1, %2, %3 offen lds"
               :: "s"(lds), "v"(voff), "s"(rs), "s"(soff) : "m0", "memory");
}
DEV void dma64v(rsrc_t rs, u32 v0, u32 v1, u32 v2, u32 v3, u32 so, u32 lds) {
  asm volatile("s_mov_b32 m0, %0\n\ts_nop 0\n\tbuffer_load_dwordx4 %1, %5, %6 offen lds\n\t"
               "buffer_load_dwordx4 %2, %5, %6 offen offset:1024 lds\n\tbuffer_load_dwordx4 %3, %5, %6 offen offset:2048 lds\n\t"
               "buffer_load_dwordx4 %4, %5, %6 offen offset:3072 lds"
               :: "s"(lds), "v"(v0), "v"(v1), "v"(v2), "v"(v3), "s"(rs), "s"(so) : "m0", "memory");
}
DEV void dma32v(rsrc_t rs, u32 v0, u32 v1, u32 so, u32 lds) {
  asm volatile("s_mov_b32 m0, %0\n\ts_nop 0\n\tbuffer_load_dwordx4 %1, %3, %4 offen lds\n\t"
               "buffer_load_dwordx4 %2, %3, %4 offen offset:1024 lds"
               :: "s"(lds), "v"(v0), "v"(v1), "s"(rs), "s"(so) : "m0", "memory");
}
DEV u32 opaque(u32 v) { asm volatile("" : "+v"(v)); return v; }
#define SB() __builtin_amdgcn_sched_barrier(0)
template <int N> DEV void waitv() { asm volatile("s_waitcnt vmcnt(%0)" :: "n"(N) : "memory"); }
DEV void waitl() { asm volatile("s_waitcnt lgkmcnt(0)" ::: "memory"); }
DEV void hwbar() { waitl(); __builtin_amdgcn_s_barrier(); asm volatile("" ::: "memory"); }
DEV void sw_arrive(u32 a) {
  u64 keep;
  asm volatile("s_waitcnt lgkmcnt(0)\n\ts_mov_b64 %0, exec\n\ts_mov_b64 exec, 1\n\tds_add_u32 %1, %2\n\ts_mov_b64 exec, %0"
               : "=&s"(keep) : "v"(a), "v"(1u) : "memory");
}
DEV void sw_wait(u32 a, u32 target) {
#pragma unroll 1
  for (int it = 0; it < (1 << 22); ++it) {
    u32 v = __hip_atomic_load((LAS u32*)(size_t)a, __ATOMIC_RELAXED, __HIP_MEMORY_SCOPE_WORKGROUP);
    if ((u32)__builtin_amdgcn_readfirstlane(v) >= target) break;
  }
  asm volatile("" ::: "memory");
}
DEV u32x4 ld16(rsrc_t rs, u32 off) { return __builtin_amdgcn_raw_buffer_load_b128(rs, off, 0, 16); }
DEV u32x2 ld8(rsrc_t rs, u32 off) { return __builtin_amdgcn_raw_buffer_load_b64(rs, off, 0, 16); }
DEV void st16(rsrc_t rs, u32 off, u32x4 v) { __builtin_amdgcn_raw_buffer_store_b128(v, rs, off, 0, 16); }
DEV void st8(rsrc_t rs, u32 off, u32x2 v) { __builtin_amdgcn_raw_buffer_store_b64(v, rs, off, 0, 16); }
constexpr u32 OOB = 0x7ffffff0u;
constexpr u32 FMIX = 0x9E3779B9u;
DEV void poll_flags(rsrc_t rs, u32 off, u32 tag) {
#pragma unroll 1
  for (int it = 0; it < SPIN_CAP; ++it) {
    asm volatile("" ::: "memory");
    u32x2 v = ld8(rs, off);
    bool ok = (off == OOB) | ((v.x == tag) & (v.y == (tag ^ FMIX)));
    if (wave_all(ok)) break;
    __builtin_amdgcn_s_sleep(1);
  }
  asm volatile("" ::: "memory");
}

#define STAMP(k) do {} while (0)
#define CSTAMP(k) do {} while (0)

#define SCAN_STEP(o, v, h)                                                                                             \
  asm("v_fma_f32 %0, %15, %10, %5\n\tv_fma_f32 %1, %16, %11, %6\n\tv_fma_f32 %2, %17, %12, %7\n\t"                   \
      "v_fma_f32 %3, %18, %13, %8\n\tv_fma_f32 %4, %19, %14, %9\n\t"                                                   \
      "v_fmac_f32_dpp %0, %10, %20 quad_perm:[1,0,3,2] row_mask:0xf bank_mask:0xf\n\t"                                 \
      "v_fmac_f32_dpp %1, %11, %21 quad_perm:[1,0,3,2] row_mask:0xf bank_mask:0xf\n\t"                                 \
      "v_fmac_f32_dpp %2, %12, %22 quad_perm:[1,0,3,2] row_mask:0xf bank_mask:0xf\n\t"                                 \
      "v_fmac_f32_dpp %3, %13, %23 quad_perm:[1,0,3,2] row_mask:0xf bank_mask:0xf\n\t"                                 \
      "v_fmac_f32_dpp %4, %14, %24 quad_perm:[1,0,3,2] row_mask:0xf bank_mask:0xf"                                     \
      : "=&v"(o[0]), "=&v"(o[1]), "=&v"(o[2]), "=&v"(o[3]), "=&v"(o[4])                                                \
      : "v"(v[0]), "v"(v[1]), "v"(v[2]), "v"(v[3]), "v"(v[4]), "v"(h[0]), "v"(h[1]), "v"(h[2]), "v"(h[3]), "v"(h[4]), \
        "v"(ma[0]), "v"(ma[1]), "v"(ma[2]), "v"(ma[3]), "v"(ma[4]), "v"(mb[0]), "v"(mb[1]), "v"(mb[2]), "v"(mb[3]),   \
        "v"(mb[4]))
DEV void scan_step(float (&o)[5], const float (&v)[5], const float (&h)[5], const float (&ma)[5], const float (&mb)[5]) {
  float t[5];
  SCAN_STEP(t, v, h);
#pragma unroll
  for (int i = 0; i < 5; ++i) o[i] = t[i];
}

NOPK __global__ void __launch_bounds__(512, 2) fused_kernel(Params P) {
  __shared__ __attribute__((aligned(16))) char smem[163840];
  const u32 L0 = (u32)(uintptr_t)(LAS char*)smem;
  const int tid = threadIdx.x, lane = tid & 63;
  const int w = __builtin_amdgcn_readfirstlane(tid >> 6);
  const int b = blockIdx.x, xcd = b & 7, r = b >> 3;
  const int c = 4 * (xcd >> 1) + (r >> 3), j = 8 * (xcd & 1) + (r & 7);
  const int ti = 4 * xcd + (r >> 3), dj = r & 7;
  const int i16 = lane & 15, g = lane >> 4;
  const rsrc_t rws = mksrd(P.ws, WS_END);
  const rsrc_t rx = mksrd(P.x, 2048 * 1280);
  const rsrc_t rCre = mksrd(P.Cre, 640 * 1280), rCim = mksrd(P.Cim, 640 * 1280);
  const int kq = w & 3, th = w >> 2;
  u32 tag;
  STAMP(0);
#define LOAD_SEED() __hip_atomic_load((GAS u32*)P.ws, __ATOMIC_RELAXED, __HIP_MEMORY_SCOPE_AGENT)
  const u32 cf0 = i16 * 128 + ((g ^ ((i16 >> 1) & 7)) << 4);
  u32x4 c0[2][5];
  auto read_c0 = [&]() INL {
    sw_wait(L0 + C_CFULL, 1);
    const u32 a0 = opaque(L0 + kq * 20480 + cf0), a1 = opaque(L0 + kq * 20480 + (cf0 ^ 64));
#pragma unroll
    for (int nt = 0; nt < 5; ++nt) { c0[0][nt] = lds_ld<u32x4>(a0 + nt * 2048); c0[1][nt] = lds_ld<u32x4>(a1 + nt * 2048); }
    sw_arrive(L0 + C_C0);
  };

  auto get_tag = [&]() INL { sw_wait(L0 + C_TAG, 1); tag = __builtin_amdgcn_readfirstlane(lds_ld<u32>(L0 + L_TAG)); };
  if (tid < 16) lds_st<u32>(opaque(L0 + L_CNT + tid * 4), 0u);

  if (w < 4) {
    __builtin_amdgcn_s_setprio(2);
    const u32 vE = LOAD_SEED();
    u32x4 xs[3];
#pragma unroll
    for (int k = 0; k < 3; ++k) {
      const int qq = min(64 * k + lane, 159), t = qq / 10, cc = qq - 10 * t;
      xs[k] = *(const GAS u32x4*)((const GAS char*)P.x + (64 * ti + 16 * w + t) * 1280 + dj * 160 + cc * 16);
    }
    const int pl = lane < 40 ? lane : 39;
    const u32 pLre = ((const GAS u16*)P.Lre)[40 * j + pl], pLim = ((const GAS u16*)P.Lim)[40 * j + pl], pLdt = ((const GAS u16*)P.ldtA)[40 * j + pl];
    hwbar();
    tag = (u32)__builtin_amdgcn_readfirstlane(vE) + 1u;
    if (w == 0) { lds_st<u32>(opaque(L0 + L_TAG), tag); sw_arrive(L0 + C_TAG); }
    STAMP(14);
    {
      const u32 sb_ = opaque(L0 + L_TAB + w * 6400);
#pragma unroll
      for (int k = 0; k < 3; ++k) lds_st<u32x4>(sb_ + min(64 * k + lane, 159) * 16, xs[k]);
      s16x4 tv[5];
#pragma unroll
      for (int dt = 0; dt < 5; ++dt) tv[dt] = lds_tr(sb_ + (4 * g + ((lane >> 2) & 3)) * 160 + (lane & 3) * 8 + dt * 32);
#pragma unroll
      for (int dt = 0; dt < 5; ++dt) lds_st<s16x4>(sb_ + i16 * 32 + g * 8 + dt * 512, tv[dt]);
#pragma unroll
      for (int dt = 0; dt < 5; ++dt) {
        const u32x2 v = lds_ld<u32x2>(sb_ + lane * 8 + dt * 512);
        st16(rws, WS_XT + (u32)(80 * dj + 16 * dt + (lane >> 2)) * 8192 + ti * 256 + w * 64 + (lane & 3) * 16, u32x4{v.x, tag, v.y, tag});
      }
    }
    STAMP(15);
    const float lre = bf2f(pLre), lim = bf2f(pLim);
    const float dtA = rbf(expf(bf2f(pLdt)));
    const float aA = dtA * lre, tA = dtA * lim;
    const f32x2 lam = cpowk(aA, tA, 1.f), l8 = cpowk(aA, tA, 8.f);
    f32x2 c2;
    {
      const float nr = lam.x - 1.f, ni = lam.y, den = 2.f * __builtin_amdgcn_rcpf(lre * lre + lim * lim);
      c2 = f32x2{(nr * lre + ni * lim) * den, (ni * lre - nr * lim) * den};
    }
    const int sig = 4 * w + g;
    float lr[5], ls[5], sa[5], sb[5], ha[5], hb[5], ca[5], cb[5];
    {
      const u32 tb = opaque(L0 + L_TAB + w * 6400 + pl * 16);
      f32x2 zs = cpowk(aA, tA, (float)(96 - 32 * w)), kh = cmul(cpowk(aA, tA, (float)(32 * w)), c2);
#pragma unroll
      for (int k = 0; k < 4; ++k) {
        lds_st<f32x4>(tb + (3 - k) * 640, f32x4{zs.x, -zs.y, zs.x, zs.y});
        lds_st<f32x4>(tb + (4 + k) * 640, f32x4{kh.x, -kh.y, -kh.x, -kh.y});
        if (k < 3) { zs = cmul(zs, l8); kh = cmul(kh, l8); }
      }
      lds_st<f32x4>(tb + 8 * 640, f32x4{lam.x, -lam.y, lam.x, lam.y});
      lds_st<f32x4>(tb + 9 * 640, f32x4{c2.x, -c2.y, -c2.x, -c2.y});
      const u32 tp = opaque(L0 + L_TAB + w * 6400 + i16 * 8), tg = opaque(tp + g * 640);
#pragma unroll
      for (int nt = 0; nt < 5; ++nt) {
        const f32x2 q0 = lds_ld<f32x2>(tg + nt * 128), q1 = lds_ld<f32x2>(tg + 4 * 640 + nt * 128);
        const f32x2 q2 = lds_ld<f32x2>(tp + 8 * 640 + nt * 128), q3 = lds_ld<f32x2>(tp + 9 * 640 + nt * 128);
        sa[nt] = q0.x; sb[nt] = q0.y; ha[nt] = q1.x; hb[nt] = q1.y; lr[nt] = q2.x; ls[nt] = q2.y; ca[nt] = q3.x; cb[nt] = q3.y;
      }
      sw_arrive(L0 + C_PRO);
    }
    const int nvalid = (c - w + 3) >> 2;
    f32x2 lam4 = cpowk(aA, tA, 512.f);
    f32x2 lame = cpowk(aA, tA, (float)(128 * ((c - 1 - w) & 3)));
    asm volatile("" : "+v"(lam4), "+v"(lame));
    STAMP(1);
    f32x4 acc[2][5], accg[2];
#pragma unroll
    for (int m = 0; m < 2; ++m) {
      accg[m] = f32x4{0.f, 0.f, 0.f, 0.f};
#pragma unroll
      for (int nt = 0; nt < 5; ++nt) acc[m][nt] = f32x4{0.f, 0.f, 0.f, 0.f};
    }
    const u32 ax0 = L0 + (32 * w + i16) * 128 + ((g ^ ((i16 >> 1) & 7)) << 4);
    const u32 ab0 = L0 + 16384 + (40 * (i16 & 1) + (i16 >> 1)) * 128 + ((g ^ (2 * (i16 >> 2) + (i16 & 1))) << 4);
#define G1_READ(S, so)                                                                                    \
    _Pragma("unroll") for (int k2 = 0; k2 < 2; ++k2) {                                                    \
      const u32 pa = (ax0 ^ (k2 * 64)) + (so), pb = (ab0 ^ (k2 * 64)) + (so);                             \
      const int H = k2 * (1 + S);                                                                         \
      fa[H][0] = lds_ld<u32x4>(pa); fb[H][0] = lds_ld<u32x4>(pb); fa[H][1] = lds_ld<u32x4>(pa + 2048); \
      _Pragma("unroll") for (int nt = 1; nt < 5; ++nt) fb[H][nt] = lds_ld<u32x4>(pb + nt * 1024);        \
      if constexpr (GATE) fg[H] = lds_ld<u32x4>(agw + k2 * 64);                                           \
    }                                                                                                     \
    agw += 128;
#define G1_MM(S, k2)                                                                                      \
    _Pragma("unroll") for (int nt = 0; nt < 5; ++nt) {                                                    \
      acc[0][nt] = mfma(fa[k2 * (1 + S)][0], fb[k2 * (1 + S)][nt], acc[0][nt]);                           \
      acc[1][nt] = mfma(fa[k2 * (1 + S)][1], fb[k2 * (1 + S)][nt], acc[1][nt]);                           \
    }                                                                                                     \
    if constexpr (GATE) {                                                                                 \
      accg[0] = mfma(fa[k2 * (1 + S)][0], fg[k2 * (1 + S)], accg[0]);                                     \
      accg[1] = mfma(fa[k2 * (1 + S)][1], fg[k2 * (1 + S)], accg[1]);                                     \
    }
    auto gemm1 = [&](auto gate_c) INL {
    constexpr bool GATE = decltype(gate_c)::value;
    u32 agw = opaque(L0 + L_GW + g * 16);
    u32x4 fa[3][2], fb[3][5], fg[3];
    sw_wait(L0 + C_FULL, 1);
    G1_READ(0, 0u);
    u32 sl = 0;
#define G1_HALF()                                                                                        \
      SB(); G1_MM(0, 0); SB();                                                                            \
      sw_arrive(L0 + C_FREE + 4 * sl);                                                                    \
      sl = sl == 2 ? 0 : sl + 1;                                                                          \
      sw_wait(L0 + C_FULL, s + 2);                                                                        \
      G1_READ(1, sl * STG);                                                                               \
      SB(); G1_MM(0, 1); SB(); G1_MM(1, 0); SB();                                                         \
      sw_arrive(L0 + C_FREE + 4 * sl);                                                                    \
      sl = sl == 2 ? 0 : sl + 1;
    int s = 0;
#pragma unroll 1
    for (; s < 8; s += 2) {
      G1_HALF();
      sw_wait(L0 + C_FULL, s + 3);
      G1_READ(0, sl * STG);
      SB(); G1_MM(1, 1); SB();
    }
    G1_HALF();
    sw_wait(L0 + C_FREE + 8, 12);
    SB(); G1_MM(1, 1); SB();
    };
    if (j == c) gemm1(BoolC<true>{}); else gemm1(BoolC<false>{});
    asm volatile("s_nop 15" : "+v"(acc[0][0]), "+v"(acc[0][1]), "+v"(acc[0][2]), "+v"(acc[0][3]), "+v"(acc[0][4]), "+v"(acc[1][0]),
                 "+v"(acc[1][1]), "+v"(acc[1][2]), "+v"(acc[1][3]), "+v"(acc[1][4]));
    STAMP(2);
    float v[8][5];
#pragma unroll
    for (int jj = 0; jj < 8; ++jj)
#pragma unroll
      for (int nt = 0; nt < 5; ++nt) v[jj][nt] = acc[jj >> 2][nt][jj & 3];
    float e[5];
#pragma unroll
    for (int nt = 0; nt < 5; ++nt) e[nt] = v[0][nt];
#pragma unroll
    for (int jj = 1; jj < 8; ++jj) scan_step(e, v[jj], e, lr, ls);
    {
      float z[5] = {0.f, 0.f, 0.f, 0.f, 0.f}, we[5];
      scan_step(we, z, e, sa, sb);
      auto s01 = __builtin_amdgcn_permlane32_swap(f2u(we[0]), f2u(we[1]), false, false);
      auto s23 = __builtin_amdgcn_permlane32_swap(f2u(we[2]), f2u(we[3]), false, false);
      auto s44 = __builtin_amdgcn_permlane32_swap(f2u(we[4]), f2u(we[4]), false, false);
      const float y01 = u2f(s01[0]) + u2f(s01[1]), y23 = u2f(s23[0]) + u2f(s23[1]), y4 = u2f(s44[0]) + u2f(s44[1]);
      auto q03 = __builtin_amdgcn_permlane16_swap(f2u(y01), f2u(y23), false, false);
      auto q44 = __builtin_amdgcn_permlane16_swap(f2u(y4), f2u(y4), false, false);
      const u32 wa = opaque(L0 + L_WS + w * 320 + i16 * 4);
      lds_st<float>(wa + (2 * (g & 1) + (g >> 1)) * 64, u2f(q03[0]) + u2f(q03[1]));
      lds_st<float>(wa + 256, u2f(q44[0]) + u2f(q44[1]));
      const u32 ra = opaque(L0 + L_ERAW + sig * 320 + i16 * 4);
#pragma unroll
      for (int nt = 0; nt < 5; ++nt) lds_st<float>(ra + nt * 64, e[nt]);
    }
    sw_arrive(L0 + C_BAR);
    sw_wait(L0 + C_BAR, 4);
    if (w == 2) {
      const u32 ap = opaque(L0 + L_WS + pl * 8);
      const f32x2 se = (lds_ld<f32x2>(ap) + lds_ld<f32x2>(ap + 320)) + (lds_ld<f32x2>(ap + 640) + lds_ld<f32x2>(ap + 960));
      st16(rws, lane < 40 ? WS_SG + (u32)(c * 640 + 40 * j + lane) * 16 : OOB, u32x4{f2u(se.x), tag, f2u(se.y), tag});
    }
    STAMP(3);
    if (j == c) {
      float gl = accg[0][0];
#pragma unroll
      for (int k = 1; k < 8; ++k) gl = (i16 == k) ? accg[k >> 2][k & 3] : gl;
      st8(rws, i16 < 8 ? WS_GG + (u32)(128 * c + 32 * w + 8 * g + i16) * 8 : OOB, u32x2{f2u(gl), tag});
    }
    float pf[5];
    {
      u32 era = opaque(L0 + L_ERAW + pl * 8);
      const f32x2 l8r = rot(l8);
      f32x2 pp = {0.f, 0.f};
#pragma unroll 1
      for (int s2 = 0; s2 < w; ++s2, era += 1280) {
        const f32x2 e0 = lds_ld<f32x2>(era), e1 = lds_ld<f32x2>(era + 320), e2 = lds_ld<f32x2>(era + 640), e3 = lds_ld<f32x2>(era + 960);
        pp = cmul(pp, l8, l8r) + e0; pp = cmul(pp, l8, l8r) + e1; pp = cmul(pp, l8, l8r) + e2; pp = cmul(pp, l8, l8r) + e3;
      }
      const f32x2 e0 = lds_ld<f32x2>(era), e1 = lds_ld<f32x2>(era + 320), e2 = lds_ld<f32x2>(era + 640);
      const u32 ptb = L0 + (w == 0 ? 79360 : w == 1 ? 80640 : w == 2 ? L_WS : L_ERAW + 3840);
      const u32 pw = opaque(ptb + pl * 8);
#pragma unroll
      for (int k = 0; k < 4; ++k) {
        const f32x2 q = cmul(pp, c2);
        lds_st<f32x2>(pw + k * 320, f32x2{q.x, -q.y});
        if (k < 3) pp = cmul(pp, l8, l8r) + (k == 0 ? e0 : k == 1 ? e1 : e2);
      }
      const u32 pr_ = opaque(ptb + g * 320 + i16 * 4);
#pragma unroll
      for (int nt = 0; nt < 5; ++nt) pf[nt] = lds_ld<float>(pr_ + nt * 64);
      const float z[5] = {0.f, 0.f, 0.f, 0.f, 0.f};
#pragma unroll
      for (int jj = 0; jj < 8; ++jj) scan_step(v[jj], z, v[jj], ca, cb);
#pragma unroll
      for (int nt = 0; nt < 5; ++nt) ls[nt] = -ls[nt];
    }
    {
      u32 off[4];
#pragma unroll
      for (int k = 0; k < 4; ++k) off[k] = (k < nvalid && lane < 40) ? WS_SG + (u32)((w + 4 * k) * 640 + 40 * j + lane) * 16 : OOB;
      u32x4 sv[4];
#pragma unroll 1
      for (int it = 0; it < SPIN_CAP; ++it) {
        asm volatile("" ::: "memory");
        bool ok = true;
#pragma unroll
        for (int k = 0; k < 4; ++k) sv[k] = ld16(rws, off[k]);
#pragma unroll
        for (int k = 0; k < 4; ++k) ok &= (off[k] == OOB) | ((sv[k].y == tag) & (sv[k].w == tag));
        if (wave_all(ok)) break;
        __builtin_amdgcn_s_sleep(1);
      }
      asm volatile("" ::: "memory");
      STAMP(4);
      f32x2 hsum = {0.f, 0.f};
      const f32x2 l4r = rot(lam4);
#pragma unroll
      for (int k = 0; k < 4; ++k)
        if (k < nvalid) hsum = cmul(hsum, lam4, l4r) + f32x2{u2f(sv[k].x), u2f(sv[k].z)};
      hsum = cmul(hsum, lame);
      const u32 hp = opaque(L0 + L_HP + lane * 32 + w * 4);
      if (lane < 40) {
        lds_st<float>(hp, hsum.x);
        lds_st<float>(hp + 16, hsum.y);
      }
    }
    sw_arrive(L0 + C_BAR);
    sw_wait(L0 + C_BAR, 8);
    {
      float hin[5], h0[5];
      const u32 hp = opaque(L0 + L_HP + i16 * 16);
#pragma unroll
      for (int nt = 0; nt < 5; ++nt) {
        const f32x4 q = lds_ld<f32x4>(hp + nt * 256);
        hin[nt] = (q.x + q.y) + (q.z + q.w);
      }
      scan_step(h0, pf, hin, ha, hb);
      scan_step(v[0], v[0], h0, lr, ls);
#pragma unroll
      for (int jj = 1; jj < 8; ++jj) scan_step(v[jj], v[jj], v[jj - 1], lr, ls);
    }
    {
      const u32 img = L0 + L_HT + w * 1280;
      const u32 wa = opaque(img + i16 * 80 + g * 16);
      const u32 ra = opaque(img + (2 * ((lane >> 2) & 3) + (g & 1)) * 80 + (g >> 1) * 32 + (lane & 3) * 8);
      const u32 hb0 = WS_H + (u32)(8 * c + 2 * w + (g >> 1)) * 40960 + (g & 1) * 2048 + i16 * 16;
      u32x2 lo[5], hi[5];
#pragma unroll
      for (int nt = 0; nt < 5; ++nt) {
        lds_st<u32x4>(wa, u32x4{pk(v[0][nt], v[1][nt]), pk(v[2][nt], v[3][nt]), pk(v[4][nt], v[5][nt]), pk(v[6][nt], v[7][nt])});
        lo[nt] = __builtin_bit_cast(u32x2, lds_tr(ra));
        hi[nt] = __builtin_bit_cast(u32x2, lds_tr(ra + 640));
      }
      SB();
#pragma unroll
      for (int nt = 0; nt < 5; ++nt) {
        const int o = 5 * j + nt;
        st16(rws, hb0 + (u32)((o >> 3) * 4096 + ((o >> 2) & 1) * 1024 + (o & 3) * 256), u32x4{lo[nt].x, lo[nt].y, hi[nt].x, hi[nt].y});
      }
    }
    STAMP(5);
    read_c0();
    waitv<0>();
    st8(rws, lane == 0 ? WS_FH + (u32)((c * 4 + w) * 16 + j) * 128 : OOB, u32x2{tag, tag ^ FMIX});
    STAMP(6);
  } else {
    const int pr = xcd * 16 + (r >> 1);
    const int nch = (r & 1) ? 2 : 3, ch = 5 * pr + ((r & 1) ? 3 : 0) + kq;
    const u32 slot = opaque(L0 + L_B0 + kq * BSLOT) ;
    if (kq == 3) {
      __builtin_amdgcn_s_setprio(3);
      hwbar();
      const rsrc_t rBre = mksrd(P.Bre, 640 * 1280), rBim = mksrd(P.Bim, 640 * 1280), rgw = mksrd(P.gw, 1280);
      const int q8 = lane >> 3;
      u32 vx[4], vb[4];
#pragma unroll
      for (int k = 0; k < 4; ++k) {
        const int m = k >> 1, i = 8 * (k & 1) + q8;
        const int t = 128 * c + 8 * (i >> 2) + 4 * m + (i & 3);
        vx[k] = (u32)t * 1280 + (((lane & 7) ^ ((i >> 1) & 7)) << 4) - 1024 * k;
        vb[k] = (u32)(40 * j + q8) * 1280 + (((lane & 7) ^ (2 * (q8 >> 1))) << 4) + 9216 * k;
      }
      const u32 vi0 = vb[0] ^ 16, vi1 = vb[1] ^ 16, vi2 = vb[2] ^ 16, vi3 = vb[3] ^ 16;
      auto head = [&](int s, u32 sl) INL {
#pragma unroll
        for (int k = 0; k < 4; ++k) dma64v(rx, vx[0], vx[1], vx[2], vx[3], (u32)s * 128 + k * 40960, sl + k * 4096);
        dma64v(rBre, vb[0], vb[1], vb[2], vb[3], (u32)s * 128, sl + 16384);
      };
      auto tail = [&](int s, u32 sl) INL {
        dma16(rBre, vb[0], (u32)s * 128 + 40960, sl + 16384 + 4096);
        dma64v(rBim, vi0, vi1, vi2, vi3, (u32)s * 128, sl + 16384 + 5120);
        dma16(rBim, vi0, (u32)s * 128 + 40960, sl + 16384 + 5120 + 4096);
      };
      head(0, L0); tail(0, L0);
      dma16(rgw, lane * 16, 0, L0 + L_GW);
      if (lane < 16) dma16(rgw, lane * 16, 1024, L0 + L_GW + 1024);
      head(1, L0 + STG); tail(1, L0 + STG);
      waitv<26>();
      sw_arrive(L0 + C_FULL);
      STAMP(1);
      sw_wait(L0 + C_PRO, 4);
      u32 sl = 2, need = 0;
#pragma unroll 1
      for (int s = 2; s < 10; ++s) {
        if (s > 2) sw_wait(L0 + C_FREE + 4 * sl, need);
        head(s, L0 + sl * STG);
        waitv<20>();
        sw_arrive(L0 + C_FULL);
        tail(s, L0 + sl * STG);
        if (sl == 2) { sl = 0; need += 4; } else ++sl;
      }
      waitv<0>();
      sw_arrive(L0 + C_FULL);
      STAMP(2);
      u32 vc[4];
#pragma unroll
      for (int k = 0; k < 4; ++k) vc[k] = (u32)(80 * dj + q8) * 1280 + (((lane & 7) ^ (lane >> 4) ^ (4 * (k & 1))) << 4) + 9216 * k;
      auto stageC = [&](int q, int uu, u32 dst) INL {
        const int u = 5 * q + uu;
        const rsrc_t rs = (u & 1) ? rCim : rCre;
        const u32 so = (u32)(u >> 1) * 128;
        dma64v(rs, vc[0], vc[1], vc[2], vc[3], so, dst);
        dma64v(rs, vc[0], vc[1], vc[2], vc[3], so + 40960, dst + 4096);
        dma32v(rs, vc[0], vc[1], so + 81920, dst + 8192);
      };
      sw_wait(L0 + C_BAR, 4);
#pragma unroll 1
      for (int q = 0; q < 4; ++q) stageC(q, 0, L0 + q * 20480);
#pragma unroll 1
      for (int q = 0; q < 3; ++q) stageC(q, 1, L0 + q * 20480 + 10240);
      waitv<30>();
      sw_arrive(L0 + C_CFULL);
      STAMP(3);
      sw_wait(L0 + C_BDONE, 3);
#pragma unroll 1
      for (int q = 0; q < 7; ++q) stageC(q >> 1, 3 + (q & 1), L0 + L_B0 + q * 10240);
      STAMP(4);
      get_tag();
      read_c0();
      sw_wait(L0 + C_C0, 8);
      stageC(3, 1, L0 + 3 * 20480 + 10240);
      stageC(3, 4, L0 + L_B0 + 7 * 10240);
#pragma unroll 1
      for (int q = 0; q < 4; ++q) stageC(q, 2, L0 + q * 20480);
      STAMP(5);
    } else if (kq < nch) {
      const GAS u32* pa = (const GAS u32*)P.Are + ch * 64 + lane;
      const u32 wAre = *pa, wAim = ((const GAS u32*)P.Aim)[ch * 64 + lane];
      const u32 wBre = ((const GAS u32*)P.sBre)[ch * 64 + lane], wBim = ((const GAS u32*)P.sBim)[ch * 64 + lane];
      const u32 wCre = ((const GAS u32*)P.sCre)[ch * 64 + lane], wCim = ((const GAS u32*)P.sCim)[ch * 64 + lane];
      const u32 wdt = ((const GAS u16*)P.ldtB)[ch];
      hwbar();
      const float dt = rbf(expf(bf2f(wdt)));
      f32x4 ka[3] = {f32x4{0.f, 0.f, 0.f, 0.f}, f32x4{0.f, 0.f, 0.f, 0.f}, f32x4{0.f, 0.f, 0.f, 0.f}};
      const u32 tw = slot + lane * 4, tr = slot + i16 * TROW + g * 16;
#pragma unroll 1
      for (int rd = 0; rd < 2; ++rd) {
        const u32 sh = rd ? 0u : 16u;
        const float a = dt * bfhi(wAre << sh), t = dt * bfhi(wAim << sh);
        const f32x2 db = {dt * bfhi(wBre << sh), dt * bfhi(wBim << sh)}, cc = {bfhi(wCre << sh), bfhi(wCim << sh)};
        const f32x2 W = cmul(cc, db);
        const f32x2 z1 = cpowk(a, t, 1.f), z8 = cpowk(a, t, 8.f), z128 = cpowk(a, t, 128.f);
        {
          const f32x2 zr = rot(z8);
          f32x2 cv = z8;
          lds_st<u32>(tw + 41 * TROW, 0x00003f80u);
          lds_st<u32>(tw + 42 * TROW, pk(cv.x, cv.y));
#pragma unroll
          for (int jj = 2; jj < 16; ++jj) { cv = cmul(cv, z8, zr); lds_st<u32>(tw + (41 + jj) * TROW, pk(cv.x, cv.y)); }
        }
        {
          const f32x2 y1 = {z1.x, -z1.y}, y1r = rot(y1), yh = {z128.x, -z128.y}, yhr = rot(yh);
          f32x2 R[8];
          R[0] = f32x2{W.x, -W.y};
#pragma unroll
          for (int k = 1; k < 8; ++k) R[k] = cmul(R[k - 1], y1, y1r);
#pragma unroll
          for (int k = 0; k < 8; ++k) lds_st<u32>(tw + k * TROW, pk(R[k].x, R[k].y));
#pragma unroll
          for (int k = 0; k < 8; ++k) { R[k] = cmul(R[k], yh, yhr); lds_st<u32>(tw + (8 + k) * TROW, pk(R[k].x, R[k].y)); }
#pragma unroll
          for (int hh = 0; hh < 2; ++hh)
#pragma unroll
            for (int k = 0; k < 4; ++k) { R[2 * k] = cmul(R[2 * k], yh, yhr); lds_st<u32>(tw + (16 + 4 * hh + k) * TROW, pk(R[2 * k].x, R[2 * k].y)); }
#pragma unroll
          for (int hh = 0; hh < 4; ++hh)
#pragma unroll
            for (int k = 0; k < 2; ++k) { R[4 * k] = cmul(R[4 * k], yh, yhr); lds_st<u32>(tw + (24 + 2 * hh + k) * TROW, pk(R[4 * k].x, R[4 * k].y)); }
#pragma unroll
          for (int hh = 0; hh < 9; ++hh) { R[0] = cmul(R[0], yh, yhr); lds_st<u32>(tw + (32 + hh) * TROW, pk(R[0].x, R[0].y)); }
        }
#pragma unroll
        for (int ks = 0; ks < 4; ++ks) {
          const u32x4 bb = lds_ld<u32x4>(tr + 41 * TROW + ks * 64);
#pragma unroll
          for (int m = 0; m < 3; ++m) ka[m] = mfma(lds_ld<u32x4>(tr + 16 * m * TROW + ks * 64), bb, ka[m]);
        }
      }
      STAMP(1);
      get_tag();
      u32x4 uv[8];
#pragma unroll
      for (int k = 0; k < 8; ++k) uv[k] = ld16(rws, WS_XT + (u32)ch * 8192 + (64 * k + lane) * 16);
      const u32 kc = slot + KC_OFF;
      lds_st<u32x2>(kc + (128 * (g >> 1) + 8 * i16 + 4 * (g & 1)) * 2, u32x2{pk(ka[0].x, ka[0].y), pk(ka[0].z, ka[0].w)});
      {
        const u32 p0 = pk(ka[1].x, ka[1].y), p1 = pk(ka[1].z, ka[1].w);
        const u32 a0 = g < 2 ? 256 + 64 * g + 4 * i16 : 384 + 64 * (g - 2) + 2 * i16;
        lds_st<u32>(kc + a0 * 2, p0);
        lds_st<u32>(kc + (a0 + (g < 2 ? 2 : 32)) * 2, p1);
        const u32 q0 = pk(ka[2].x, ka[2].y), q1 = pk(ka[2].z, ka[2].w);
        const u32 a2 = kc + (512 + 64 * g + i16) * 2;
        if (g < 3) lds_st<u16>(a2, (u16)q0);
        if (g < 2) { lds_st<u16>(a2 + 32, (u16)(q0 >> 16)); lds_st<u16>(a2 + 64, (u16)q1); lds_st<u16>(a2 + 96, (u16)(q1 >> 16)); }
        if (lane == 0) lds_st<u32>(kc - 4, 0u);
      }
      const u32 k0 = slot + KOFF + KPAD, k1 = k0 + KCOPY;
      if (lane < 14) lds_st<u32x4>(slot + KOFF + (lane >= 7 ? KCOPY - 112 : 0) + lane * 16, u32x4{0u, 0u, 0u, 0u});
#define PERM(hi, lo, sel) __builtin_amdgcn_perm(hi, lo, sel)
      if (lane < 32) {
        const u32x4 d = lds_ld<u32x4>(kc + lane * 16);
        const u32 dm = lds_ld<u32>(kc + lane * 16 - 4);
        lds_st<u32x4>(k1 + lane * 16, u32x4{PERM(d.w, d.w, 0x01000302u), PERM(d.z, d.z, 0x01000302u), PERM(d.y, d.y, 0x01000302u), PERM(d.x, d.x, 0x01000302u)});
        lds_st<u32x4>(k0 + lane * 16, u32x4{PERM(d.z, d.w, 0x07060100u), PERM(d.y, d.z, 0x07060100u), PERM(d.x, d.y, 0x07060100u), PERM(dm, d.x, 0x07060100u)});
      } else {
        const u32 ea_ = kc + (256 + 4 * (lane - 32)) * 2;
        const u32x2 E = lds_ld<u32x2>(ea_);
        const u32 E2 = lds_ld<u32>(ea_ + 8), Em = lds_ld<u32>(ea_ - 4);
        const u32 e22 = PERM(E.y, E.y, 0x01000100u);
        lds_st<u32x4>(k1 + lane * 16, u32x4{PERM(E.y, E2, 0x07060100u), e22, PERM(E.x, E.y, 0x07060100u), PERM(E.x, E.x, 0x01000100u)});
        lds_st<u32x4>(k0 + lane * 16, u32x4{PERM(E.y, E.y, 0x01000302u), e22, PERM(E.x, E.x, 0x01000302u), PERM(Em, E.x, lane == 32 ? 0x07060100u : 0x01000100u)});
      }
      {
        const u32 ea_ = kc + (384 + 2 * lane) * 2;
        const u32 E0 = lds_ld<u32>(ea_), E1 = lds_ld<u32>(ea_ + 4);
        const u32 e00 = PERM(E0, E0, 0x01000100u), e11 = PERM(E0, E0, 0x03020302u);
        lds_st<u32x4>(k1 + (64 + lane) * 16, u32x4{PERM(E1, E1, 0x01000100u), e11, PERM(E0, E0, 0x01000302u), e00});
        lds_st<u32x4>(k0 + (64 + lane) * 16, u32x4{PERM(E0, E1, 0x07060100u), e11, e00, e00});
      }
#pragma unroll
      for (int rr = 0; rr < 2; ++rr) {
        const u32 ea_ = kc + (512 + 64 * rr + lane) * 2;
        const u32 e0 = lds_ld<u16>(ea_), e1 = lds_ld<u16>(ea_ + 2);
        const u32 t4 = (lane & 1) ? e1 : e0;
        const u32 e00 = e0 | (e0 << 16), e11 = e1 | (e1 << 16);
        lds_st<u32x4>(k1 + (128 + 64 * rr + lane) * 16, u32x4{e11, e1 | (t4 << 16), e00, e00});
        lds_st<u32x4>(k0 + (128 + 64 * rr + lane) * 16, u32x4{e11, t4 | (e0 << 16), e00, e00});
      }
      if (lane < 25) {
#pragma unroll
        for (int cc = 0; cc < 4; ++cc) lds_st<u32x4>(slot + cc * UCOPY + lane * 16, u32x4{0u, 0u, 0u, 0u});
      }
#pragma unroll 1
      for (int it = 0; it < SPIN_CAP; ++it) {
        bool ok = true;
#pragma unroll
        for (int k = 0; k < 8; ++k) ok &= (uv[k].y == tag) & (uv[k].w == tag);
        if (wave_all(ok)) break;
        __builtin_amdgcn_s_sleep(2);
        asm volatile("" ::: "memory");
#pragma unroll
        for (int k = 0; k < 8; ++k) uv[k] = ld16(rws, WS_XT + (u32)ch * 8192 + (64 * k + lane) * 16);
      }
      asm volatile("" ::: "memory");
      STAMP(2);
#pragma unroll
      for (int k = 0; k < 8; ++k) {
        const u32 ua = slot + UPAD + (64 * k + lane) * 8;
        lds_st<u32x2>(ua, u32x2{uv[k].x, uv[k].z});
        lds_st<u32>(ua + UCOPY + 4, uv[k].x);
        lds_st<u32>(ua + UCOPY + 8, uv[k].z);
        lds_st<u32x2>(ua + 2 * UCOPY + 8, u32x2{uv[k].x, uv[k].z});
        lds_st<u32>(ua + 3 * UCOPY + 12, uv[k].x);
        lds_st<u32>(ua + 3 * UCOPY + 16, uv[k].z);
      }
      f32x4 ya[8];
#pragma unroll
      for (int A = 0; A < 8; ++A) ya[A] = f32x4{0.f, 0.f, 0.f, 0.f};
      const u32 ta = opaque(slot + KOFF + KPAD + (i16 & 1) * KCOPY + ((i16 >> 1) - g) * 16 - 64);
      const u32 ua = opaque(slot + (i16 & 3) * UCOPY + (i16 >> 2) * 128 + g * 16);
      u32x4 T[2][8], U[2][8];
#define CV_READ(S, st)                                                                                        \
      _Pragma("unroll") for (int D = 0; D < 8; ++D) {                                                         \
        T[S][D] = lds_ld<u32x4>(ta + ((st) >> 1) * 128 + D * 512 + (1 - ((st) & 1)) * 64);                   \
        U[S][D] = lds_ld<u32x4>(ua + UPAD - ((st) >> 1) * 128 + D * 512 + ((st) & 1) * 64);                  \
      }
      CV_READ(0, 0);
#pragma unroll
      for (int st = 0; st < 8; ++st) {
        SB();
        if (st < 7) { CV_READ((st + 1) & 1, st + 1); }
#pragma unroll
        for (int S = 0; S < 8; ++S)
#pragma unroll
          for (int D = 0; D <= S; ++D) ya[S] = mfma(T[st & 1][D], U[st & 1][S - D], ya[S]);
        if (st < 7) {
#pragma unroll
          for (int k = 0; k < 16; ++k) {
            __builtin_amdgcn_sched_group_barrier(0x8, 2, 0);
            __builtin_amdgcn_sched_group_barrier(0x100, 1, 0);
          }
          __builtin_amdgcn_sched_group_barrier(0x8, 4, 0);
        }
      }
      SB();
      STAMP(3);
      {
        const u32 oa = slot + ((i16 >> 2) * 36 + 8 * g + 3 - (i16 & 3)) * 4;
#pragma unroll
        for (int A = 0; A < 8; ++A) {
          lds_st<u32>(oa + A * 576, pk(ya[A].x, ya[A].y));
          lds_st<u32>(oa + A * 576 + 16, pk(ya[A].z, ya[A].w));
        }
#pragma unroll
        for (int k = 0; k < 4; ++k) {
          const int m = 64 * k + lane;
          st16(rws, WS_BT + (u32)ch * 4096 + m * 16, lds_ld<u32x4>(slot + (m >> 3) * 144 + (m & 7) * 16));
        }
        sw_arrive(L0 + C_BDONE);
        read_c0();
        waitv<0>();
        st8(rws, lane == 0 ? WS_FB + (u32)ch * 128 : OOB, u32x2{tag, tag ^ FMIX});
      }
      STAMP(4);
    } else {
      hwbar();
      get_tag();
      sw_arrive(L0 + C_BDONE);
      read_c0();
    }
  }

  __builtin_amdgcn_s_setprio(0);
  STAMP(7);
  const int et = tid >> 3, es = tid & 7;
  u32x2 ex[3], eda[3], edb[3];
#pragma unroll
  for (int k = 0; k < 3; ++k) {
    const int dq = (k == 2 && es >= 4) ? es : es + 8 * k;
    ex[k] = *(const GAS u32x2*)((const GAS char*)P.x + (64 * ti + et) * 1280 + dj * 160 + dq * 8);
    eda[k] = *(const GAS u32x2*)((const GAS char*)P.DA + dj * 160 + dq * 8);
    edb[k] = *(const GAS u32x2*)((const GAS char*)P.DB + dj * 160 + dq * 8);
  }
  float gate, ogate;
  {
    const u32 go = WS_GG + (u32)(64 * ti + et) * 8;
    u32x2 gv;
#pragma unroll 1
    for (int it = 0; it < SPIN_CAP; ++it) {
      asm volatile("" ::: "memory");
      gv = ld8(rws, go);
      if (wave_all(gv.y == tag)) break;
      __builtin_amdgcn_s_sleep(1);
    }
    const float s = u2f(gv.x);
    gate = rbf(1.f / (1.f + expf(-rbf(s))));
    ogate = rbf(1.f - gate);
  }
  poll_flags(rws, lane < 16 ? WS_FH + (u32)((2 * ti + th) * 16 + lane) * 128 : OOB, tag);
  waitv<0>();
  hwbar();
  STAMP(8);
  u32x4 hf_[10][2];
  {
    const u32 ho = WS_H + (u32)(4 * ti + 2 * th) * 40960 + kq * 10240 + lane * 16;
#pragma unroll
    for (int ks = 0; ks < 10; ++ks)
#pragma unroll
      for (int mt = 0; mt < 2; ++mt) hf_[ks][mt] = ld16(rws, ho + mt * 40960 + ks * 1024);
  }
  SB();
  f32x4 y[2][5];
#pragma unroll
  for (int mt = 0; mt < 2; ++mt)
#pragma unroll
    for (int nt = 0; nt < 5; ++nt) y[mt][nt] = f32x4{0.f, 0.f, 0.f, 0.f};
#pragma unroll
  for (int hf = 0; hf < 2; ++hf)
#pragma unroll
    for (int nt = 0; nt < 5; ++nt)
#pragma unroll
      for (int mt = 0; mt < 2; ++mt) y[mt][nt] = mfma(c0[hf][nt], hf_[hf][mt], y[mt][nt]);
  STAMP(9);
  const u32 cab[2][2] = {{opaque(L0 + kq * 20480 + cf0), opaque(L0 + kq * 20480 + (cf0 ^ 64))},
                         {opaque(L0 + 81920 + kq * 20480 + cf0), opaque(L0 + 81920 + kq * 20480 + (cf0 ^ 64))}};
#pragma unroll
  for (int uu = 1; uu < 5; ++uu) {
#pragma unroll
    for (int hf = 0; hf < 2; ++hf)
#pragma unroll
      for (int nt = 0; nt < 5; ++nt) {
        const u32x4 cc = lds_ld<u32x4>(cab[uu > 2][hf] + ((uu & 1) == (uu > 2) ? 0 : 10240) + nt * 2048);
#pragma unroll
        for (int mt = 0; mt < 2; ++mt) y[mt][nt] = mfma(cc, hf_[2 * uu + hf][mt], y[mt][nt]);
      }
  }
  STAMP(10);
  poll_flags(rws, lane < 8 ? WS_FB + (u32)(80 * dj + 8 * w + lane) * 128 : (lane < 16 && w < 2) ? WS_FB + (u32)(80 * dj + 56 + 8 * w + lane) * 128 : OOB, tag);
  const u32 bo = WS_BT + (u32)(80 * dj + 8 * w + (lane >> 3)) * 4096 + ti * 128 + (lane & 7) * 16;
  const u32x4 bp0 = ld16(rws, bo), bp1 = ld16(rws, w < 2 ? bo + 64 * 4096 : OOB);
  hwbar();
  STAMP(11);
#pragma unroll
  for (int mt = 0; mt < 2; ++mt)
#pragma unroll
    for (int nt = 0; nt < 5; ++nt)
      lds_st<f32x4>(L0 + L_IMG + kq * 20480 + (32 * th + 16 * mt + i16) * 320 + (4 * nt + (g ^ ((i16 >> 1) & 3))) * 16, y[mt][nt]);
  {
    const u32 raw = L0 + L_BRAW + w * 1024;
    const u32 ra = raw + ((lane >> 2) & 3) * 128 + g * 32 + (lane & 3) * 8;
    const u32 wa = L0 + L_BI + (16 * g + i16) * 168 + w * 16;
    lds_st<u32x4>(raw + lane * 16, bp0);
    lds_st<s16x4>(wa, lds_tr(ra));
    lds_st<s16x4>(wa + 8, lds_tr(ra + 512));
    if (w < 2) {
      lds_st<u32x4>(raw + lane * 16, bp1);
      lds_st<s16x4>(wa + 128, lds_tr(ra));
      lds_st<s16x4>(wa + 136, lds_tr(ra + 512));
    }
  }
  hwbar();
  STAMP(12);
#pragma unroll
  for (int k = 0; k < 3; ++k) {
    if (k == 2 && es >= 4) break;
    const int dq = es + 8 * k;
    const u32 ia = L0 + L_IMG + et * 320 + ((dq & ~3) | ((dq ^ (et >> 1)) & 3)) * 16;
    const f32x4 ys = (lds_ld<f32x4>(ia) + lds_ld<f32x4>(ia + 20480)) + (lds_ld<f32x4>(ia + 40960) + lds_ld<f32x4>(ia + 61440));
    const u32x2 bv = lds_ld<u32x2>(L0 + L_BI + et * 168 + dq * 8);
    f32x4 o;
#pragma unroll
    for (int e = 0; e < 2; ++e) {
      const float x0 = bflo(ex[k][e]), x1 = bfhi(ex[k][e]);
      const u32 sa_ = pk(x0 * bflo(eda[k][e]), x1 * bfhi(eda[k][e]));
      const u32 sb_ = pk(x0 * bflo(edb[k][e]), x1 * bfhi(edb[k][e]));
      const u32 bb = pk(bflo(bv[e]) + bflo(sb_), bfhi(bv[e]) + bfhi(sb_));
      const u32 ob = pk(ogate * bflo(bb), ogate * bfhi(bb));
      o[2 * e] = gate * (ys[2 * e] + bflo(sa_)) + bflo(ob);
      o[2 * e + 1] = gate * (ys[2 * e + 1] + bfhi(sa_)) + bfhi(ob);
    }
    *(GAS f32x4*)((GAS char*)P.out + (64 * ti + et) * 2560 + dj * 320 + dq * 16) = o;
  }
  STAMP(13);
  if (b == 255) {
    poll_flags(rws, WS_FH + (u32)tid * 128, tag);
    poll_flags(rws, WS_FH + (u32)(512 + tid) * 128, tag);
    hwbar();
    if (tid == 0) __hip_atomic_store((GAS u32*)P.ws, tag, __ATOMIC_RELAXED, __HIP_MEMORY_SCOPE_AGENT);
  }
}

extern "C" void kernel_launch(void* const* d_in, const int* in_sizes, int n_in, void* d_out, int out_size, void* d_ws,
                              size_t ws_size, hipStream_t stream) {
  Params p;
  p.x = (const u16*)d_in[0];
  p.Lre = (const u16*)d_in[1];
  p.Lim = (const u16*)d_in[2];
  p.Bre = (const u16*)d_in[3];
  p.Bim = (const u16*)d_in[4];
  p.Cre = (const u16*)d_in[5];
  p.Cim = (const u16*)d_in[6];
  p.ldtA = (const u16*)d_in[7];
  p.DA = (const u16*)d_in[8];
  p.Are = (const u16*)d_in[10];
  p.Aim = (const u16*)d_in[11];
  p.sBre = (const u16*)d_in[12];
  p.sBim = (const u16*)d_in[13];
  p.sCre = (const u16*)d_in[14];
  p.sCim = (const u16*)d_in[15];
  p.ldtB = (const u16*)d_in[16];
  p.DB = (const u16*)d_in[17];
  p.gw = (const u16*)d_in[18];
  p.out = (float*)d_out;
  p.ws = (char*)d_ws;
  fused_kernel<<<256, 512, 0, stream>>>(p);
}
```

```cpp
#include <hip/hip_runtime.h>
#include <stdint.h>

typedef unsigned short u16;
typedef unsigned int u32;
typedef unsigned long long u64;
typedef __bf16 bf16x8 __attribute__((ext_vector_type(8)));
typedef short s16x4 __attribute__((ext_vector_type(4)));
typedef float f32x4 __attribute__((ext_vector_type(4)));
typedef float f32x2 __attribute__((ext_vector_type(2)));
typedef u32 u32x4 __attribute__((ext_vector_type(4)));
typedef u32 u32x2 __attribute__((ext_vector_type(2)));
typedef __bf16 bf16x2_t __attribute__((ext_vector_type(2)));
typedef __amdgpu_buffer_rsrc_t rsrc_t;
#define LAS __attribute__((address_space(3)))
#define GAS __attribute__((address_space(1)))
#define DEV __device__ __forceinline__
#define INL __attribute__((always_inline))
#if defined(__HIP_DEVICE_COMPILE__)
#define NOPK __attribute__((target("no-packed-fp32-ops")))
#else
#define NOPK
#endif

constexpr u32 WS_E = 0;
constexpr u32 WS_FH = 4096;
constexpr u32 WS_FB = WS_FH + 1024 * 128;
constexpr u32 WS_FX = WS_FB + 640 * 128;
constexpr u32 WS_SG = WS_FX + 1024 * 128;
constexpr u32 WS_GG = WS_SG + 16 * 640 * 16;
constexpr u32 WS_XT = WS_GG + 2048 * 8;
constexpr u32 WS_BT = WS_XT + 640 * 2048 * 2;
constexpr u32 WS_H = WS_BT + 640 * 2048 * 2;
constexpr u32 WS_END = WS_H + 128 * 40 * 1024;
constexpr u32 WS_DIAG = (WS_END + 4095) & ~4095u;

constexpr u32 STG = 26624;
constexpr u32 L_GW = 79872;
constexpr u32 L_CNT = 162944;
constexpr u32 C_FULL = L_CNT, C_FREE = L_CNT + 4  , C_PRO = L_CNT + 16, C_BAR = L_CNT + 20,
              C_CFULL = L_CNT + 28, C_C0 = L_CNT + 32, C_BDONE = L_CNT + 36, C_TAG = L_CNT + 40, L_TAG = L_CNT + 64;
constexpr u32 L_B0 = 81920;
constexpr u32 BSLOT = 27008;
constexpr u32 L_RAW = 2 * STG;
constexpr u32 L_TAB = L_RAW + 10240;
constexpr u32 L_ERAW = 71680;
constexpr u32 L_HT = 71680;
constexpr u32 L_WS = 76800;
constexpr u32 L_HP = 78080;
constexpr u32 L_IMG0 = 81920, L_IMG1 = 122880;
constexpr u32 L_XI = 20480;
constexpr u32 L_BI = 61440;
constexpr u32 UCOPY = 4640, UPAD = 384, KOFF = 4 * UCOPY, KCOPY = 4224, KPAD = 112;
constexpr u32 KC_OFF = 16640;
constexpr u32 TROW = 288;

constexpr int SPIN_CAP = 1 << 16;

template <bool B> struct BoolC { static constexpr bool value = B; };

struct Params {
  const u16 *x, *Lre, *Lim, *Bre, *Bim, *Cre, *Cim, *ldtA, *DA;
  const u16 *Are, *Aim, *sBre, *sBim, *sCre, *sCim, *ldtB, *DB, *gw;
  float* out;
  char* ws;
};

DEV bool wave_all(bool p) { return __builtin_amdgcn_ballot_w64(!p) == 0; }
DEV float bf2f(u32 v) { return __builtin_bit_cast(float, v << 16); }
DEV float bflo(u32 v) { return __builtin_bit_cast(float, v << 16); }
DEV float bfhi(u32 v) { return __builtin_bit_cast(float, v & 0xffff0000u); }
DEV u32 pk(float a, float b) { bf16x2_t v = {(__bf16)a, (__bf16)b}; return __builtin_bit_cast(u32, v); }
DEV float rbf(float a) { return bflo(pk(a, a)); }
DEV u32 f2u(float a) { return __builtin_bit_cast(u32, a); }
DEV float u2f(u32 a) { return __builtin_bit_cast(float, a); }
DEV float swp(float x) { return u2f(__builtin_amdgcn_update_dpp(0, f2u(x), 0xB1, 0xf, 0xf, true)); }
DEV f32x2 rot(f32x2 z) { return f32x2{-z.y, z.x}; }
DEV f32x2 cmul(f32x2 c, f32x2 z, f32x2 zr) { return __builtin_elementwise_fma(f32x2{c.y, c.y}, zr, f32x2{c.x, c.x} * z); }
DEV f32x2 cmul(f32x2 c, f32x2 z) { return cmul(c, z, rot(z)); }
DEV f32x2 cpowk(float a, float th, float k) {
  const float L2E = 1.44269504088896f, IH = 0.15915494f, IL = 6.4206382e-9f;
  float m = __builtin_amdgcn_exp2f(k * a * L2E);
  float x = k * th;
  float hi = x * IH;
  float lo = __builtin_fmaf(x, IL, __builtin_fmaf(x, IH, -hi));
  float r = __builtin_amdgcn_fractf(hi) + lo;
  return f32x2{m * __builtin_amdgcn_cosf(r), m * __builtin_amdgcn_sinf(r)};
}
template <typename T> DEV T lds_ld(u32 a) { typedef T __attribute__((may_alias)) TA; return *(const LAS TA*)(size_t)a; }
template <typename T> DEV void lds_st(u32 a, T v) { typedef T __attribute__((may_alias)) TA; *(LAS TA*)(size_t)a = v; }
DEV s16x4 lds_tr(u32 a) { return __builtin_amdgcn_ds_read_tr16_b64_v4i16((LAS s16x4*)(size_t)a); }
DEV f32x4 mfma(u32x4 a, u32x4 b, f32x4 c) {
  return __builtin_amdgcn_mfma_f32_16x16x32_bf16(__builtin_bit_cast(bf16x8, a), __builtin_bit_cast(bf16x8, b), c, 0, 0, 0);
}
DEV rsrc_t mksrd(const void* p, u32 n) { return __builtin_amdgcn_make_buffer_rsrc((void*)p, (short)0, (int)n, 0x00020000); }
DEV void dma16(rsrc_t rs, u32 voff, u32 soff, u32 lds) {
  asm volatile("s_mov_b32 m0, %0\n\ts_nop 0\n\tbuffer_load_dwordx4 %1, %2, %3 offen lds"
               :: "s"(lds), "v"(voff), "s"(rs), "s"(soff) : "m0", "memory");
}
DEV void dma64v(rsrc_t rs, u32 v0, u32 v1, u32 v2, u32 v3, u32 so, u32 lds) {
  asm volatile("s_mov_b32 m0, %0\n\ts_nop 0\n\tbuffer_load_dwordx4 %1, %5, %6 offen lds\n\t"
               "buffer_load_dwordx4 %2, %5, %6 offen offset:1024 lds\n\tbuffer_load_dwordx4 %3, %5, %6 offen offset:2048 lds\n\t"
               "buffer_load_dwordx4 %4, %5, %6 offen offset:3072 lds"
               :: "s"(lds), "v"(v0), "v"(v1), "v"(v2), "v"(v3), "s"(rs), "s"(so) : "m0", "memory");
}
DEV void dma32v(rsrc_t rs, u32 v0, u32 v1, u32 so, u32 lds) {
  asm volatile("s_mov_b32 m0, %0\n\ts_nop 0\n\tbuffer_load_dwordx4 %1, %3, %4 offen lds\n\t"
               "buffer_load_dwordx4 %2, %3, %4 offen offset:1024 lds"
               :: "s"(lds), "v"(v0), "v"(v1), "s"(rs), "s"(so) : "m0", "memory");
}
DEV u32 opaque(u32 v) { asm volatile("" : "+v"(v)); return v; }
#define SB() __builtin_amdgcn_sched_barrier(0)
template <int N> DEV void waitv() { asm volatile("s_waitcnt vmcnt(%0)" :: "n"(N) : "memory"); }
DEV void waitl() { asm volatile("s_waitcnt lgkmcnt(0)" ::: "memory"); }
DEV void hwbar() { waitl(); __builtin_amdgcn_s_barrier(); asm volatile("" ::: "memory"); }
DEV void sw_arrive(u32 a) {
  u64 keep;
  asm volatile("s_waitcnt lgkmcnt(0)\n\ts_mov_b64 %0, exec\n\ts_mov_b64 exec, 1\n\tds_add_u32 %1, %2\n\ts_mov_b64 exec, %0"
               : "=&s"(keep) : "v"(a), "v"(1u) : "memory");
}
DEV void sw_wait(u32 a, u32 target) {
  u32 v, sv, cnt = 1u << 22;
  asm volatile("1:\n\tds_read_b32 %0, %4\n\ts_sub_u32 %2, %2, 1\n\ts_cselect_b32 %3, 0, %3\n\ts_waitcnt lgkmcnt(0)\n\t"
               "v_readfirstlane_b32 %1, %0\n\ts_cmp_lt_u32 %1, %3\n\ts_cbranch_scc1 1b"
               : "=&v"(v), "=&s"(sv), "+s"(cnt), "+s"(target) : "v"(a) : "scc", "memory");
}
DEV u32 peek(u32 a) { return __hip_atomic_load((LAS u32*)(size_t)a, __ATOMIC_RELAXED, __HIP_MEMORY_SCOPE_WORKGROUP); }
DEV u32x4 ld16(rsrc_t rs, u32 off) { return __builtin_amdgcn_raw_buffer_load_b128(rs, off, 0, 16); }
DEV u32x2 ld8(rsrc_t rs, u32 off) { return __builtin_amdgcn_raw_buffer_load_b64(rs, off, 0, 16); }
DEV void st16(rsrc_t rs, u32 off, u32x4 v) { __builtin_amdgcn_raw_buffer_store_b128(v, rs, off, 0, 16); }
DEV void st8(rsrc_t rs, u32 off, u32x2 v) { __builtin_amdgcn_raw_buffer_store_b64(v, rs, off, 0, 16); }
constexpr u32 OOB = 0x7ffffff0u;
constexpr u32 FMIX = 0x9E3779B9u;
DEV bool flag_ok(u32x2 v, u32 off, u32 tag) { return wave_all((off == OOB) | ((v.x == tag) & (v.y == (tag ^ FMIX)))); }
DEV void poll_flags(rsrc_t rs, u32 off, u32 tag) {
#pragma unroll 1
  for (int it = 0; it < SPIN_CAP; ++it) {
    asm volatile("" ::: "memory");
    if (flag_ok(ld8(rs, off), off, tag)) break;
    __builtin_amdgcn_s_sleep(1);
  }
  asm volatile("" ::: "memory");
}

#define STAMP(k) do {} while (0)
#define CSTAMP(k) do {} while (0)

#define SCAN_STEP(o, v, h)                                                                                             \
  asm("v_fma_f32 %0, %15, %10, %5\n\tv_fma_f32 %1, %16, %11, %6\n\tv_fma_f32 %2, %17, %12, %7\n\t"                   \
      "v_fma_f32 %3, %18, %13, %8\n\tv_fma_f32 %4, %19, %14, %9\n\t"                                                   \
      "v_fmac_f32_dpp %0, %10, %20 quad_perm:[1,0,3,2] row_mask:0xf bank_mask:0xf\n\t"                                 \
      "v_fmac_f32_dpp %1, %11, %21 quad_perm:[1,0,3,2] row_mask:0xf bank_mask:0xf\n\t"                                 \
      "v_fmac_f32_dpp %2, %12, %22 quad_perm:[1,0,3,2] row_mask:0xf bank_mask:0xf\n\t"                                 \
      "v_fmac_f32_dpp %3, %13, %23 quad_perm:[1,0,3,2] row_mask:0xf bank_mask:0xf\n\t"                                 \
      "v_fmac_f32_dpp %4, %14, %24 quad_perm:[1,0,3,2] row_mask:0xf bank_mask:0xf"                                     \
      : "=&v"(o[0]), "=&v"(o[1]), "=&v"(o[2]), "=&v"(o[3]), "=&v"(o[4])                                                \
      : "v"(v[0]), "v"(v[1]), "v"(v[2]), "v"(v[3]), "v"(v[4]), "v"(h[0]), "v"(h[1]), "v"(h[2]), "v"(h[3]), "v"(h[4]), \
        "v"(ma[0]), "v"(ma[1]), "v"(ma[2]), "v"(ma[3]), "v"(ma[4]), "v"(mb[0]), "v"(mb[1]), "v"(mb[2]), "v"(mb[3]),   \
        "v"(mb[4]))
DEV void scan_step(float (&o)[5], const float (&v)[5], const float (&h)[5], const float (&ma)[5], const float (&mb)[5]) {
  float t[5];
  SCAN_STEP(t, v, h);
#pragma unroll
  for (int i = 0; i < 5; ++i) o[i] = t[i];
}

NOPK __global__ void __launch_bounds__(512, 2) fused_kernel(Params P) {
  __shared__ __attribute__((aligned(16))) char smem[163840];
  const u32 L0 = (u32)(uintptr_t)(LAS char*)smem;
  const int tid = threadIdx.x, lane = tid & 63;
  const int w = __builtin_amdgcn_readfirstlane(tid >> 6);
  const int b = blockIdx.x, xcd = b & 7, r = b >> 3;
  const int c = 4 * (xcd >> 1) + (r >> 3), j = 8 * (xcd & 1) + (r & 7);
  const int ti = 4 * xcd + (r >> 3), dj = r & 7;
  const int i16 = lane & 15, g = lane >> 4;
  const rsrc_t rws = mksrd(P.ws, WS_END);
  const rsrc_t rx = mksrd(P.x, 2048 * 1280);
  const rsrc_t rCre = mksrd(P.Cre, 640 * 1280), rCim = mksrd(P.Cim, 640 * 1280);
  const int kq = w & 3, kh = w >> 2;
  const int rot10 = ((r & 7) + 2 * (r >> 3) + 3 * xcd) % 10;
  u32 tag;
  STAMP(0);
#define LOAD_SEED() __hip_atomic_load((GAS u32*)P.ws, __ATOMIC_RELAXED, __HIP_MEMORY_SCOPE_AGENT)
  const u32 cf0 = i16 * 128 + ((g ^ ((i16 >> 1) & 7)) << 4);
  u32x4 c0[4][5];
  auto read_c0 = [&]() INL {
    sw_wait(L0 + C_CFULL, 1);
    const u32 a0 = opaque(L0 + kh * 40960 + cf0), a1 = opaque(L0 + kh * 40960 + (cf0 ^ 64));
#pragma unroll
    for (int k = 0; k < 4; ++k)
#pragma unroll
      for (int nt = 0; nt < 5; ++nt) c0[k][nt] = lds_ld<u32x4>(((k & 1) ? a1 : a0) + (k >> 1) * 10240 + nt * 2048);
    sw_arrive(L0 + C_C0);
  };
  auto get_tag = [&]() INL { sw_wait(L0 + C_TAG, 1); tag = __builtin_amdgcn_readfirstlane(lds_ld<u32>(L0 + L_TAG)); };
  if (tid < 16) lds_st<u32>(opaque(L0 + L_CNT + tid * 4), 0u);

  if (w < 4) {
    __builtin_amdgcn_s_setprio(2);
    const u32 vE = LOAD_SEED();
    u32x4 xs[3];
#pragma unroll
    for (int k = 0; k < 3; ++k) {
      const int qq = min(64 * k + lane, 159), t = qq / 10, cc = qq - 10 * t;
      xs[k] = *(const GAS u32x4*)((const GAS char*)P.x + (64 * ti + 16 * w + t) * 1280 + dj * 160 + cc * 16);
    }
    const int pl = lane < 40 ? lane : 39;
    const u32 pLre = ((const GAS u16*)P.Lre)[40 * j + pl], pLim = ((const GAS u16*)P.Lim)[40 * j + pl], pLdt = ((const GAS u16*)P.ldtA)[40 * j + pl];
    hwbar();
    tag = (u32)__builtin_amdgcn_readfirstlane(vE) + 1u;
    if (w == 0) { lds_st<u32>(opaque(L0 + L_TAG), tag); sw_arrive(L0 + C_TAG); }
    STAMP(14);
#pragma unroll
    for (int k = 0; k < 3; ++k) lds_st<u32x4>(L0 + L_RAW + w * 2560 + min(64 * k + lane, 159) * 16, xs[k]);
    sw_arrive(L0 + C_BAR);
    const float lre = bf2f(pLre), lim = bf2f(pLim);
    const float dtA = rbf(expf(bf2f(pLdt)));
    const float aA = dtA * lre, tA = dtA * lim;
    const f32x2 lam = cpowk(aA, tA, 1.f), l8 = cpowk(aA, tA, 8.f);
    f32x2 c2;
    {
      const float nr = lam.x - 1.f, ni = lam.y, den = 2.f * __builtin_amdgcn_rcpf(lre * lre + lim * lim);
      c2 = f32x2{(nr * lre + ni * lim) * den, (ni * lre - nr * lim) * den};
    }
    f32x2 zs[4], zh[4];
    zs[3] = cpowk(aA, tA, (float)(96 - 32 * w));
    zh[0] = cmul(cpowk(aA, tA, (float)(32 * w)), c2);
#pragma unroll
    for (int k = 1; k < 4; ++k) { zs[3 - k] = cmul(zs[4 - k], l8); zh[k] = cmul(zh[k - 1], l8); }
    asm volatile("" : "+v"(zs[0]), "+v"(zh[3]));
    sw_wait(L0 + C_BAR, 4);
    {
      const u32 ra = opaque(L0 + L_RAW + (16 * g + 4 * (lane & 3) + ((lane >> 2) & 3)) * 160 + 40 * w);
      const u32 im = L0 + L_TAB + w * 4096;
      const u32 wa = opaque(im + (i16 & 3) * 144 + (4 * g + (i16 >> 2)) * 8);
      s16x4 tv[5];
#pragma unroll
      for (int k = 0; k < 5; ++k) tv[k] = lds_tr(ra + k * 8);
#pragma unroll
      for (int k = 0; k < 5; ++k) lds_st<s16x4>(wa + k * 576, tv[k]);
      const u32 xa = opaque(im + (lane >> 3) * 144 + (lane & 7) * 16);
      const u32 xo = WS_XT + (u32)(80 * dj + 20 * w + (lane >> 3)) * 4096 + ti * 128 + (lane & 7) * 16;
      const u32x4 o0 = lds_ld<u32x4>(xa), o1 = lds_ld<u32x4>(xa + 1152), o2 = lds_ld<u32x4>(xa + 2304);
      st16(rws, xo, o0);
      st16(rws, xo + 8 * 4096, o1);
      st16(rws, lane < 32 ? xo + 16 * 4096 : OOB, o2);
    }
    STAMP(15);
    const int sig = 4 * w + g;
    float lr[5], ls[5], sa[5], sb[5], ha[5], hb[5], ca[5], cb[5];
    {
      const u32 tb = opaque(L0 + L_TAB + w * 4096 + pl * 8);
#pragma unroll
      for (int k = 0; k < 4; ++k) {
        lds_st<f32x2>(tb + k * 320, zs[k]);
        lds_st<f32x2>(tb + (4 + k) * 320, f32x2{zh[k].x, -zh[k].y});
      }
      lds_st<f32x2>(tb + 8 * 320, lam);
      lds_st<f32x2>(tb + 9 * 320, f32x2{c2.x, -c2.y});
      const u32 tp = opaque(L0 + L_TAB + w * 4096 + (i16 >> 1) * 8), tg = opaque(tp + g * 320);
      f32x2 q0[5], q1[5], q2[5], q3[5];
#pragma unroll
      for (int nt = 0; nt < 5; ++nt) {
        q0[nt] = lds_ld<f32x2>(tg + nt * 64); q1[nt] = lds_ld<f32x2>(tg + 4 * 320 + nt * 64);
        q2[nt] = lds_ld<f32x2>(tp + 8 * 320 + nt * 64); q3[nt] = lds_ld<f32x2>(tp + 9 * 320 + nt * 64);
      }
      sw_arrive(L0 + C_PRO);
      const float sg = (lane & 1) ? 1.f : -1.f;
#pragma unroll
      for (int nt = 0; nt < 5; ++nt) {
        sa[nt] = q0[nt].x; sb[nt] = sg * q0[nt].y; ha[nt] = -sg * q1[nt].x; hb[nt] = q1[nt].y;
        lr[nt] = q2[nt].x; ls[nt] = sg * q2[nt].y; ca[nt] = -sg * q3[nt].x; cb[nt] = q3[nt].y;
        asm volatile("" : "+v"(sb[nt]), "+v"(ha[nt]), "+v"(ls[nt]), "+v"(ca[nt]));
      }
    }
    const int nvalid = (c - w + 3) >> 2;
    f32x2 lam4 = cpowk(aA, tA, 512.f);
    f32x2 lame = cpowk(aA, tA, (float)(128 * ((c - 1 - w) & 3)));
    asm volatile("" : "+v"(lam4), "+v"(lame));
    STAMP(1);
    f32x4 acc[2][5], accg[2];
#pragma unroll
    for (int m = 0; m < 2; ++m) {
      accg[m] = f32x4{0.f, 0.f, 0.f, 0.f};
#pragma unroll
      for (int nt = 0; nt < 5; ++nt) acc[m][nt] = f32x4{0.f, 0.f, 0.f, 0.f};
    }
    const u32 ax0 = L0 + (32 * w + i16) * 128 + ((g ^ ((i16 >> 1) & 7)) << 4);
    const u32 ab0 = L0 + 16384 + (40 * (i16 & 1) + (i16 >> 1)) * 128 + ((g ^ (2 * (i16 >> 2) + (i16 & 1))) << 4);
#define G1_READ(S, so)                                                                                    \
    _Pragma("unroll") for (int k2 = 0; k2 < 2; ++k2) {                                                    \
      const u32 pa = (ax0 ^ (k2 * 64)) + (so), pb = (ab0 ^ (k2 * 64)) + (so);                             \
      const int H = k2 * (1 + S);                                                                         \
      fa[H][0] = lds_ld<u32x4>(pa); fb[H][0] = lds_ld<u32x4>(pb); fa[H][1] = lds_ld<u32x4>(pa + 2048); \
      _Pragma("unroll") for (int nt = 1; nt < 5; ++nt) fb[H][nt] = lds_ld<u32x4>(pb + nt * 1024);        \
      if constexpr (GATE) fg[H] = lds_ld<u32x4>(agw + k2 * 64);                                           \
    }                                                                                                     \
    if constexpr (GATE) { agw += 128; agw = (agw == agw_end) ? agw - 1280 : agw; }
#define G1_MM(S, k2)                                                                                      \
    _Pragma("unroll") for (int nt = 0; nt < 5; ++nt) {                                                    \
      acc[0][nt] = mfma(fa[k2 * (1 + S)][0], fb[k2 * (1 + S)][nt], acc[0][nt]);                           \
      acc[1][nt] = mfma(fa[k2 * (1 + S)][1], fb[k2 * (1 + S)][nt], acc[1][nt]);                           \
    }                                                                                                     \
    if constexpr (GATE) {                                                                                 \
      accg[0] = mfma(fa[k2 * (1 + S)][0], fg[k2 * (1 + S)], accg[0]);                                     \
      accg[1] = mfma(fa[k2 * (1 + S)][1], fg[k2 * (1 + S)], accg[1]);                                     \
    }
    auto gemm1 = [&](auto gate_c) INL {
    constexpr bool GATE = decltype(gate_c)::value;
    u32 agw = opaque(L0 + L_GW + g * 16 + rot10 * 128);
    const u32 agw_end = L0 + L_GW + g * 16 + 1280;
    u32x4 fa[3][2], fb[3][5], fg[3];
    const u32 cfull = opaque(L0 + C_FULL);
    sw_wait(cfull, 1);
    G1_READ(0, 0u);
    u32 sl = 0;
#define G1_MIX()                                                                                          \
      _Pragma("unroll") for (int k = 0; k < (GATE ? 8 : 7); ++k) {                                        \
        __builtin_amdgcn_sched_group_barrier(0x8, 1, 0);                                                  \
        __builtin_amdgcn_sched_group_barrier(0x100, 2, 0);                                                \
      }                                                                                                   \
      __builtin_amdgcn_sched_group_barrier(0x8, 4, 0);
#define G1_STAGE(S, need)                                                                                 \
      { const u32 cv = peek(cfull);                                                                       \
      SB(); G1_MM(S, 0); SB();                                                                            \
      sw_arrive(L0 + C_FREE + 4 * sl);                                                                    \
      sl = sl == 2 ? 0 : sl + 1;                                                                          \
      if ((u32)__builtin_amdgcn_readfirstlane(cv) < (u32)(need)) sw_wait(cfull, need);                    \
      const u32 so_ = sl * STG;                                                                           \
      G1_READ(1 - S, so_); G1_MM(S, 1); G1_MIX(); SB(); }
    int s = 0;
#pragma unroll 1
    for (; s < 8; s += 2) {
      G1_STAGE(0, s + 2);
      G1_STAGE(1, s + 3);
      if (s == 0) {
        waitv<0>();
        st8(rws, lane == 0 ? WS_FX + (u32)((ti * 8 + dj) * 4 + w) * 128 : OOB, u32x2{tag, tag ^ FMIX});
      }
    }
    G1_STAGE(0, 10);
    {
      const u32 cv = peek(L0 + C_FREE + 8);
      SB(); G1_MM(1, 0); G1_MM(1, 1); SB();
      if ((u32)__builtin_amdgcn_readfirstlane(cv) < 12u) sw_wait(L0 + C_FREE + 8, 12);
    }
    };
    if (j == c) gemm1(BoolC<true>{}); else gemm1(BoolC<false>{});
    asm volatile("s_nop 15" : "+v"(acc[0][0]), "+v"(acc[0][1]), "+v"(acc[0][2]), "+v"(acc[0][3]), "+v"(acc[0][4]), "+v"(acc[1][0]),
                 "+v"(acc[1][1]), "+v"(acc[1][2]), "+v"(acc[1][3]), "+v"(acc[1][4]));
    STAMP(2);
    float v[8][5];
#pragma unroll
    for (int jj = 0; jj < 8; ++jj)
#pragma unroll
      for (int nt = 0; nt < 5; ++nt) v[jj][nt] = acc[jj >> 2][nt][jj & 3];
    float e[5];
#pragma unroll
    for (int nt = 0; nt < 5; ++nt) e[nt] = v[0][nt];
#pragma unroll
    for (int jj = 1; jj < 8; ++jj) scan_step(e, v[jj], e, lr, ls);
    {
      float z[5] = {0.f, 0.f, 0.f, 0.f, 0.f}, we[5];
      scan_step(we, z, e, sa, sb);
      auto s01 = __builtin_amdgcn_permlane32_swap(f2u(we[0]), f2u(we[1]), false, false);
      auto s23 = __builtin_amdgcn_permlane32_swap(f2u(we[2]), f2u(we[3]), false, false);
      auto s44 = __builtin_amdgcn_permlane32_swap(f2u(we[4]), f2u(we[4]), false, false);
      const float y01 = u2f(s01[0]) + u2f(s01[1]), y23 = u2f(s23[0]) + u2f(s23[1]), y4 = u2f(s44[0]) + u2f(s44[1]);
      auto q03 = __builtin_amdgcn_permlane16_swap(f2u(y01), f2u(y23), false, false);
      auto q44 = __builtin_amdgcn_permlane16_swap(f2u(y4), f2u(y4), false, false);
      const u32 wa = opaque(L0 + L_WS + w * 320 + i16 * 4);
      lds_st<float>(wa + (2 * (g & 1) + (g >> 1)) * 64, u2f(q03[0]) + u2f(q03[1]));
      lds_st<float>(wa + 256, u2f(q44[0]) + u2f(q44[1]));
      const u32 ra = opaque(L0 + L_ERAW + sig * 320 + i16 * 4);
#pragma unroll
      for (int nt = 0; nt < 5; ++nt) lds_st<float>(ra + nt * 64, e[nt]);
    }
    sw_arrive(L0 + C_BAR);
    sw_wait(L0 + C_BAR, 8);
    if (w == 2) {
      const u32 ap = opaque(L0 + L_WS + pl * 8);
      const f32x2 se = (lds_ld<f32x2>(ap) + lds_ld<f32x2>(ap + 320)) + (lds_ld<f32x2>(ap + 640) + lds_ld<f32x2>(ap + 960));
      st16(rws, lane < 40 ? WS_SG + (u32)(c * 640 + 40 * j + lane) * 16 : OOB, u32x4{f2u(se.x), tag, f2u(se.y), tag});
    }
    STAMP(3);
    if (j == c) {
      float gl = accg[0][0];
#pragma unroll
      for (int k = 1; k < 8; ++k) gl = (i16 == k) ? accg[k >> 2][k & 3] : gl;
      st8(rws, i16 < 8 ? WS_GG + (u32)(128 * c + 32 * w + 8 * g + i16) * 8 : OOB, u32x2{f2u(gl), tag});
    }
    float pf[5];
    {
      u32 era = opaque(L0 + L_ERAW + pl * 8);
      const f32x2 l8r = rot(l8);
      f32x2 pp = {0.f, 0.f};
#pragma unroll 1
      for (int s2 = 0; s2 < w; ++s2, era += 1280) {
        const f32x2 e0 = lds_ld<f32x2>(era), e1 = lds_ld<f32x2>(era + 320), e2 = lds_ld<f32x2>(era + 640), e3 = lds_ld<f32x2>(era + 960);
        pp = cmul(pp, l8, l8r) + e0; pp = cmul(pp, l8, l8r) + e1; pp = cmul(pp, l8, l8r) + e2; pp = cmul(pp, l8, l8r) + e3;
      }
      const f32x2 e0 = lds_ld<f32x2>(era), e1 = lds_ld<f32x2>(era + 320), e2 = lds_ld<f32x2>(era + 640);
      const u32 ptb = L0 + (w == 0 ? 79360 : w == 1 ? 80640 : w == 2 ? L_WS : L_ERAW + 3840);
      const u32 pw = opaque(ptb + pl * 8);
#pragma unroll
      for (int k = 0; k < 4; ++k) {
        const f32x2 q = cmul(pp, c2);
        lds_st<f32x2>(pw + k * 320, f32x2{q.x, -q.y});
        if (k < 3) pp = cmul(pp, l8, l8r) + (k == 0 ? e0 : k == 1 ? e1 : e2);
      }
      const u32 pr_ = opaque(ptb + g * 320 + i16 * 4);
#pragma unroll
      for (int nt = 0; nt < 5; ++nt) pf[nt] = lds_ld<float>(pr_ + nt * 64);
      const float z[5] = {0.f, 0.f, 0.f, 0.f, 0.f};
#pragma unroll
      for (int jj = 0; jj < 8; ++jj) scan_step(v[jj], z, v[jj], ca, cb);
#pragma unroll
      for (int nt = 0; nt < 5; ++nt) ls[nt] = -ls[nt];
    }
    {
      u32 off[4];
#pragma unroll
      for (int k = 0; k < 4; ++k) off[k] = (k < nvalid && lane < 40) ? WS_SG + (u32)((w + 4 * k) * 640 + 40 * j + lane) * 16 : OOB;
      u32x4 sv[4];
#pragma unroll 1
      for (int it = 0; it < SPIN_CAP; ++it) {
        asm volatile("" ::: "memory");
        bool ok = true;
#pragma unroll
        for (int k = 0; k < 4; ++k) sv[k] = ld16(rws, off[k]);
#pragma unroll
        for (int k = 0; k < 4; ++k) ok &= (off[k] == OOB) | ((sv[k].y == tag) & (sv[k].w == tag));
        if (wave_all(ok)) break;
        __builtin_amdgcn_s_sleep(1);
      }
      asm volatile("" ::: "memory");
      STAMP(4);
      f32x2 hsum = {0.f, 0.f};
      const f32x2 l4r = rot(lam4);
#pragma unroll
      for (int k = 0; k < 4; ++k)
        if (k < nvalid) hsum = cmul(hsum, lam4, l4r) + f32x2{u2f(sv[k].x), u2f(sv[k].z)};
      hsum = cmul(hsum, lame);
      const u32 hp = opaque(L0 + L_HP + lane * 32 + w * 4);
      if (lane < 40) {
        lds_st<float>(hp, hsum.x);
        lds_st<float>(hp + 16, hsum.y);
      }
    }
    sw_arrive(L0 + C_BAR);
    sw_wait(L0 + C_BAR, 12);
    {
      float hin[5], h0[5];
      const u32 hp = opaque(L0 + L_HP + i16 * 16);
#pragma unroll
      for (int nt = 0; nt < 5; ++nt) {
        const f32x4 q = lds_ld<f32x4>(hp + nt * 256);
        hin[nt] = (q.x + q.y) + (q.z + q.w);
      }
      scan_step(h0, pf, hin, ha, hb);
      scan_step(v[0], v[0], h0, lr, ls);
#pragma unroll
      for (int jj = 1; jj < 8; ++jj) scan_step(v[jj], v[jj], v[jj - 1], lr, ls);
    }
    {
      const u32 img = L0 + L_HT + w * 1280;
      const u32 wa = opaque(img + i16 * 80 + g * 16);
      const u32 ra = opaque(img + (2 * ((lane >> 2) & 3) + (g & 1)) * 80 + (g >> 1) * 32 + (lane & 3) * 8);
      const u32 hb0 = WS_H + (u32)(8 * c + 2 * w + (g >> 1)) * 40960 + (g & 1) * 2048 + i16 * 16;
      u32x2 lo[5], hi[5];
#pragma unroll
      for (int nt = 0; nt < 5; ++nt) {
        lds_st<u32x4>(wa, u32x4{pk(v[0][nt], v[1][nt]), pk(v[2][nt], v[3][nt]), pk(v[4][nt], v[5][nt]), pk(v[6][nt], v[7][nt])});
        lo[nt] = __builtin_bit_cast(u32x2, lds_tr(ra));
        hi[nt] = __builtin_bit_cast(u32x2, lds_tr(ra + 640));
      }
      SB();
#pragma unroll
      for (int nt = 0; nt < 5; ++nt) {
        const int o = 5 * j + nt;
        st16(rws, hb0 + (u32)((o >> 3) * 4096 + ((o >> 2) & 1) * 1024 + (o & 3) * 256), u32x4{lo[nt].x, lo[nt].y, hi[nt].x, hi[nt].y});
      }
    }
    STAMP(5);
    read_c0();
    waitv<0>();
    st8(rws, lane == 0 ? WS_FH + (u32)((c * 4 + w) * 16 + j) * 128 : OOB, u32x2{tag, tag ^ FMIX});
    STAMP(6);
  } else {
    const int pr = xcd * 16 + (r >> 1);
    const int nch = (r & 1) ? 2 : 3, ch = 5 * pr + ((r & 1) ? 3 : 0) + kq;
    const u32 slot = opaque(L0 + L_B0 + kq * BSLOT) ;
    if (kq == 3) {
      __builtin_amdgcn_s_setprio(3);
      hwbar();
      const rsrc_t rBre = mksrd(P.Bre, 640 * 1280), rBim = mksrd(P.Bim, 640 * 1280), rgw = mksrd(P.gw, 1280);
      const int q8 = lane >> 3;
      u32 vx[4], vb[4];
#pragma unroll
      for (int k = 0; k < 4; ++k) {
        const int m = k >> 1, i = 8 * (k & 1) + q8;
        const int t = 128 * c + 8 * (i >> 2) + 4 * m + (i & 3);
        vx[k] = (u32)t * 1280 + (((lane & 7) ^ ((i >> 1) & 7)) << 4) - 1024 * k;
        vb[k] = (u32)(40 * j + q8) * 1280 + (((lane & 7) ^ (2 * (q8 >> 1))) << 4) + 9216 * k;
      }
      const u32 vi0 = vb[0] ^ 16, vi1 = vb[1] ^ 16, vi2 = vb[2] ^ 16, vi3 = vb[3] ^ 16;
      auto head = [&](int s, u32 sl) INL {
        s += rot10; s = s >= 10 ? s - 10 : s;
#pragma unroll
        for (int k = 0; k < 4; ++k) dma64v(rx, vx[0], vx[1], vx[2], vx[3], (u32)s * 128 + k * 40960, sl + k * 4096);
        dma64v(rBre, vb[0], vb[1], vb[2], vb[3], (u32)s * 128, sl + 16384);
      };
      auto tail = [&](int s, u32 sl) INL {
        s += rot10; s = s >= 10 ? s - 10 : s;
        dma16(rBre, vb[0], (u32)s * 128 + 40960, sl + 16384 + 4096);
        dma64v(rBim, vi0, vi1, vi2, vi3, (u32)s * 128, sl + 16384 + 5120);
        dma16(rBim, vi0, (u32)s * 128 + 40960, sl + 16384 + 5120 + 4096);
      };
      head(0, L0); tail(0, L0);
      dma16(rgw, lane * 16, 0, L0 + L_GW);
      if (lane < 16) dma16(rgw, lane * 16, 1024, L0 + L_GW + 1024);
      head(1, L0 + STG); tail(1, L0 + STG);
      waitv<26>();
      sw_arrive(L0 + C_FULL);
      STAMP(1);
      sw_wait(L0 + C_PRO, 4);
      u32 sl = 2, need = 0;
#pragma unroll 1
      for (int s = 2; s < 10; ++s) {
        if (s > 2) sw_wait(L0 + C_FREE + 4 * sl, need);
        head(s, L0 + sl * STG);
        waitv<20>();
        sw_arrive(L0 + C_FULL);
        tail(s, L0 + sl * STG);
        if (sl == 2) { sl = 0; need += 4; } else ++sl;
      }
      waitv<0>();
      sw_arrive(L0 + C_FULL);
      STAMP(2);
      u32 vc[4];
#pragma unroll
      for (int k = 0; k < 4; ++k) vc[k] = (u32)(80 * dj + q8) * 1280 + (((lane & 7) ^ (lane >> 4) ^ (4 * (k & 1))) << 4) + 9216 * k;
      auto stageC = [&](int u, u32 dst) INL {
        const rsrc_t rs = (u & 1) ? rCim : rCre;
        const u32 so = (u32)(u >> 1) * 128;
        dma64v(rs, vc[0], vc[1], vc[2], vc[3], so, dst);
        dma64v(rs, vc[0], vc[1], vc[2], vc[3], so + 40960, dst + 4096);
        dma32v(rs, vc[0], vc[1], so + 81920, dst + 8192);
      };
      sw_wait(L0 + C_BAR, 8);
#pragma unroll 1
      for (int q = 0; q < 6; ++q) { const int hh = q & 1, uu = q >> 1; stageC(10 * hh + uu, L0 + (4 * hh + uu) * 10240); }
      stageC(3, L0 + 3 * 10240);
      waitv<10>();
      sw_arrive(L0 + C_CFULL);
      STAMP(3);
      sw_wait(L0 + C_BDONE, 3);
#pragma unroll 1
      for (int q = 0; q < 7; ++q) { const int hh = q & 1, uu = q >> 1; stageC(10 * hh + 4 + uu, L0 + L_B0 + (4 * hh + uu) * 10240); }
      STAMP(4);
      get_tag();
      read_c0();
      sw_wait(L0 + C_C0, 8);
      stageC(13, L0 + 7 * 10240);
      stageC(17, L0 + L_B0 + 7 * 10240);
#pragma unroll 1
      for (int q = 0; q < 4; ++q) { const int hh = q & 1, uu = q >> 1; stageC(10 * hh + 8 + uu, L0 + (4 * hh + uu) * 10240); }
      STAMP(5);
    } else if (kq < nch) {
      const GAS u32* pa = (const GAS u32*)P.Are + ch * 64 + lane;
      const u32 wAre = *pa, wAim = ((const GAS u32*)P.Aim)[ch * 64 + lane];
      const u32 wBre = ((const GAS u32*)P.sBre)[ch * 64 + lane], wBim = ((const GAS u32*)P.sBim)[ch * 64 + lane];
      const u32 wCre = ((const GAS u32*)P.sCre)[ch * 64 + lane], wCim = ((const GAS u32*)P.sCim)[ch * 64 + lane];
      const u32 wdt = ((const GAS u16*)P.ldtB)[ch];
      hwbar();
      const float dt = rbf(expf(bf2f(wdt)));
      f32x4 ka[3] = {f32x4{0.f, 0.f, 0.f, 0.f}, f32x4{0.f, 0.f, 0.f, 0.f}, f32x4{0.f, 0.f, 0.f, 0.f}};
      const u32 tw = slot + lane * 4, tr = slot + i16 * TROW + g * 16;
      const u32 fxo = lane < 32 ? WS_FX + (u32)((lane * 8 + ch / 80) * 4 + (ch % 80) / 20) * 128 : OOB;
#pragma unroll 1
      for (int rd = 0; rd < 2; ++rd) {
        const u32 sh = rd ? 0u : 16u;
        const float a = dt * bfhi(wAre << sh), t = dt * bfhi(wAim << sh);
        const f32x2 db = {dt * bfhi(wBre << sh), dt * bfhi(wBim << sh)}, cc = {bfhi(wCre << sh), bfhi(wCim << sh)};
        const f32x2 W = cmul(cc, db);
        const f32x2 z1 = cpowk(a, t, 1.f), z8 = cpowk(a, t, 8.f), z128 = cpowk(a, t, 128.f);
        {
          const f32x2 zr = rot(z8);
          f32x2 cv = z8;
          lds_st<u32>(tw + 41 * TROW, 0x00003f80u);
          lds_st<u32>(tw + 42 * TROW, pk(cv.x, cv.y));
#pragma unroll
          for (int jj = 2; jj < 16; ++jj) { cv = cmul(cv, z8, zr); lds_st<u32>(tw + (41 + jj) * TROW, pk(cv.x, cv.y)); }
        }
        {
          const f32x2 y1 = {z1.x, -z1.y}, y1r = rot(y1), yh = {z128.x, -z128.y}, yhr = rot(yh);
          f32x2 R[8];
          R[0] = f32x2{W.x, -W.y};
#pragma unroll
          for (int k = 1; k < 8; ++k) R[k] = cmul(R[k - 1], y1, y1r);
#pragma unroll
          for (int k = 0; k < 8; ++k) lds_st<u32>(tw + k * TROW, pk(R[k].x, R[k].y));
#pragma unroll
          for (int k = 0; k < 8; ++k) { R[k] = cmul(R[k], yh, yhr); lds_st<u32>(tw + (8 + k) * TROW, pk(R[k].x, R[k].y)); }
#pragma unroll
          for (int hh = 0; hh < 2; ++hh)
#pragma unroll
            for (int k = 0; k < 4; ++k) { R[2 * k] = cmul(R[2 * k], yh, yhr); lds_st<u32>(tw + (16 + 4 * hh + k) * TROW, pk(R[2 * k].x, R[2 * k].y)); }
#pragma unroll
          for (int hh = 0; hh < 4; ++hh)
#pragma unroll
            for (int k = 0; k < 2; ++k) { R[4 * k] = cmul(R[4 * k], yh, yhr); lds_st<u32>(tw + (24 + 2 * hh + k) * TROW, pk(R[4 * k].x, R[4 * k].y)); }
#pragma unroll
          for (int hh = 0; hh < 9; ++hh) { R[0] = cmul(R[0], yh, yhr); lds_st<u32>(tw + (32 + hh) * TROW, pk(R[0].x, R[0].y)); }
        }
#pragma unroll
        for (int ks = 0; ks < 4; ++ks) {
          const u32x4 bb = lds_ld<u32x4>(tr + 41 * TROW + ks * 64);
#pragma unroll
          for (int m = 0; m < 3; ++m) ka[m] = mfma(lds_ld<u32x4>(tr + 16 * m * TROW + ks * 64), bb, ka[m]);
        }
      }
      STAMP(1);
      get_tag();
      const u32x2 fx = ld8(rws, fxo);
      const u32 kc = slot + KC_OFF;
      lds_st<u32x2>(kc + (128 * (g >> 1) + 8 * i16 + 4 * (g & 1)) * 2, u32x2{pk(ka[0].x, ka[0].y), pk(ka[0].z, ka[0].w)});
      {
        const u32 p0 = pk(ka[1].x, ka[1].y), p1 = pk(ka[1].z, ka[1].w);
        const u32 a0 = g < 2 ? 256 + 64 * g + 4 * i16 : 384 + 64 * (g - 2) + 2 * i16;
        lds_st<u32>(kc + a0 * 2, p0);
        lds_st<u32>(kc + (a0 + (g < 2 ? 2 : 32)) * 2, p1);
        const u32 q0 = pk(ka[2].x, ka[2].y), q1 = pk(ka[2].z, ka[2].w);
        const u32 a2 = kc + (512 + 64 * g + i16) * 2;
        if (g < 3) lds_st<u16>(a2, (u16)q0);
        if (g < 2) { lds_st<u16>(a2 + 32, (u16)(q0 >> 16)); lds_st<u16>(a2 + 64, (u16)q1); lds_st<u16>(a2 + 96, (u16)(q1 >> 16)); }
        if (lane == 0) lds_st<u32>(kc - 4, 0u);
      }
      if (!flag_ok(fx, fxo, tag)) poll_flags(rws, fxo, tag);
      asm volatile("" ::: "memory");
      u32x4 uv[4];
#pragma unroll
      for (int k = 0; k < 4; ++k) uv[k] = ld16(rws, WS_XT + (u32)ch * 4096 + (64 * k + lane) * 16);
      SB();
      const u32 k0 = slot + KOFF + KPAD, k1 = k0 + KCOPY;
      if (lane < 14) lds_st<u32x4>(slot + KOFF + (lane >= 7 ? KCOPY - 112 : 0) + lane * 16, u32x4{0u, 0u, 0u, 0u});
#define PERM(hi, lo, sel) __builtin_amdgcn_perm(hi, lo, sel)
      if (lane < 32) {
        const u32x4 d = lds_ld<u32x4>(kc + lane * 16);
        const u32 dm = lds_ld<u32>(kc + lane * 16 - 4);
        lds_st<u32x4>(k1 + lane * 16, u32x4{PERM(d.w, d.w, 0x01000302u), PERM(d.z, d.z, 0x01000302u), PERM(d.y, d.y, 0x01000302u), PERM(d.x, d.x, 0x01000302u)});
        lds_st<u32x4>(k0 + lane * 16, u32x4{PERM(d.z, d.w, 0x07060100u), PERM(d.y, d.z, 0x07060100u), PERM(d.x, d.y, 0x07060100u), PERM(dm, d.x, 0x07060100u)});
      } else {
        const u32 ea_ = kc + (256 + 4 * (lane - 32)) * 2;
        const u32x2 E = lds_ld<u32x2>(ea_);
        const u32 E2 = lds_ld<u32>(ea_ + 8), Em = lds_ld<u32>(ea_ - 4);
        const u32 e22 = PERM(E.y, E.y, 0x01000100u);
        lds_st<u32x4>(k1 + lane * 16, u32x4{PERM(E.y, E2, 0x07060100u), e22, PERM(E.x, E.y, 0x07060100u), PERM(E.x, E.x, 0x01000100u)});
        lds_st<u32x4>(k0 + lane * 16, u32x4{PERM(E.y, E.y, 0x01000302u), e22, PERM(E.x, E.x, 0x01000302u), PERM(Em, E.x, lane == 32 ? 0x07060100u : 0x01000100u)});
      }
      {
        const u32 ea_ = kc + (384 + 2 * lane) * 2;
        const u32 E0 = lds_ld<u32>(ea_), E1 = lds_ld<u32>(ea_ + 4);
        const u32 e00 = PERM(E0, E0, 0x01000100u), e11 = PERM(E0, E0, 0x03020302u);
        lds_st<u32x4>(k1 + (64 + lane) * 16, u32x4{PERM(E1, E1, 0x01000100u), e11, PERM(E0, E0, 0x01000302u), e00});
        lds_st<u32x4>(k0 + (64 + lane) * 16, u32x4{PERM(E0, E1, 0x07060100u), e11, e00, e00});
      }
#pragma unroll
      for (int rr = 0; rr < 2; ++rr) {
        const u32 ea_ = kc + (512 + 64 * rr + lane) * 2;
        const u32 e0 = lds_ld<u16>(ea_), e1 = lds_ld<u16>(ea_ + 2);
        const u32 t4 = (lane & 1) ? e1 : e0;
        const u32 e00 = e0 | (e0 << 16), e11 = e1 | (e1 << 16);
        lds_st<u32x4>(k1 + (128 + 64 * rr + lane) * 16, u32x4{e11, e1 | (t4 << 16), e00, e00});
        lds_st<u32x4>(k0 + (128 + 64 * rr + lane) * 16, u32x4{e11, t4 | (e0 << 16), e00, e00});
      }
      if (lane < 25) {
#pragma unroll
        for (int cc = 0; cc < 4; ++cc) lds_st<u32x4>(slot + cc * UCOPY + lane * 16, u32x4{0u, 0u, 0u, 0u});
      }
      STAMP(2);
#pragma unroll
      for (int k = 0; k < 4; ++k) {
        const u32 ua = slot + UPAD + (64 * k + lane) * 16;
        lds_st<u32x4>(ua, uv[k]);
#pragma unroll
        for (int e = 0; e < 4; ++e) { lds_st<u32>(ua + UCOPY + 4 + 4 * e, uv[k][e]); lds_st<u32>(ua + 3 * UCOPY + 12 + 4 * e, uv[k][e]); }
        lds_st<u32x2>(ua + 2 * UCOPY + 8, u32x2{uv[k].x, uv[k].y});
        lds_st<u32x2>(ua + 2 * UCOPY + 16, u32x2{uv[k].z, uv[k].w});
      }
      f32x4 ya[8];
#pragma unroll
      for (int A = 0; A < 8; ++A) ya[A] = f32x4{0.f, 0.f, 0.f, 0.f};
      const u32 ta = opaque(slot + KOFF + KPAD + (i16 & 1) * KCOPY + ((i16 >> 1) - g) * 16 - 64);
      const u32 ua = opaque(slot + (i16 & 3) * UCOPY + (i16 >> 2) * 128 + g * 16);
      u32x4 T[2][8], U[2][8];
#define CV_READ(S, st)                                                                                        \
      _Pragma("unroll") for (int D = 0; D < 8; ++D) {                                                         \
        T[S][D] = lds_ld<u32x4>(ta + ((st) >> 1) * 128 + D * 512 + (1 - ((st) & 1)) * 64);                   \
        U[S][D] = lds_ld<u32x4>(ua + UPAD - ((st) >> 1) * 128 + D * 512 + ((st) & 1) * 64);                  \
      }
      CV_READ(0, 0);
#pragma unroll
      for (int st = 0; st < 8; ++st) {
        SB();
        if (st < 7) { CV_READ((st + 1) & 1, st + 1); }
#pragma unroll
        for (int S = 0; S < 8; ++S)
#pragma unroll
          for (int D = 0; D <= S; ++D) ya[S] = mfma(T[st & 1][D], U[st & 1][S - D], ya[S]);
        if (st < 7) {
#pragma unroll
          for (int k = 0; k < 16; ++k) {
            __builtin_amdgcn_sched_group_barrier(0x8, 2, 0);
            __builtin_amdgcn_sched_group_barrier(0x100, 1, 0);
          }
          __builtin_amdgcn_sched_group_barrier(0x8, 4, 0);
        }
      }
      SB();
      STAMP(3);
      {
        const u32 oa = slot + ((i16 >> 2) * 36 + 8 * g + 3 - (i16 & 3)) * 4;
#pragma unroll
        for (int A = 0; A < 8; ++A) {
          lds_st<u32>(oa + A * 576, pk(ya[A].x, ya[A].y));
          lds_st<u32>(oa + A * 576 + 16, pk(ya[A].z, ya[A].w));
        }
#pragma unroll
        for (int k = 0; k < 4; ++k) {
          const int m = 64 * k + lane;
          st16(rws, WS_BT + (u32)ch * 4096 + m * 16, lds_ld<u32x4>(slot + (m >> 3) * 144 + (m & 7) * 16));
        }
        sw_arrive(L0 + C_BDONE);
        read_c0();
        waitv<0>();
        st8(rws, lane == 0 ? WS_FB + (u32)ch * 128 : OOB, u32x2{tag, tag ^ FMIX});
      }
      STAMP(4);
    } else {
      hwbar();
      get_tag();
      sw_arrive(L0 + C_BDONE);
      read_c0();
    }
  }

  __builtin_amdgcn_s_setprio(0);
  STAMP(7);
  u32x2 eda[3], edb[3];
#pragma unroll
  for (int k = 0; k < 3; ++k) {
    const int d0 = 80 * dj + min(48 * kh + 16 * k, 64) + 4 * g;
    eda[k] = *(const GAS u32x2*)((const GAS u16*)P.DA + d0);
    edb[k] = *(const GAS u32x2*)((const GAS u16*)P.DB + d0);
  }
  u32x4 xt0, xt1;
  {
    const int q1 = min(512 + tid, 639), t0 = tid / 10, t1 = q1 / 10;
    xt0 = *(const GAS u32x4*)((const GAS char*)P.x + (64 * ti + t0) * 1280 + dj * 160 + (tid - 10 * t0) * 16);
    xt1 = *(const GAS u32x4*)((const GAS char*)P.x + (64 * ti + t1) * 1280 + dj * 160 + (q1 - 10 * t1) * 16);
  }
  float gate, ogate;
  {
    const u32 go = WS_GG + (u32)(64 * ti + 16 * kq + i16) * 8;
    u32x2 gv;
#pragma unroll 1
    for (int it = 0; it < SPIN_CAP; ++it) {
      asm volatile("" ::: "memory");
      gv = ld8(rws, go);
      if (wave_all(gv.y == tag)) break;
      __builtin_amdgcn_s_sleep(1);
    }
    gate = rbf(1.f / (1.f + expf(-rbf(u2f(gv.x)))));
    ogate = rbf(1.f - gate);
    asm volatile("" : "+v"(gate), "+v"(ogate));
  }
  poll_flags(rws, lane < 8 ? WS_FH + (u32)((2 * ti + (kq >> 1)) * 16 + 8 * kh + lane) * 128 : OOB, tag);
  STAMP(8);
  const u32 fbo = lane < 8 ? WS_FB + (u32)(80 * dj + 8 * w + lane) * 128 : (lane < 16 && w < 2) ? WS_FB + (u32)(80 * dj + 56 + 8 * w + lane) * 128 : OOB;
  const u32x2 fbv = ld8(rws, fbo);
  u32x4 hf_[20];
  const u32 ho = WS_H + (u32)((4 * ti + kq) * 40 + 20 * kh) * 1024 + lane * 16;
#pragma unroll
  for (int ks = 0; ks < 12; ++ks) hf_[ks] = ld16(rws, ho + ks * 1024);
  SB();
  f32x4 y[5];
#pragma unroll
  for (int nt = 0; nt < 5; ++nt) y[nt] = f32x4{0.f, 0.f, 0.f, 0.f};
  const u32 cab[2][2] = {{opaque(L0 + kh * 40960 + cf0), opaque(L0 + kh * 40960 + (cf0 ^ 64))},
                         {opaque(L0 + L_B0 + kh * 40960 + cf0), opaque(L0 + L_B0 + kh * 40960 + (cf0 ^ 64))}};
#define G2_UNIT(uu)                                                                                         \
  _Pragma("unroll") for (int hf = 0; hf < 2; ++hf)                                                          \
  _Pragma("unroll") for (int nt = 0; nt < 5; ++nt)                                                          \
    y[nt] = mfma(lds_ld<u32x4>(cab[(uu >> 2) & 1][hf] + (uu & 3) * 10240 + nt * 2048), hf_[2 * uu + hf], y[nt]);
#pragma unroll
  for (int k = 0; k < 4; ++k)
#pragma unroll
    for (int nt = 0; nt < 5; ++nt) y[nt] = mfma(c0[k][nt], hf_[k], y[nt]);
  G2_UNIT(2);
  SB();
  STAMP(9);
  if (!flag_ok(fbv, fbo, tag)) poll_flags(rws, fbo, tag);
  asm volatile("" ::: "memory");
  const u32 bo = WS_BT + (u32)(80 * dj + (tid >> 3)) * 4096 + ti * 128 + (lane & 7) * 16;
  const u32x4 bp0 = ld16(rws, bo), bp1 = ld16(rws, w < 2 ? bo + 64 * 4096 : OOB);
#pragma unroll
  for (int ks = 12; ks < 20; ++ks) hf_[ks] = ld16(rws, ho + ks * 1024);
  SB();
  hwbar();
  if (b == 255 && tid == 0) __hip_atomic_store((GAS u32*)P.ws, tag, __ATOMIC_RELAXED, __HIP_MEMORY_SCOPE_AGENT);
  lds_st<u32x4>(L0 + L_XI + tid * 16, xt0);
  if (w < 2) lds_st<u32x4>(L0 + L_XI + 8192 + tid * 16, xt1);
  G2_UNIT(3); G2_UNIT(4); G2_UNIT(5);
  SB();
  lds_st<u32x4>(L0 + L_BI + tid * 16, bp0);
  if (w < 2) lds_st<u32x4>(L0 + L_BI + 8192 + tid * 16, bp1);
  hwbar();
  STAMP(10);
  f32x4 base[3];
  {
    const u32 xa = opaque(L0 + L_XI + (16 * kq + i16) * 160 + 96 * kh + 8 * g);
    const u32 ba = opaque(L0 + L_BI + (48 * kh + 4 * g + ((lane >> 2) & 3)) * 128 + 32 * kq + (lane & 3) * 8);
#pragma unroll
    for (int k = 0; k < 3; ++k) {
      if (k == 2 && kh) break;
      const u32x2 xv = lds_ld<u32x2>(xa + 32 * k), bv = __builtin_bit_cast(u32x2, lds_tr(ba + 2048 * k));
#pragma unroll
      for (int e = 0; e < 2; ++e) {
        const float x0 = bflo(xv[e]), x1 = bfhi(xv[e]);
        const u32 sa_ = pk(x0 * bflo(eda[k][e]), x1 * bfhi(eda[k][e]));
        const u32 sb_ = pk(x0 * bflo(edb[k][e]), x1 * bfhi(edb[k][e]));
        const u32 bb = pk(bflo(bv[e]) + bflo(sb_), bfhi(bv[e]) + bfhi(sb_));
        const u32 ob = pk(ogate * bflo(bb), ogate * bfhi(bb));
        base[k][2 * e] = gate * bflo(sa_) + bflo(ob);
        base[k][2 * e + 1] = gate * bfhi(sa_) + bfhi(ob);
      }
      asm volatile("" : "+v"(base[k]));
    }
  }
  SB();
  G2_UNIT(6); G2_UNIT(7); G2_UNIT(8); G2_UNIT(9);
  STAMP(11);
  {
    const u32 ia = opaque(L0 + (kh ? L_IMG1 : L_IMG0) + (16 * kq + i16) * 320 + (g ^ ((i16 >> 1) & 3)) * 16);
    if (kh == 0) {
#pragma unroll
      for (int nt = 0; nt < 5; ++nt) lds_st<f32x4>(ia + nt * 64, nt < 3 ? gate * y[nt] + base[nt < 3 ? nt : 0] : gate * y[nt]);
    } else {
#pragma unroll
      for (int nt = 0; nt < 5; ++nt) lds_st<f32x4>(ia + nt * 64, nt >= 3 ? gate * y[nt] + base[nt >= 3 ? nt - 3 : 0] : gate * y[nt]);
    }
  }
  hwbar();
  STAMP(12);
  {
    const int et = tid >> 3, es = tid & 7;
    const u32 ia = opaque(L0 + L_IMG0 + et * 320 + ((es & 4) | ((es ^ (et >> 1)) & 3)) * 16);
    GAS char* op = (GAS char*)P.out + (64 * ti + et) * 2560 + dj * 320 + es * 16;
    const f32x4 o0 = lds_ld<f32x4>(ia) + lds_ld<f32x4>(ia + (L_IMG1 - L_IMG0));
    const f32x4 o1 = lds_ld<f32x4>(ia + 128) + lds_ld<f32x4>(ia + 128 + (L_IMG1 - L_IMG0));
    *(GAS f32x4*)op = o0;
    *(GAS f32x4*)(op + 128) = o1;
    if (es < 4) *(GAS f32x4*)(op + 256) = lds_ld<f32x4>(ia + 256) + lds_ld<f32x4>(ia + 256 + (L_IMG1 - L_IMG0));
  }
  STAMP(13);
}

extern "C" void kernel_launch(void* const* d_in, const int* in_sizes, int n_in, void* d_out, int out_size, void* d_ws,
                              size_t ws_size, hipStream_t stream) {
  Params p;
  p.x = (const u16*)d_in[0];
  p.Lre = (const u16*)d_in[1];
  p.Lim = (const u16*)d_in[2];
  p.Bre = (const u16*)d_in[3];
  p.Bim = (const u16*)d_in[4];
  p.Cre = (const u16*)d_in[5];
  p.Cim = (const u16*)d_in[6];
  p.ldtA = (const u16*)d_in[7];
  p.DA = (const u16*)d_in[8];
  p.Are = (const u16*)d_in[10];
  p.Aim = (const u16*)d_in[11];
  p.sBre = (const u16*)d_in[12];
  p.sBim = (const u16*)d_in[13];
  p.sCre = (const u16*)d_in[14];
  p.sCim = (const u16*)d_in[15];
  p.ldtB = (const u16*)d_in[16];
  p.DB = (const u16*)d_in[17];
  p.gw = (const u16*)d_in[18];
  p.out = (float*)d_out;
  p.ws = (char*)d_ws;
  fused_kernel<<<256, 512, 0, stream>>>(p);
}
```

```cpp
#include <hip/hip_runtime.h>
#include <stdint.h>

typedef unsigned short u16;
typedef unsigned int u32;
typedef unsigned long long u64;
typedef __bf16 bf16x8 __attribute__((ext_vector_type(8)));
typedef short s16x4 __attribute__((ext_vector_type(4)));
typedef float f32x4 __attribute__((ext_vector_type(4)));
typedef float f32x2 __attribute__((ext_vector_type(2)));
typedef u32 u32x4 __attribute__((ext_vector_type(4)));
typedef u32 u32x2 __attribute__((ext_vector_type(2)));
typedef u64 u64x8 __attribute__((ext_vector_type(8)));
typedef u64 u64x4 __attribute__((ext_vector_type(4)));
typedef __bf16 bf16x2_t __attribute__((ext_vector_type(2)));
typedef __amdgpu_buffer_rsrc_t rsrc_t;
#define LAS __attribute__((address_space(3)))
#define GAS __attribute__((address_space(1)))
#define DEV __device__ __forceinline__
#define INL __attribute__((always_inline))
#if defined(__HIP_DEVICE_COMPILE__)
#define NOPK __attribute__((target("no-packed-fp32-ops")))
#else
#define NOPK
#endif

constexpr u32 WS_E = 0;
constexpr u32 WS_FH = 4096;
constexpr u32 WS_FX = WS_FH + 256 * 128;
constexpr u32 WS_SG = WS_FX + 1024 * 128;
constexpr u32 WS_GG = WS_SG + 16 * 640 * 16;
constexpr u32 WS_XT = WS_GG + 10 * 2048 * 8;
constexpr u32 WS_BT = WS_XT + 640 * 2048 * 2;
constexpr u32 WS_H = WS_BT + 640 * 1024 * 8;
constexpr u32 WS_END = WS_H + 128 * 40 * 1024;
constexpr u32 WS_DIAG = (WS_END + 4095) & ~4095u;

constexpr u32 STG = 26624;
constexpr u32 L_A0 = 81920;
constexpr u32 L_B0 = 0;
__attribute__((always_inline)) constexpr u32 ring(int s) { return s % 6 < 3 ? L_A0 + (s % 6) * STG : (s % 6 - 3) * STG; }
constexpr u32 BSLOT = 27008;
constexpr u32 L_CNT = 81024;
constexpr u32 C_FREE = L_CNT, C_PRO = L_CNT + 16, C_BAR = L_CNT + 32, C_BDONE = L_CNT + 48, C_C0 = L_CNT + 64, C_BPREP = L_CNT + 80,
              C_STRIP = L_CNT + 96, C_FULL = L_CNT + 112, C_CFULL = L_CNT + 116, C_CLR = L_CNT + 120, L_TAG = L_CNT + 128;
constexpr u32 L_GATE = L_CNT + 256;
constexpr u32 L_RAW = ring(5);
constexpr u32 L_TAB = ring(4);
constexpr u32 L_ERAW = L_A0;
constexpr u32 L_WS = L_A0 + 5120;
constexpr u32 L_HP = L_A0 + 6400;
constexpr u32 L_PT = L_A0 + 7680;
constexpr u32 L_HT = L_A0 + 12800;
constexpr u32 L_BST = L_A0;
constexpr u32 L_IMG0 = L_A0 + 10240, L_IMG1 = L_A0 + 30720;
constexpr u32 UCOPY = 4640, UPAD = 384, KOFF = 4 * UCOPY, KCOPY = 4224, KPAD = 112;
constexpr u32 KC_OFF = 16640;
constexpr u32 TROW = 288;

constexpr int SPIN_CAP = 1 << 16;

template <bool B> struct BoolC { static constexpr bool value = B; };

struct Params {
  const u16 *x, *Lre, *Lim, *Bre, *Bim, *Cre, *Cim, *ldtA, *DA;
  const u16 *Are, *Aim, *sBre, *sBim, *sCre, *sCim, *ldtB, *DB, *gw;
  float* out;
  char* ws;
  u32 seq[16];
};

DEV bool wave_all(bool p) { return __builtin_amdgcn_ballot_w64(!p) == 0; }
DEV float bf2f(u32 v) { return __builtin_bit_cast(float, v << 16); }
DEV float bflo(u32 v) { return __builtin_bit_cast(float, v << 16); }
DEV float bfhi(u32 v) { return __builtin_bit_cast(float, v & 0xffff0000u); }
DEV u32 pk(float a, float b) { bf16x2_t v = {(__bf16)a, (__bf16)b}; return __builtin_bit_cast(u32, v); }
DEV float rbf(float a) { return bflo(pk(a, a)); }
DEV u32 f2u(float a) { return __builtin_bit_cast(u32, a); }
DEV float u2f(u32 a) { return __builtin_bit_cast(float, a); }
DEV float swp(float x) { return u2f(__builtin_amdgcn_update_dpp(0, f2u(x), 0xB1, 0xf, 0xf, true)); }
DEV f32x2 rot(f32x2 z) { return f32x2{-z.y, z.x}; }
DEV f32x2 cmul(f32x2 c, f32x2 z, f32x2 zr) { return __builtin_elementwise_fma(f32x2{c.y, c.y}, zr, f32x2{c.x, c.x} * z); }
DEV f32x2 cmul(f32x2 c, f32x2 z) { return cmul(c, z, rot(z)); }
DEV f32x2 cpowk(float a, float th, float k) {
  const float L2E = 1.44269504088896f, IH = 0.15915494f, IL = 6.4206382e-9f;
  float m = __builtin_amdgcn_exp2f(k * a * L2E);
  float x = k * th;
  float hi = x * IH;
  float lo = __builtin_fmaf(x, IL, __builtin_fmaf(x, IH, -hi));
  float r = __builtin_amdgcn_fractf(hi) + lo;
  return f32x2{m * __builtin_amdgcn_cosf(r), m * __builtin_amdgcn_sinf(r)};
}
template <typename T> DEV T lds_ld(u32 a) { typedef T __attribute__((may_alias)) TA; return *(const LAS TA*)(size_t)a; }
template <typename T> DEV void lds_st(u32 a, T v) { typedef T __attribute__((may_alias)) TA; *(LAS TA*)(size_t)a = v; }
DEV s16x4 lds_tr(u32 a) { return __builtin_amdgcn_ds_read_tr16_b64_v4i16((LAS s16x4*)(size_t)a); }
DEV f32x4 mfma(u32x4 a, u32x4 b, f32x4 c) {
  return __builtin_amdgcn_mfma_f32_16x16x32_bf16(__builtin_bit_cast(bf16x8, a), __builtin_bit_cast(bf16x8, b), c, 0, 0, 0);
}
DEV rsrc_t mksrd(const void* p, u32 n) { return __builtin_amdgcn_make_buffer_rsrc((void*)p, (short)0, (int)n, 0x00020000); }
DEV void dma16(rsrc_t rs, u32 voff, u32 soff, u32 lds) {
  asm volatile("s_mov_b32 m0, %0\n\ts_nop 0\n\tbuffer_load_dwordx4 %1, %2, %3 offen lds"
               :: "s"(lds), "v"(voff), "s"(rs), "s"(soff) : "m0", "memory");
}
DEV void dma64v(rsrc_t rs, u32 v0, u32 v1, u32 v2, u32 v3, u32 so, u32 lds) {
  asm volatile("s_mov_b32 m0, %0\n\ts_nop 0\n\tbuffer_load_dwordx4 %1, %5, %6 offen lds\n\t"
               "buffer_load_dwordx4 %2, %5, %6 offen offset:1024 lds\n\tbuffer_load_dwordx4 %3, %5, %6 offen offset:2048 lds\n\t"
               "buffer_load_dwordx4 %4, %5, %6 offen offset:3072 lds"
               :: "s"(lds), "v"(v0), "v"(v1), "v"(v2), "v"(v3), "s"(rs), "s"(so) : "m0", "memory");
}
DEV void dma32v(rsrc_t rs, u32 v0, u32 v1, u32 so, u32 lds) {
  asm volatile("s_mov_b32 m0, %0\n\ts_nop 0\n\tbuffer_load_dwordx4 %1, %3, %4 offen lds\n\t"
               "buffer_load_dwordx4 %2, %3, %4 offen offset:1024 lds"
               :: "s"(lds), "v"(v0), "v"(v1), "s"(rs), "s"(so) : "m0", "memory");
}
template <int OFF> DEV void dmaq(rsrc_t r0, rsrc_t r1, u32 v0, u32 v1, u32 v2, u32 v3, u32 so, u32 slot) {
  asm volatile("s_add_u32 m0, %0, %8\n\ts_nop 0\n\tbuffer_load_dwordx4 %1, %5, %7 offen lds\n\t"
               "buffer_load_dwordx4 %2, %6, %7 offen offset:1024 lds\n\tbuffer_load_dwordx4 %3, %6, %7 offen offset:2048 lds\n\t"
               "buffer_load_dwordx4 %4, %6, %7 offen offset:3072 lds"
               :: "s"(slot), "v"(v0), "v"(v1), "v"(v2), "v"(v3), "s"(r0), "s"(r1), "s"(so), "n"(OFF) : "m0", "scc", "memory");
}
template <int OFF> DEV void dmad(rsrc_t r0, u32 v0, u32 v1, u32 so, u32 slot) {
  asm volatile("s_add_u32 m0, %0, %5\n\ts_nop 0\n\tbuffer_load_dwordx4 %1, %3, %4 offen lds\n\t"
               "buffer_load_dwordx4 %2, %3, %4 offen offset:1024 lds"
               :: "s"(slot), "v"(v0), "v"(v1), "s"(r0), "s"(so), "n"(OFF) : "m0", "scc", "memory");
}
DEV void dma_mark(rsrc_t rs, u32 so, u32 lds) {
  u64 keep;
  asm volatile("s_mov_b32 m0, %1\n\ts_mov_b64 %0, exec\n\ts_mov_b64 exec, 1\n\tbuffer_load_dword off, %2, %3 lds\n\ts_mov_b64 exec, %0"
               : "=&s"(keep) : "s"(lds), "s"(rs), "s"(so) : "m0", "memory");
}
DEV u32 opaque(u32 v) { asm volatile("" : "+v"(v)); return v; }
#define SB() __builtin_amdgcn_sched_barrier(0)
#define RARE(x) __builtin_expect(!!(x), 0)
template <int N> DEV void waitv() { asm volatile("s_waitcnt vmcnt(%0)" :: "n"(N) : "memory"); }
DEV void waitl() { asm volatile("s_waitcnt lgkmcnt(0)" ::: "memory"); }
DEV void hwbar() { waitl(); __builtin_amdgcn_s_barrier(); asm volatile("" ::: "memory"); }
DEV void post(u32 a, u32 v) {
  asm volatile("" ::: "memory");
  lds_st<u32>(a, v);
  asm volatile("" ::: "memory");
}
DEV u32 min4(u32x4 v) { return (u32)__builtin_amdgcn_readfirstlane(min(min(v.x, v.y), min(v.z, v.w))); }
DEV u32x4 peek4(u32 a) { asm volatile("" ::: "memory"); return lds_ld<u32x4>(a); }
DEV void sw_wait4(u32 a, u32 target) {
#pragma unroll 1
  for (u32 it = 0; it < (1u << 22) && min4(peek4(a)) < target; ++it) {}
  asm volatile("" ::: "memory");
}
DEV void sw_wait(u32 a, u32 target) {
  u32 v, sv, cnt = 1u << 22;
  asm volatile("1:\n\tds_read_b32 %0, %4\n\ts_sub_u32 %2, %2, 1\n\ts_cselect_b32 %3, 0, %3\n\ts_waitcnt lgkmcnt(0)\n\t"
               "v_readfirstlane_b32 %1, %0\n\ts_cmp_lt_u32 %1, %3\n\ts_cbranch_scc1 1b"
               : "=&v"(v), "=&s"(sv), "+s"(cnt), "+s"(target) : "v"(a) : "scc", "memory");
}
DEV u32 peek(u32 a) { return __hip_atomic_load((LAS u32*)(size_t)a, __ATOMIC_RELAXED, __HIP_MEMORY_SCOPE_WORKGROUP); }
DEV u32x4 ld16(rsrc_t rs, u32 off) { return __builtin_amdgcn_raw_buffer_load_b128(rs, off, 0, 16); }
DEV u32x2 ld8(rsrc_t rs, u32 off) { return __builtin_amdgcn_raw_buffer_load_b64(rs, off, 0, 16); }
DEV void st16(rsrc_t rs, u32 off, u32x4 v) { __builtin_amdgcn_raw_buffer_store_b128(v, rs, off, 0, 16); }
DEV void st8(rsrc_t rs, u32 off, u32x2 v) { __builtin_amdgcn_raw_buffer_store_b64(v, rs, off, 0, 16); }
constexpr u32 OOB = 0x7ff00000u;
constexpr u32 FMIX = 0x9E3779B9u;
DEV bool flag_ok(u32x2 v, u32 off, u32 tag) { return wave_all((off == OOB) | ((v.x == tag) & (v.y == (tag ^ FMIX)))); }
DEV void poll_flags(rsrc_t rs, u32 off, u32 tag) {
#pragma unroll 1
  for (int it = 0; it < SPIN_CAP; ++it) {
    asm volatile("" ::: "memory");
    if (flag_ok(ld8(rs, off), off, tag)) break;
    __builtin_amdgcn_s_sleep(1);
  }
  asm volatile("" ::: "memory");
}

#define TR(k) do {} while (0)
#define DUMP() do {} while (0)
#define STAMP(k) do {} while (0)
#define CSTAMP(k) do {} while (0)

#define SCAN_STEP(o, v, h)                                                                                             \
  asm("v_fma_f32 %0, %15, %10, %5\n\tv_fma_f32 %1, %16, %11, %6\n\tv_fma_f32 %2, %17, %12, %7\n\t"                   \
      "v_fma_f32 %3, %18, %13, %8\n\tv_fma_f32 %4, %19, %14, %9\n\t"                                                   \
      "v_fmac_f32_dpp %0, %10, %20 quad_perm:[1,0,3,2] row_mask:0xf bank_mask:0xf\n\t"                                 \
      "v_fmac_f32_dpp %1, %11, %21 quad_perm:[1,0,3,2] row_mask:0xf bank_mask:0xf\n\t"                                 \
      "v_fmac_f32_dpp %2, %12, %22 quad_perm:[1,0,3,2] row_mask:0xf bank_mask:0xf\n\t"                                 \
      "v_fmac_f32_dpp %3, %13, %23 quad_perm:[1,0,3,2] row_mask:0xf bank_mask:0xf\n\t"                                 \
      "v_fmac_f32_dpp %4, %14, %24 quad_perm:[1,0,3,2] row_mask:0xf bank_mask:0xf"                                     \
      : "=&v"(o[0]), "=&v"(o[1]), "=&v"(o[2]), "=&v"(o[3]), "=&v"(o[4])                                                \
      : "v"(v[0]), "v"(v[1]), "v"(v[2]), "v"(v[3]), "v"(v[4]), "v"(h[0]), "v"(h[1]), "v"(h[2]), "v"(h[3]), "v"(h[4]), \
        "v"(ma[0]), "v"(ma[1]), "v"(ma[2]), "v"(ma[3]), "v"(ma[4]), "v"(mb[0]), "v"(mb[1]), "v"(mb[2]), "v"(mb[3]),   \
        "v"(mb[4]))
DEV void scan_step(float (&o)[5], const float (&v)[5], const float (&h)[5], const float (&ma)[5], const float (&mb)[5]) {
  float t[5];
  SCAN_STEP(t, v, h);
#pragma unroll
  for (int i = 0; i < 5; ++i) o[i] = t[i];
}

struct Frag { u32x4 a0, a1, b0, b1, b2, b3, b4; };
#define MF(d, a, b) "v_mfma_f32_16x16x32_bf16 %" #d ", %" #a ", %" #b ", %" #d "\n\t"
#define RA(d, o) "ds_read_b128 %" #d ", %24 offset:" #o "\n\t"
#define RB(d, o) "ds_read_b128 %" #d ", %25 offset:" #o "\n\t"
#define WL(n) "s_waitcnt lgkmcnt(" #n ")\n\t"
#define G1_BODY(w1, w2, w3, w5, w7, w9)                                                                    \
  w1 MF(0, 17, 19) RA(10, 0) w2 MF(5, 18, 19) RB(12, 0) w3 MF(1, 17, 20) RA(11, 2048) MF(6, 18, 20) RB(13, 1024) \
  w5 MF(2, 17, 21) RB(14, 2048) MF(7, 18, 21) RB(15, 3072) w7 MF(3, 17, 22) RB(16, 4096) MF(8, 18, 22)    \
  w9 MF(4, 17, 23) MF(9, 18, 23)
#define G1_ACC(A) "+v"(A[0][0]), "+v"(A[0][1]), "+v"(A[0][2]), "+v"(A[0][3]), "+v"(A[0][4]), "+v"(A[1][0]), "+v"(A[1][1]), "+v"(A[1][2]), "+v"(A[1][3]), "+v"(A[1][4])
#define G1_OUT(N) "=&v"(N.a0), "=&v"(N.a1), "=&v"(N.b0), "=&v"(N.b1), "=&v"(N.b2), "=&v"(N.b3), "=&v"(N.b4)
#define G1_IN(C) "v"(C.a0), "v"(C.a1), "v"(C.b0), "v"(C.b1), "v"(C.b2), "v"(C.b3), "v"(C.b4)
template <bool WAITS> DEV void g1_half(f32x4 (&A)[2][5], Frag& N, const Frag& C, u32 pa, u32 pb) {
  if constexpr (WAITS)
    asm volatile(G1_BODY(WL(5), WL(5), WL(5), WL(6), WL(7), WL(7)) : G1_ACC(A), G1_OUT(N) : G1_IN(C), "v"(pa), "v"(pb) : "memory");
  else
    asm volatile(G1_BODY("", "", "", "", "", "") : G1_ACC(A), G1_OUT(N) : G1_IN(C), "v"(pa), "v"(pb) : "memory");
}
DEV u32 g1_half_peek(f32x4 (&A)[2][5], Frag& N, const Frag& C, u32 pa, u32 pb, u32 pf) {
  u32 t = pf;
  asm volatile("ds_read_b32 %0, %0" : "+v"(t) :: "memory");
  asm volatile(G1_BODY(WL(6), WL(6), WL(6), WL(7), WL(8), WL(8)) WL(7) : G1_ACC(A), G1_OUT(N) : G1_IN(C), "v"(pa), "v"(pb) : "memory");
  u32 r;
  asm volatile("v_readfirstlane_b32 %0, %1" : "=s"(r) : "v"(t) : "memory");
  return r;
}
DEV void g1_last(f32x4 (&A)[2][5], const Frag& C) {
  asm volatile(WL(5) MF(0, 10, 12) WL(4) MF(5, 11, 12) WL(3) MF(1, 10, 13) MF(6, 11, 13) WL(2) MF(2, 10, 14) MF(7, 11, 14)
               WL(1) MF(3, 10, 15) MF(8, 11, 15) WL(0) MF(4, 10, 16) MF(9, 11, 16) "s_nop 7"
               : G1_ACC(A) : G1_IN(C) : "memory");
}

struct CF { u32x4 f[5]; };
DEV u32 peek_issue(u32 a) { asm volatile("ds_read_b32 %0, %0" : "+v"(a) :: "memory"); return a; }
DEV u32 peek_get(u32 t) { u32 r; asm volatile("s_waitcnt lgkmcnt(5)\n\tv_readfirstlane_b32 %0, %1" : "=s"(r) : "v"(t) : "memory"); return r; }
#define M2(d, a) "v_mfma_f32_16x16x32_bf16 %" #d ", %" #a ", %15, %" #d "\n\t"
#define R2(d, o) "ds_read_b128 %" #d ", %16 offset:%17+" #o "\n\t"
template <int OFF, int WT> DEV void g2_half(f32x4 (&y)[5], CF& N, const CF& Cc, u32x4 h, u32 pa) {
  asm volatile("s_waitcnt lgkmcnt(%18)\n\t" M2(0, 10) R2(5, 0) "s_waitcnt lgkmcnt(%18)\n\t" M2(1, 11) R2(6, 2048) "s_waitcnt lgkmcnt(%18)\n\t" M2(2, 12) R2(7, 4096)
               "s_waitcnt lgkmcnt(%18)\n\t" M2(3, 13) R2(8, 6144) "s_waitcnt lgkmcnt(%18)\n\t" M2(4, 14) R2(9, 8192)
               : "+v"(y[0]), "+v"(y[1]), "+v"(y[2]), "+v"(y[3]), "+v"(y[4]), "=&v"(N.f[0]), "=&v"(N.f[1]), "=&v"(N.f[2]), "=&v"(N.f[3]), "=&v"(N.f[4])
               : "v"(Cc.f[0]), "v"(Cc.f[1]), "v"(Cc.f[2]), "v"(Cc.f[3]), "v"(Cc.f[4]), "v"(h), "v"(pa), "n"(OFF), "n"(WT) : "memory");
}
#define M3(d, a) "v_mfma_f32_16x16x32_bf16 %" #d ", %" #a ", %10, %" #d "\n\t"
template <bool LAST> DEV void g2_half0(f32x4 (&y)[5], const CF& Cc, u32x4 h) {
  if constexpr (LAST)
    asm volatile(WL(4) M3(0, 5) WL(3) M3(1, 6) WL(2) M3(2, 7) WL(1) M3(3, 8) WL(0) M3(4, 9) "s_nop 7"
                 : "+v"(y[0]), "+v"(y[1]), "+v"(y[2]), "+v"(y[3]), "+v"(y[4])
                 : "v"(Cc.f[0]), "v"(Cc.f[1]), "v"(Cc.f[2]), "v"(Cc.f[3]), "v"(Cc.f[4]), "v"(h) : "memory");
  else
    asm volatile(M3(0, 5) M3(1, 6) M3(2, 7) M3(3, 8) M3(4, 9)
                 : "+v"(y[0]), "+v"(y[1]), "+v"(y[2]), "+v"(y[3]), "+v"(y[4])
                 : "v"(Cc.f[0]), "v"(Cc.f[1]), "v"(Cc.f[2]), "v"(Cc.f[3]), "v"(Cc.f[4]), "v"(h) : "memory");
}
template <int A, int B> DEV bool h_bad(const u32x4 (&h)[20]) {
  u32 m = 0;
#pragma unroll
  for (int k = A; k < B; ++k) m = max(max(max(h[k].x, h[k].y), h[k].z), max(h[k].w, m));
  return __builtin_amdgcn_ballot_w64(m == 0xffffffffu) != 0;
}
template <int A, int B> DEV void h_check(u32x4 (&h)[20], rsrc_t rs, u32 ho) {
  if (RARE((h_bad<A, B>(h)))) {
#pragma unroll 1
    for (int it = 0; it < SPIN_CAP; ++it) {
      __builtin_amdgcn_s_sleep(2);
      asm volatile("" ::: "memory");
#pragma unroll
      for (int k = A; k < B; ++k) h[k] = ld16(rs, ho + k * 1024);
      if (!h_bad<A, B>(h)) break;
    }
  }
}

NOPK __global__ void __launch_bounds__(512, 2) fused_kernel(Params) {
  __shared__ __attribute__((aligned(16))) char smem[163840];
  asm volatile("" :: "v"((u32)(uintptr_t)(LAS char*)smem));
  constexpr u32 L0 = 0;
  const u64 kap = (u64)__builtin_amdgcn_kernarg_segment_ptr();
  u64x8 k0, k1; u64x4 k2;
  asm volatile("s_load_dwordx16 %0, %3, 0x0\n\ts_load_dwordx16 %1, %3, 0x40\n\ts_load_dwordx8 %2, %3, 0x80"
               : "=&s"(k0), "=&s"(k1), "=&s"(k2) : "s"(kap));
  lds_st<u32>(L0 + L_CNT + (threadIdx.x & 31) * 4, 0u);
  asm volatile("s_waitcnt lgkmcnt(0)\n\ts_barrier" : "+s"(k0), "+s"(k1), "+s"(k2) :: "memory");
  struct { const u16 *x, *Lre, *Lim, *Bre, *Bim, *Cre, *Cim, *ldtA, *DA, *Are, *Aim, *sBre, *sBim, *sCre, *sCim, *ldtB, *DB, *gw; float* out; char* ws; }
  const P = {(const u16*)k0[0], (const u16*)k0[1], (const u16*)k0[2], (const u16*)k0[3], (const u16*)k0[4], (const u16*)k0[5], (const u16*)k0[6],
             (const u16*)k0[7], (const u16*)k1[0], (const u16*)k1[1], (const u16*)k1[2], (const u16*)k1[3], (const u16*)k1[4], (const u16*)k1[5],
             (const u16*)k1[6], (const u16*)k1[7], (const u16*)k2[0], (const u16*)k2[1], (float*)k2[2], (char*)k2[3]};
  const int tid = threadIdx.x, lane = tid & 63;
  const int w = __builtin_amdgcn_readfirstlane(tid >> 6);
  const int b = blockIdx.x, xcd = b & 7, r = b >> 3;
  const int c = 4 * (xcd >> 1) + (r >> 3), j = 8 * (xcd & 1) + (r & 7);
  const int ti = 4 * xcd + (r >> 3), dj = r & 7;
  const int i16 = lane & 15, g = lane >> 4;
  const rsrc_t rws = mksrd(P.ws, WS_END);
  const rsrc_t rx = mksrd(P.x, 2048 * 1280);
  const rsrc_t rCre = mksrd(P.Cre, 640 * 1280), rCim = mksrd(P.Cim, 640 * 1280);
  const rsrc_t rka = mksrd((const void*)kap, sizeof(Params));
  constexpr u32 SEQ = __builtin_offsetof(Params, seq);
  const int kq = w & 3, kh = w >> 2;
  const int rot10 = ((r & 7) + 2 * (r >> 3) + 3 * xcd) % 10;
  u32 tag;
  STAMP(0);
  TR(0);
  const u32 cf0 = i16 * 128 + ((g ^ ((i16 >> 1) & 7)) << 4);
  const u32 ccf = L0 + C_CFULL;
  const u32 fho = lane < 8 ? WS_FH + (u32)((ti >> 1) * 16 + 8 * kh + lane) * 128 : OOB;
  const u32 ho = WS_H + (u32)((4 * ti + kq) * 40 + 20 * kh) * 1024 + lane * 16;
  const u32 ima = L0 + (kh ? L_IMG1 : L_IMG0) + (16 * kq + i16) * 320 + (g ^ ((i16 >> 1) & 3)) * 16;
#define G2_REGS(name, n, at)                                                                                \
  CF name[2 * n];                                                                                           \
  { const u32 a0 = opaque(L0 + (at) + cf0), a1 = opaque(L0 + (at) + (cf0 ^ 64));                            \
    _Pragma("unroll") for (int k = 0; k < 2 * n; ++k)                                                       \
    _Pragma("unroll") for (int nt = 0; nt < 5; ++nt) name[k].f[nt] = lds_ld<u32x4>(((k & 1) ? a1 : a0) + (k >> 1) * 10240 + nt * 2048); \
    _Pragma("unroll") for (int k = 0; k < 2 * n; ++k)                                                       \
      asm volatile("s_waitcnt lgkmcnt(0)" : "+v"(name[k].f[0]), "+v"(name[k].f[1]), "+v"(name[k].f[2]), "+v"(name[k].f[3]), "+v"(name[k].f[4]) :: "memory"); }
#define G2_LOADH()                                                                                          \
  u32x4 hf_[20];                                                                                            \
  _Pragma("unroll") for (int ks = 0; ks < 20; ++ks) hf_[ks] = ld16(rws, ho + ks * 1024);                    \
  SB();
#define G2_INIT(at)                                                                                         \
  f32x4 y[5];                                                                                               \
  _Pragma("unroll") for (int nt = 0; nt < 5; ++nt) y[nt] = f32x4{0.f, 0.f, 0.f, 0.f};                       \
  CF P0, P1;                                                                                                \
  const u32 cab[2][2] = {{opaque(L0 + (at) + cf0), opaque(L0 + (at) + (cf0 ^ 64))},                         \
                         {opaque(L0 + (at) + 40960 + cf0), opaque(L0 + (at) + 40960 + (cf0 ^ 64))}};        \
  asm volatile("s_nop 1" : "+v"(y[0]), "+v"(y[1]), "+v"(y[2]), "+v"(y[3]), "+v"(y[4]));
#define G2_U(uu, slot, need)                                                                                \
  { const u32 t_ = peek_issue(ccf);                                                                         \
    g2_half<((slot) & 3) * 10240, 5>(y, P1, P0, hf_[2 * (uu)], cab[(slot) >> 2][1]);                        \
    if (RARE(peek_get(t_) < (need))) sw_wait(ccf, need);                                                    \
    g2_half<(((slot) + 1) & 3) * 10240, 4>(y, P0, P1, hf_[2 * (uu) + 1], cab[((slot) + 1) >> 2][0]); }
#define G2_UN(uu, slot)                                                                                     \
  g2_half<((slot) & 3) * 10240, 4>(y, P1, P0, hf_[2 * (uu)], cab[(slot) >> 2][1]);                          \
  g2_half<(((slot) + 1) & 3) * 10240, 4>(y, P0, P1, hf_[2 * (uu) + 1], cab[((slot) + 1) >> 2][0]);
#define G2_ULAST(uu, slot)                                                                                  \
  g2_half<((slot) & 3) * 10240, 4>(y, P1, P0, hf_[2 * (uu)], cab[(slot) >> 2][1]);                          \
  g2_half0<true>(y, P1, hf_[2 * (uu) + 1]);
#define G2_B_IMAGE()                                                                                        \
  { STAMP(11);                                                                                              \
    sw_wait4(L0 + C_BAR, 4);                                                                                \
    const u32 ia = opaque(ima);                                                                             \
    _Pragma("unroll") for (int nt = 0; nt < 5; ++nt) lds_st<f32x4>(ia + nt * 64, y[nt]); }
  auto get_tag = [&]() INL { sw_wait(L0 + C_FULL, 1); tag = __builtin_amdgcn_readfirstlane(lds_ld<u32>(opaque(L0 + L_TAG))) + 1u; };

  const int pl = lane < 40 ? lane : 39;
  u32 pLre, pLim, pLdt; u32x4 gw0, gw1;
  if (w < 4) {
#pragma unroll
    for (int k = 0; k < 3; ++k) {
      const int qq = 64 * k + lane, t = (qq * 6554) >> 16, cc = qq - 10 * t;
      if (k < 2 || lane < 32) dma16(rx, (u32)(64 * ti + 16 * w + t) * 1280 + dj * 160 + cc * 16, 0, L0 + L_RAW + w * 2560 + k * 1024);
    }
    dma_mark(rka, SEQ, L0 + C_STRIP + 4 * w);
    pLre = ((const GAS u16*)P.Lre)[40 * j + pl]; pLim = ((const GAS u16*)P.Lim)[40 * j + pl]; pLdt = ((const GAS u16*)P.ldtA)[40 * j + pl];
    gw0 = *(const GAS u32x4*)((const GAS char*)P.gw + rot10 * 128 + g * 16); gw1 = *(const GAS u32x4*)((const GAS char*)P.gw + rot10 * 128 + 64 + g * 16);
    asm volatile("" ::: "memory");
  }
  if (__builtin_expect(w == 7, 1)) {
      __builtin_amdgcn_s_setprio(1);
      const rsrc_t rBre = mksrd(P.Bre, 640 * 1280), rBim = mksrd((const char*)P.Bim - 1024, 640 * 1280 + 1024);
      const int q8 = lane >> 3;
      u32 vx[4], vb[10];
      {
        const u32 x0 = (u32)(128 * c + 8 * (q8 >> 2) + (q8 & 3)) * 1280 + (((lane & 7) ^ (q8 >> 1)) << 4);
        vx[0] = x0; vx[1] = (x0 ^ 64) + 16 * 1280 - 1024; vx[2] = x0 + 4 * 1280 - 2048; vx[3] = (x0 ^ 64) + 20 * 1280 - 3072;
        const u32 b0 = (u32)(40 * j + q8) * 1280 + (((lane & 7) ^ (q8 & 6)) << 4), b1 = (b0 ^ 16) + 1024;
#pragma unroll
        for (int k = 0; k < 10; ++k) vb[k] = (k < 5 ? b0 : b1) + 10240 * (k % 5) - 1024 * (k & 3);
      }
      u32 so = rot10 * 128;
      {
        u64 keep;
        asm volatile("s_mov_b32 m0, %1\n\ts_mov_b64 %0, exec\n\ts_mov_b64 exec, 1\n\tbuffer_load_dword off, %2, 0 sc1 lds\n\ts_mov_b64 exec, %0"
                     : "=&s"(keep) : "s"(L0 + L_TAG), "s"(rws) : "m0", "memory");
      }
      auto head = [&](u32 sl) INL {
        dmaq<0>(rx, rx, vx[0], vx[1], vx[2], vx[3], so, sl);
        dmaq<4096>(rx, rx, vx[0], vx[1], vx[2], vx[3], so + 40960, sl);
        dmaq<8192>(rx, rx, vx[0], vx[1], vx[2], vx[3], so + 81920, sl);
        dmaq<12288>(rx, rx, vx[0], vx[1], vx[2], vx[3], so + 122880, sl);
        dmaq<16384>(rBre, rBre, vb[0], vb[1], vb[2], vb[3], so, sl);
      };
      auto tail = [&](u32 sl) INL {
        dmaq<20480>(rBre, rBim, vb[4], vb[5], vb[6], vb[7], so, sl);
        dmad<24576>(rBim, vb[8], vb[9], so, sl);
        so = so == 1152 ? 0 : so + 128;
      };
#pragma unroll
      for (int s = 0; s < 10; ++s) {
        const u32 sb = L0 + ring(s);
        if (s == 5) sw_wait4(L0 + C_PRO, 1);
        if (s == 6 || s == 8) sw_wait4(L0 + C_FREE, s - 4);
        TR(4 + 4 * s);
        if (s >= 2) waitv<36>();
        TR(5 + 4 * s);
        head(sb);
        tail(sb);
        TR(6 + 4 * s);
        dma_mark(rka, SEQ + 4 * s, L0 + C_FULL);
      }
      STAMP(2);
      u32 vc[4];
#pragma unroll
      for (int k = 0; k < 4; ++k) vc[k] = (u32)(80 * dj + q8) * 1280 + (((lane & 7) ^ (lane >> 4) ^ (4 * (k & 1))) << 4) + 9216 * k;
      auto stageC = [&](int u, u32 dst) INL {
        const rsrc_t rs = (u & 1) ? rCim : rCre;
        const u32 so = (u32)(u >> 1) * 128;
        dma64v(rs, vc[0], vc[1], vc[2], vc[3], so, dst);
        dma64v(rs, vc[0], vc[1], vc[2], vc[3], so + 40960, dst + 4096);
        dma32v(rs, vc[0], vc[1], so + 81920, dst + 8192);
      };
      {
        u32 t0 = 0;
#pragma unroll
        for (int k = 0; k < 7; ++k) {
          const u32 ln = min(r * 400 + k * 64 + lane, r * 400 + 399);
          asm volatile("buffer_load_dword %0, %1, %2, 0 offen" : "+v"(t0) : "v"((ln >= 6400 ? ln - 6400 : ln) * 128), "s"(r >= 16 ? rCim : rCre) : "memory");
        }
        const u32 hb = WS_H + (u32)(8 * c + (lane >> 5)) * 40960 + (g & 1) * 2048 + i16 * 16;
#pragma unroll
        for (int nt = 0; nt < 5; ++nt) {
          const int o = 5 * j + nt;
#pragma unroll
          for (int k = 0; k < 4; ++k)
            st16(rws, hb + k * 81920 + (u32)((o >> 3) * 4096 + ((o >> 2) & 1) * 1024 + (o & 3) * 256), u32x4{~0u, ~0u, ~0u, ~0u});
        }
        get_tag();
        asm volatile("s_waitcnt vmcnt(0)" : "+v"(t0) :: "memory");
        st8(rws, lane == 0 ? WS_FH + (u32)(c * 16 + j) * 128 : OOB, u32x2{tag, tag ^ FMIX});
        post(L0 + C_CLR, 1u);
        post(L0 + C_BPREP + 12, 1u);
      }
      poll_flags(rws, fho, tag);
      sw_wait4(L0 + C_BPREP, 1);
      sw_wait4(L0 + C_BAR, 2);
      stageC(10, L0 + L_A0 + 5 * 10240); stageC(11, L0 + L_A0 + 6 * 10240); stageC(12, L0 + L_A0 + 7 * 10240);
      stageC(0, L0 + L_A0 + 3 * 10240); stageC(1, L0 + L_A0 + 4 * 10240);
      dma_mark(rka, SEQ, ccf);
      stageC(4, L0 + L_A0 + 2 * 10240);
      sw_wait(ccf, 1);
      G2_REGS(cr, 3, L_A0 + 5 * 10240);
      post(L0 + C_BDONE + 12, 1u);
      sw_wait4(L0 + C_BAR, 3);
      stageC(2, L0 + L_A0); stageC(3, L0 + L_A0 + 10240);
      dma_mark(rka, SEQ + 4, ccf);
      STAMP(3);
      sw_wait4(L0 + C_C0, 1);
      waitv<30>();
      stageC(5, L0 + L_A0 + 3 * 10240); stageC(6, L0 + L_A0 + 4 * 10240);
      dma_mark(rka, SEQ + 8, ccf);
      waitv<21>();
      STAMP(8);
      u32x4 hf_[20];
#pragma unroll
      for (int ks = 0; ks < 20; ++ks)
        asm volatile("buffer_load_dwordx4 %0, %1, %2, %3 offen offset:%4 sc1" : "=v"(hf_[ks]) : "v"(ho), "s"(rws), "s"((ks >> 2) * 4096), "n"((ks & 3) * 1024) : "memory");
      G2_INIT(L_B0);
      sw_wait4(L0 + C_BDONE, 1);
      STAMP(4);
#define FILL(q, unit, at) stageC(unit, L0 + (at)); dma_mark(rka, SEQ + 4 * (3 + (q)), ccf);
      FILL(0, 13, L_B0); FILL(1, 14, L_B0 + 10240); waitv<50>(); FILL(2, 7, L_A0 + 5 * 10240);
      asm volatile("s_waitcnt vmcnt(33)" : "+v"(hf_[0]), "+v"(hf_[1]), "+v"(hf_[2]), "+v"(hf_[3]), "+v"(hf_[4]), "+v"(hf_[5]), "+v"(hf_[6]), "+v"(hf_[7]), "+v"(hf_[8]), "+v"(hf_[9]) :: "memory");
      asm volatile("" : "+v"(hf_[10]), "+v"(hf_[11]), "+v"(hf_[12]), "+v"(hf_[13]), "+v"(hf_[14]), "+v"(hf_[15]), "+v"(hf_[16]), "+v"(hf_[17]), "+v"(hf_[18]), "+v"(hf_[19]));
      FILL(3, 15, L_B0 + 2 * 10240);
      h_check<0, 20>(hf_, rws, ho);
      FILL(4, 8, L_A0 + 6 * 10240);
      g2_half0<false>(y, cr[0], hf_[0]); g2_half0<false>(y, cr[1], hf_[1]);
      waitv<40>(); FILL(5, 16, L_B0 + 3 * 10240);
      g2_half0<false>(y, cr[2], hf_[2]); g2_half0<false>(y, cr[3], hf_[3]);
      waitv<40>(); FILL(6, 9, L_A0 + 7 * 10240);
      g2_half0<false>(y, cr[4], hf_[4]); g2_half<0, 4>(y, P0, cr[5], hf_[5], cab[0][0]);
      waitv<40>(); FILL(7, 17, L_B0 + 4 * 10240);
      G2_UN(3, 0);
      waitv<40>(); FILL(8, 18, L_B0 + 5 * 10240);
      G2_UN(4, 1);
      waitv<40>(); FILL(9, 19, L_B0 + 6 * 10240);
      STAMP(5);
      __builtin_amdgcn_s_setprio(0);
      G2_U(5, 2, 9); G2_U(6, 3, 11); G2_U(7, 4, 12); G2_U(8, 5, 13); G2_ULAST(9, 6);
      G2_B_IMAGE();
  } else if (__builtin_expect(w < 4, 1)) {
    __builtin_amdgcn_s_setprio(2);
    f32x4 acc[2][5], accg[2] = {f32x4{0.f, 0.f, 0.f, 0.f}, f32x4{0.f, 0.f, 0.f, 0.f}};
#pragma unroll
    for (int m = 0; m < 2; ++m)
#pragma unroll
      for (int nt = 0; nt < 5; ++nt) acc[m][nt] = f32x4{0.f, 0.f, 0.f, 0.f};
    asm volatile("" : G1_ACC(acc));
    sw_wait4(L0 + C_STRIP, 1);
    STAMP(14);
    u32x2 xq[5];
    {
      const u32 ra = opaque(L0 + L_RAW + (32 * (g & 1) + 8 * (lane & 3) + ((lane >> 2) & 3)) * 160 + 40 * w + 8 * (g >> 1));
      u32x2 lo[3], hi[3];
#pragma unroll
      for (int k = 0; k < 3; ++k) { lo[k] = __builtin_bit_cast(u32x2, lds_tr(ra + k * 16)); hi[k] = __builtin_bit_cast(u32x2, lds_tr(ra + k * 16 + 640)); }
      const u32 xr = opaque(L0 + L_RAW + (16 * w + i16) * 160 + 8 * g);
#pragma unroll
      for (int nt = 0; nt < 5; ++nt) xq[nt] = lds_ld<u32x2>(xr + 32 * nt);
      const u32 xo = WS_XT + (u32)(20 * dj + 5 * w + (g >> 1)) * 16384 + (8 * ti + 4 * (g & 1)) * 64 + i16 * 16;
      waitl();
      dma_mark(rka, SEQ, L0 + C_PRO + 4 * w);
#pragma unroll
      for (int k = 0; k < 3; ++k) st16(rws, (k < 2 || lane < 32) ? xo + k * 32768 : OOB, u32x4{lo[k].x, lo[k].y, hi[k].x, hi[k].y});
    }
    STAMP(15);
    asm volatile("" : "+v"(pLre), "+v"(pLim), "+v"(pLdt));
    const float lre = bf2f(pLre), lim = bf2f(pLim);
    const float dtA = rbf(expf(bf2f(pLdt)));
    const float aA = dtA * lre, tA = dtA * lim;
    const f32x2 lam = cpowk(aA, tA, 1.f), l8 = cpowk(aA, tA, 8.f);
    f32x2 c2;
    {
      const float nr = lam.x - 1.f, ni = lam.y, den = 2.f * __builtin_amdgcn_rcpf(lre * lre + lim * lim);
      c2 = f32x2{(nr * lre + ni * lim) * den, (ni * lre - nr * lim) * den};
    }
    f32x2 zs[4], zh[4];
    zs[3] = cpowk(aA, tA, (float)(96 - 32 * w));
    zh[0] = cmul(cpowk(aA, tA, (float)(32 * w)), c2);
#pragma unroll
    for (int k = 1; k < 4; ++k) { zs[3 - k] = cmul(zs[4 - k], l8); zh[k] = cmul(zh[k - 1], l8); }
    const int sig = 4 * w + g;
    float lr[5], ls[5], sa[5], sb[5], ha[5], hb[5], ca[5], cb[5];
#pragma unroll
    for (int k = 0; k < 4; ++k) { zh[k].y = -zh[k].y; asm volatile("" : "+v"(zs[k]), "+v"(zh[k])); }
    f32x4 q0[5], q1[5];
    const u32 tb = opaque(L0 + L_TAB + w * 3200 + pl * 16), tp = opaque(L0 + L_TAB + w * 3200 + (i16 >> 1) * 16), tg = opaque(tp + g * 640);
    const int nvalid = (c - w + 3) >> 2;
    f32x2 lam4 = cpowk(aA, tA, 512.f);
    f32x2 lame = cpowk(aA, tA, (float)(128 * ((c - 1 - w) & 3)));
    asm volatile("" : "+v"(lam4), "+v"(lame));
    STAMP(1);
    const u32 ax0 = L0 + (32 * w + i16) * 128 + ((g ^ ((i16 >> 1) & 7)) << 4);
    const u32 ab0 = L0 + 16384 + (40 * (i16 & 1) + (i16 >> 1)) * 128 + ((g ^ (2 * (i16 >> 2) + (i16 & 1))) << 4);
    const u32 ax1 = ax0 ^ 64, ab1 = ab0 ^ 64;
    const u32 cfull = opaque(L0 + C_FULL), cfree = opaque(L0 + C_FREE);
    const bool gater = (xcd & 1) ? ((r & 7) >= 2 && (r & 7) < 7) : (r & 7) < 5;
    Frag X, Y;
    sw_wait(cfull, 1);
    const u32 seed = lds_ld<u32>(opaque(L0 + L_TAG));
    {
      const u32 pa = opaque(ax0 + L_A0), pb = opaque(ab0 + L_A0);
      X.a0 = lds_ld<u32x4>(pa); X.b0 = lds_ld<u32x4>(pb); X.a1 = lds_ld<u32x4>(pa + 2048); X.b1 = lds_ld<u32x4>(pb + 1024);
      X.b2 = lds_ld<u32x4>(pb + 2048); X.b3 = lds_ld<u32x4>(pb + 3072); X.b4 = lds_ld<u32x4>(pb + 4096);
    }
    if (gater) {
      accg[0] = mfma(lds_ld<u32x4>(ax1 + L_A0), gw1, mfma(X.a0, gw0, accg[0]));
      accg[1] = mfma(lds_ld<u32x4>(ax1 + L_A0 + 2048), gw1, mfma(X.a1, gw0, accg[1]));
    }
    asm volatile("s_waitcnt lgkmcnt(0)" : "+v"(X.a0), "+v"(X.a1), "+v"(X.b0), "+v"(X.b1), "+v"(X.b2), "+v"(X.b3), "+v"(X.b4));
    tag = __builtin_amdgcn_readfirstlane(seed) + 1u;
    const u32 fxo_ = lane == 0 ? WS_FX + (u32)((ti * 8 + dj) * 4 + w) * 128 : OOB;
    const u32x2 fxv = {tag, tag ^ FMIX};
    u32 so = L_A0;
#pragma unroll 1
    for (int s = 0; s < 8; s += 2) {
      u32 have = g1_half_peek(acc, Y, X, ax1 + so, ab1 + so, cfull);
      so = s == 2 ? 0 : so + STG;
      TR(4 + 4 * s);
      if (RARE(have < (u32)(s + 2))) sw_wait(cfull, s + 2);
      g1_half<true>(acc, X, Y, ax0 + so, ab0 + so);
      have = g1_half_peek(acc, Y, X, ax1 + so, ab1 + so, cfull);
      so = s == 4 ? L_A0 : so + STG;
      TR(5 + 4 * s);
      if (RARE(have < (u32)(s + 3))) sw_wait(cfull, s + 3);
      g1_half<true>(acc, X, Y, ax0 + so, ab0 + so);
      TR(6 + 4 * s);
      dma_mark(rka, SEQ + 4 * s + 4, L0 + C_FREE + 4 * w);
      if (RARE(s == 0)) { waitv<1>(); st8(rws, fxo_, fxv); }
    }
    {
      const u32 have = g1_half_peek(acc, Y, X, ax1 + ring(8), ab1 + ring(8), cfull);
      if (RARE(have < 10u)) sw_wait(cfull, 10);
      sw_wait4(cfree, 6);
#pragma unroll
      for (int k = 0; k < 4; ++k) lds_st<f32x4>(tb + k * 640, f32x4{zs[k].x, zs[k].y, zh[k].x, zh[k].y});
      lds_st<f32x4>(tb + 4 * 640, f32x4{lam.x, lam.y, c2.x, -c2.y});
      g1_half<false>(acc, X, Y, ax0 + ring(9), ab0 + ring(9));
#pragma unroll
      for (int nt = 0; nt < 5; ++nt) { q1[nt] = lds_ld<f32x4>(tp + 4 * 640 + nt * 128); q0[nt] = lds_ld<f32x4>(tg + nt * 128); }
      asm volatile("s_waitcnt lgkmcnt(10)" ::: "memory");
      g1_half<false>(acc, Y, X, ax1 + ring(9), ab1 + ring(9));
      g1_last(acc, Y);
      waitv<0>();
      post(L0 + C_FREE + 4 * w, 10u);
    }
    const u32x4 fr10 = peek4(cfree);
    {
      const float sg = (lane & 1) ? 1.f : -1.f;
#pragma unroll
      for (int nt = 0; nt < 5; ++nt) {
        sa[nt] = q0[nt].x; sb[nt] = sg * q0[nt].y; ha[nt] = -sg * q0[nt].z; hb[nt] = q0[nt].w;
        lr[nt] = q1[nt].x; ls[nt] = sg * q1[nt].y; ca[nt] = -sg * q1[nt].z; cb[nt] = q1[nt].w;
      }
    }
    STAMP(2);
    TR(44);
    float v[8][5];
#pragma unroll
    for (int jj = 0; jj < 8; ++jj)
#pragma unroll
      for (int nt = 0; nt < 5; ++nt) v[jj][nt] = acc[jj >> 2][nt][jj & 3];
    float e[5];
#pragma unroll
    for (int nt = 0; nt < 5; ++nt) e[nt] = v[0][nt];
#pragma unroll
    for (int jj = 1; jj < 8; ++jj) scan_step(e, v[jj], e, lr, ls);
    {
      float z[5] = {0.f, 0.f, 0.f, 0.f, 0.f}, we[5];
      scan_step(we, z, e, sa, sb);
      auto s01 = __builtin_amdgcn_permlane32_swap(f2u(we[0]), f2u(we[1]), false, false);
      auto s23 = __builtin_amdgcn_permlane32_swap(f2u(we[2]), f2u(we[3]), false, false);
      auto s44 = __builtin_amdgcn_permlane32_swap(f2u(we[4]), f2u(we[4]), false, false);
      const float y01 = u2f(s01[0]) + u2f(s01[1]), y23 = u2f(s23[0]) + u2f(s23[1]), y4 = u2f(s44[0]) + u2f(s44[1]);
      auto q03 = __builtin_amdgcn_permlane16_swap(f2u(y01), f2u(y23), false, false);
      auto q44 = __builtin_amdgcn_permlane16_swap(f2u(y4), f2u(y4), false, false);
      if (RARE(min4(fr10) < 10u)) sw_wait4(cfree, 10);
      const u32 wa = opaque(L0 + L_WS + w * 320 + i16 * 4);
      lds_st<float>(wa + (2 * (g & 1) + (g >> 1)) * 64, u2f(q03[0]) + u2f(q03[1]));
      lds_st<float>(wa + 256, u2f(q44[0]) + u2f(q44[1]));
      const u32 ra = opaque(L0 + L_ERAW + sig * 320 + i16 * 4);
#pragma unroll
      for (int nt = 0; nt < 5; ++nt) lds_st<float>(ra + nt * 64, e[nt]);
    }
    post(L0 + C_BAR + 4 * w, 1u);
    sw_wait4(L0 + C_BAR, 1);
    if (w == 2) {
      const u32 ap = opaque(L0 + L_WS + pl * 8);
      const f32x2 se = (lds_ld<f32x2>(ap) + lds_ld<f32x2>(ap + 320)) + (lds_ld<f32x2>(ap + 640) + lds_ld<f32x2>(ap + 960));
      st16(rws, lane < 40 ? WS_SG + (u32)(c * 640 + 40 * j + lane) * 16 : OOB, u32x4{f2u(se.x), tag, f2u(se.y), tag});
    }
    STAMP(3);
    if (gater) {
      float gl = accg[0][0];
#pragma unroll
      for (int k = 1; k < 8; ++k) gl = (i16 == k) ? accg[k >> 2][k & 3] : gl;
      st8(rws, i16 < 8 ? WS_GG + (u32)(rot10 * 2048 + 128 * c + 32 * w + 8 * g + i16) * 8 : OOB, u32x2{f2u(gl), tag});
    }
    float pf[5];
    {
      u32 era = opaque(L0 + L_ERAW + pl * 8);
      const f32x2 l8r = rot(l8);
      f32x2 pp = {0.f, 0.f};
#pragma unroll 1
      for (int s2 = 0; s2 < w; ++s2, era += 1280) {
        const f32x2 e0 = lds_ld<f32x2>(era), e1 = lds_ld<f32x2>(era + 320), e2 = lds_ld<f32x2>(era + 640), e3 = lds_ld<f32x2>(era + 960);
        pp = cmul(pp, l8, l8r) + e0; pp = cmul(pp, l8, l8r) + e1; pp = cmul(pp, l8, l8r) + e2; pp = cmul(pp, l8, l8r) + e3;
      }
      const f32x2 e0 = lds_ld<f32x2>(era), e1 = lds_ld<f32x2>(era + 320), e2 = lds_ld<f32x2>(era + 640);
      const u32 ptb = L0 + L_PT + w * 1280;
      const u32 pw = opaque(ptb + pl * 8);
#pragma unroll
      for (int k = 0; k < 4; ++k) {
        const f32x2 q = cmul(pp, c2);
        lds_st<f32x2>(pw + k * 320, f32x2{q.x, -q.y});
        if (k < 3) pp = cmul(pp, l8, l8r) + (k == 0 ? e0 : k == 1 ? e1 : e2);
      }
      const u32 pr_ = opaque(ptb + g * 320 + i16 * 4);
#pragma unroll
      for (int nt = 0; nt < 5; ++nt) pf[nt] = lds_ld<float>(pr_ + nt * 64);
      const float z[5] = {0.f, 0.f, 0.f, 0.f, 0.f};
#pragma unroll
      for (int jj = 0; jj < 8; ++jj) scan_step(v[jj], z, v[jj], ca, cb);
#pragma unroll
      for (int nt = 0; nt < 5; ++nt) ls[nt] = -ls[nt];
    }
    {
      u32 off[4];
#pragma unroll
      for (int k = 0; k < 4; ++k) off[k] = ((k < nvalid || c + w + k == 0) && lane < 40) ? WS_SG + (u32)((w + 4 * k) * 640 + 40 * j + lane) * 16 : OOB;
      u32x4 sv[4];
#pragma unroll 1
      for (int it = 0; it < SPIN_CAP; ++it) {
        asm volatile("" ::: "memory");
        bool ok = true;
#pragma unroll
        for (int k = 0; k < 4; ++k) sv[k] = ld16(rws, off[k]);
#pragma unroll
        for (int k = 0; k < 4; ++k) ok &= (off[k] == OOB) | ((sv[k].y == tag) & (sv[k].w == tag));
        if (wave_all(ok)) break;
        __builtin_amdgcn_s_sleep(1);
      }
      asm volatile("" ::: "memory");
      STAMP(4);
      f32x2 hsum = {0.f, 0.f};
      const f32x2 l4r = rot(lam4);
#pragma unroll
      for (int k = 0; k < 4; ++k)
        if (k < nvalid) hsum = cmul(hsum, lam4, l4r) + f32x2{u2f(sv[k].x), u2f(sv[k].z)};
      hsum = cmul(hsum, lame);
      const u32 hp = opaque(L0 + L_HP + lane * 32 + w * 4);
      if (lane < 40) {
        lds_st<float>(hp, hsum.x);
        lds_st<float>(hp + 16, hsum.y);
      }
    }
    post(L0 + C_BAR + 4 * w, 2u);
    const u32 clr = peek(L0 + C_CLR);
    sw_wait4(L0 + C_BAR, 2);
    {
      float hin[5], h0[5];
      const u32 hp = opaque(L0 + L_HP + i16 * 16);
#pragma unroll
      for (int nt = 0; nt < 5; ++nt) {
        const f32x4 q = lds_ld<f32x4>(hp + nt * 256);
        hin[nt] = (q.x + q.y) + (q.z + q.w);
      }
      scan_step(h0, pf, hin, ha, hb);
      scan_step(v[0], v[0], h0, lr, ls);
#pragma unroll
      for (int jj = 1; jj < 8; ++jj) scan_step(v[jj], v[jj], v[jj - 1], lr, ls);
    }
    {
      const u32 img = L0 + L_HT + w * 1280;
      const u32 wa = opaque(img + i16 * 80 + g * 16);
      const u32 ra = opaque(img + (2 * ((lane >> 2) & 3) + (g & 1)) * 80 + (g >> 1) * 32 + (lane & 3) * 8);
      const u32 hb0 = WS_H + (u32)(8 * c + 2 * w + (g >> 1)) * 40960 + (g & 1) * 2048 + i16 * 16;
      u32x2 lo[5], hi[5];
#pragma unroll
      for (int nt = 0; nt < 5; ++nt) {
        lds_st<u32x4>(wa, u32x4{pk(v[0][nt], v[1][nt]), pk(v[2][nt], v[3][nt]), pk(v[4][nt], v[5][nt]), pk(v[6][nt], v[7][nt])});
        lo[nt] = __builtin_bit_cast(u32x2, lds_tr(ra));
        hi[nt] = __builtin_bit_cast(u32x2, lds_tr(ra + 640));
      }
      SB();
      if (RARE(__builtin_amdgcn_readfirstlane(clr) == 0)) sw_wait(L0 + C_CLR, 1);
#pragma unroll
      for (int nt = 0; nt < 5; ++nt) {
        const int o = 5 * j + nt;
        st16(rws, hb0 + (u32)((o >> 3) * 4096 + ((o >> 2) & 1) * 1024 + (o & 3) * 256), u32x4{lo[nt].x, lo[nt].y, hi[nt].x, hi[nt].y});
      }
    }
    post(L0 + C_BAR + 4 * w, 3u);
    STAMP(5);
    u32x2 eda[5], edb[5];
#pragma unroll
    for (int nt = 0; nt < 5; ++nt) {
      eda[nt] = *(const GAS u32x2*)((const GAS u16*)P.DA + 80 * dj + 16 * nt + 4 * g);
      edb[nt] = *(const GAS u32x2*)((const GAS u16*)P.DB + 80 * dj + 16 * nt + 4 * g);
    }
    const u32 go = WS_GG + (u32)(g * 2048 + 64 * ti + 16 * kq + i16) * 8, go2 = g < 2 ? go + 8 * 2048 * 8 : OOB;
    u32x2 g0 = ld8(rws, go), g1 = ld8(rws, go + 4 * 2048 * 8), g2 = ld8(rws, go2);
    const u32x2 fhv = ld8(rws, fho);
    sw_wait(ccf, 1);
    G2_REGS(cr, 2, L_A0 + 3 * 10240);
    dma_mark(rka, SEQ, L0 + C_C0 + 4 * w);
#pragma unroll 1
    for (int it = 0; it < SPIN_CAP && RARE(!wave_all((g0.y == tag) & (g1.y == tag) & ((g2.y == tag) | (g >= 2)))); ++it) {
      __builtin_amdgcn_s_sleep(1);
      asm volatile("" ::: "memory");
      g0 = ld8(rws, go); g1 = ld8(rws, go + 4 * 2048 * 8); g2 = ld8(rws, go2);
    }
    float gate, ogate;
    {
      const float s3 = (u2f(g0.x) + u2f(g1.x)) + u2f(g2.x);
      auto sa_ = __builtin_amdgcn_permlane32_swap(f2u(s3), f2u(s3), false, false);
      const float s2 = u2f(sa_[0]) + u2f(sa_[1]);
      auto sb_ = __builtin_amdgcn_permlane16_swap(f2u(s2), f2u(s2), false, false);
      gate = rbf(1.f / (1.f + expf(-rbf(u2f(sb_[0]) + u2f(sb_[1])))));
      ogate = rbf(1.f - gate);
    }
    u32x2 ska[5], skb[5];
#pragma unroll
    for (int nt = 0; nt < 5; ++nt) {
#pragma unroll
      for (int e = 0; e < 2; ++e) {
        const float x0 = bflo(xq[nt][e]), x1 = bfhi(xq[nt][e]);
        ska[nt][e] = pk(x0 * bflo(eda[nt][e]), x1 * bfhi(eda[nt][e]));
        skb[nt][e] = pk(x0 * bflo(edb[nt][e]), x1 * bfhi(edb[nt][e]));
      }
      asm volatile("" : "+v"(ska[nt]), "+v"(skb[nt]));
    }
    waitv<1>();
    if (RARE(!flag_ok(fhv, fho, tag))) poll_flags(rws, fho, tag);
    asm volatile("" ::: "memory");
    __builtin_amdgcn_s_setprio(0);
    STAMP(6);
    G2_LOADH();
    STAMP(7);
    G2_INIT(L_A0);
    h_check<0, 4>(hf_, rws, ho);
    g2_half0<false>(y, cr[0], hf_[0]); g2_half0<false>(y, cr[1], hf_[1]); g2_half0<false>(y, cr[2], hf_[2]);
    sw_wait(ccf, 2);
    g2_half<0, 4>(y, P0, cr[3], hf_[3], cab[0][0]);
    STAMP(8);
    h_check<4, 10>(hf_, rws, ho);
    G2_UN(2, 0); G2_UN(3, 1); G2_U(4, 2, 3);
    h_check<10, 14>(hf_, rws, ho);
    G2_UN(5, 3);
    h_check<14, 20>(hf_, rws, ho);
    STAMP(9);
    G2_U(6, 4, 6);
    lds_st<float>(opaque(L0 + L_GATE + (16 * w + i16) * 4), gate);
    post(L0 + C_BAR + 4 * w, 4u);
    waitl();
    const u32 bo = WS_BT + (u32)(80 * dj + (lane >> 2)) * 8192 + (64 * ti + 16 * w) * 4 + (lane & 3) * 16;
    u32x4 bv[5];
#pragma unroll
    for (int k = 0; k < 5; ++k) bv[k] = ld16(rws, bo + k * 16 * 8192);
    SB();
    G2_U(7, 5, 8); G2_U(8, 6, 10); G2_ULAST(9, 7);
    STAMP(10);
#pragma unroll 1
    for (int it = 0; it < SPIN_CAP; ++it) {
      bool ok = true;
#pragma unroll
      for (int k = 0; k < 5; ++k) ok &= (bv[k].y == tag) & (bv[k].w == tag);
      if (!RARE(!wave_all(ok))) break;
      __builtin_amdgcn_s_sleep(2);
      asm volatile("" ::: "memory");
#pragma unroll
      for (int k = 0; k < 5; ++k) bv[k] = ld16(rws, bo + k * 16 * 8192);
    }
    STAMP(11);
    sw_wait4(L0 + C_BAR, 4);
    {
      const u32 ba = opaque(L0 + L_BST + w * 2560 + lane * 8), ia = opaque(ima);
#pragma unroll
      for (int k = 0; k < 5; ++k) lds_st<u32x2>(ba + k * 512, u32x2{bv[k].x, bv[k].z});
#pragma unroll
      for (int nt = 0; nt < 5; ++nt) {
        const u32x2 yb = __builtin_bit_cast(u32x2, lds_tr(ba + nt * 512));
        f32x4 o;
#pragma unroll
        for (int e = 0; e < 2; ++e) {
          const u32 bb = pk(bflo(yb[e]) + bflo(skb[nt][e]), bfhi(yb[e]) + bfhi(skb[nt][e]));
          const u32 ob = pk(ogate * bflo(bb), ogate * bfhi(bb));
          o[2 * e] = gate * (y[nt][2 * e] + bflo(ska[nt][e])) + bflo(ob);
          o[2 * e + 1] = gate * (y[nt][2 * e + 1] + bfhi(ska[nt][e])) + bfhi(ob);
        }
        lds_st<f32x4>(ia + nt * 64, o);
      }
    }
  } else {
    const int pr = xcd * 16 + (r >> 1);
    const int nch = (r & 1) ? 2 : 3, ch = 5 * pr + ((r & 1) ? 3 : 0) + kq;
    const u32 slot = opaque(L0 + L_B0 + kq * BSLOT) ;
    u32x2 fhv;
    f32x4 ya[8];
    if (__builtin_expect(kq < nch, 1)) {
      const GAS u32* pa = (const GAS u32*)P.Are + ch * 64 + lane;
      const u32 wAre = *pa, wAim = ((const GAS u32*)P.Aim)[ch * 64 + lane];
      const u32 wBre = ((const GAS u32*)P.sBre)[ch * 64 + lane], wBim = ((const GAS u32*)P.sBim)[ch * 64 + lane];
      const u32 wCre = ((const GAS u32*)P.sCre)[ch * 64 + lane], wCim = ((const GAS u32*)P.sCim)[ch * 64 + lane];
      const u32 wdt = ((const GAS u16*)P.ldtB)[ch];
      const float dt = rbf(expf(bf2f(wdt)));
      f32x4 ka[3] = {f32x4{0.f, 0.f, 0.f, 0.f}, f32x4{0.f, 0.f, 0.f, 0.f}, f32x4{0.f, 0.f, 0.f, 0.f}};
      const u32 tr = opaque(slot + i16 * TROW + g * 16);
      const u32 fxo = lane < 32 ? WS_FX + (u32)((lane * 8 + ch / 80) * 4 + (ch % 80) / 20) * 128 : OOB;
      u32 tv[56], tu[56];
#pragma unroll 1
      for (int rd = 0; rd < 2; ++rd) {
#pragma unroll
        for (int k = 0; k < 56; ++k) tu[k] = tv[k];
        const u32 sh = rd ? 0u : 16u;
        const float a = dt * bfhi(wAre << sh), t = dt * bfhi(wAim << sh);
        const f32x2 db = {dt * bfhi(wBre << sh), dt * bfhi(wBim << sh)}, cc = {bfhi(wCre << sh), bfhi(wCim << sh)};
        const f32x2 W = cmul(cc, db);
        const f32x2 z1 = cpowk(a, t, 1.f), z8 = cpowk(a, t, 8.f), z128 = cpowk(a, t, 128.f);
        {
          f32x2 cv = z8;
          tv[41] = pk(cv.x, cv.y);
#pragma unroll
          for (int jj = 2; jj < 16; ++jj) { cv = cmul(cv, z8); tv[40 + jj] = pk(cv.x, cv.y); }
        }
        {
          const f32x2 y1 = {z1.x, -z1.y}, yh = {z128.x, -z128.y};
          f32x2 R[8];
          R[0] = f32x2{W.x, -W.y};
#pragma unroll
          for (int k = 1; k < 8; ++k) R[k] = cmul(R[k - 1], y1);
#pragma unroll
          for (int k = 0; k < 8; ++k) tv[k] = pk(R[k].x, R[k].y);
#pragma unroll
          for (int k = 0; k < 8; ++k) { R[k] = cmul(R[k], yh); tv[8 + k] = pk(R[k].x, R[k].y); }
#pragma unroll
          for (int hh = 0; hh < 2; ++hh)
#pragma unroll
            for (int k = 0; k < 4; ++k) { R[2 * k] = cmul(R[2 * k], yh); tv[16 + 4 * hh + k] = pk(R[2 * k].x, R[2 * k].y); }
#pragma unroll
          for (int hh = 0; hh < 4; ++hh)
#pragma unroll
            for (int k = 0; k < 2; ++k) { R[4 * k] = cmul(R[4 * k], yh); tv[24 + 2 * hh + k] = pk(R[4 * k].x, R[4 * k].y); }
#pragma unroll
          for (int hh = 0; hh < 9; ++hh) { R[0] = cmul(R[0], yh); tv[32 + hh] = pk(R[0].x, R[0].y); }
        }
      }
      STAMP(1);
      get_tag();
      poll_flags(rws, fxo, tag);
      u32x4 uv[4];
#pragma unroll
      for (int k = 0; k < 4; ++k) uv[k] = ld16(rws, WS_XT + (u32)(ch >> 2) * 16384 + (ch & 3) * 16 + (64 * k + lane) * 64);
      const u32 uw = opaque(slot + UPAD + lane * 16);
      const u32 kc = slot + KC_OFF;
      const bool r1 = lane < 32;
      const u32 xa = opaque(kc - 4 + (r1 ? 16 * lane : 256 + 8 * lane)), xb = opaque(xa + (r1 ? 12 : 8));
      const u32 sA = opaque(r1 ? 0x05040706u : 0x03020504u), sB = opaque(r1 ? 0x05040706u : 0x05040504u), sC = opaque(r1 ? 0x03020504u : 0x01000302u),
                sD = opaque(r1 ? 0x03020504u : 0x05040504u), sE = opaque(lane <= 32 ? 0x03020504u : 0x05040504u);
      const u32 ka0 = opaque(kc + (128 * (g >> 1) + 8 * i16 + 4 * (g & 1)) * 2);
      const u32 ka1 = opaque(kc + (g < 2 ? 256 + 64 * g + 4 * i16 : 256 + 64 * g + 2 * i16) * 2), ka1b = opaque(ka1 + (g < 2 ? 4 : 64));
      const u32 ka2 = opaque(kc + (512 + 64 * min(g, 2) + i16) * 2);
      const u32 sbase = __builtin_amdgcn_readfirstlane(slot);
      sw_wait4(L0 + C_FREE, 10);
      STAMP(6);
#define TW(v, row) asm volatile("ds_write_addtid_b32 %0 offset:%1" :: "v"(v), "n"((row) * TROW) : "memory")
      asm volatile("s_mov_b32 m0, %0\n\ts_nop 0" :: "s"(sbase) : "m0", "memory");
      TW(0x00003f80u, 41);
#pragma unroll
      for (int rd = 0; rd < 2; ++rd) {
#pragma unroll
        for (int k = 0; k < 56; ++k) TW(rd ? tv[k] : tu[k], k < 41 ? k : k + 1);
#pragma unroll
        for (int ks = 0; ks < 4; ++ks) {
          const u32x4 bb = lds_ld<u32x4>(tr + 41 * TROW + ks * 64);
#pragma unroll
          for (int m = 0; m < 3; ++m) ka[m] = mfma(lds_ld<u32x4>(tr + 16 * m * TROW + ks * 64), bb, ka[m]);
        }
      }
#define UW(v, off) asm volatile("ds_write_addtid_b32 %0 offset:%1" :: "v"(v), "n"(off) : "memory")
#pragma unroll
      for (int cc = 0; cc < 4; ++cc) { UW(0u, cc * UCOPY); UW(0u, cc * UCOPY + 144); }
#pragma unroll
      for (int k = 0; k < 4; ++k) {
        lds_st<u32x4>(uw + k * 1024, uv[k]);
#pragma unroll
        for (int e = 0; e < 4; ++e) lds_st<u32>(uw + k * 1024 + UCOPY + 4 + 4 * e, uv[k][e]);
        lds_st<u32x2>(uw + k * 1024 + 2 * UCOPY + 8, u32x2{uv[k].x, uv[k].y});
        lds_st<u32x2>(uw + k * 1024 + 2 * UCOPY + 16, u32x2{uv[k].z, uv[k].w});
      }
      STAMP(5);
      {
        const u32 q0 = pk(ka[2].x, ka[2].y), q1 = pk(ka[2].z, ka[2].w);
        if (g < 3) { lds_st<u16>(ka2, (u16)q0); lds_st<u16>(ka2 + 32, (u16)(q0 >> 16)); lds_st<u16>(ka2 + 64, (u16)q1); lds_st<u16>(ka2 + 96, (u16)(q1 >> 16)); }
        lds_st<u32x2>(ka0, u32x2{pk(ka[0].x, ka[0].y), pk(ka[0].z, ka[0].w)});
        lds_st<u32>(ka1, pk(ka[1].x, ka[1].y));
        lds_st<u32>(ka1b, pk(ka[1].z, ka[1].w));
        lds_st<u32>(kc - 4, 0u);
      }
#define PERM(hi, lo, sel) __builtin_amdgcn_perm(hi, lo, sel)
      {
        const u32 R0 = lds_ld<u32>(xa), R1 = lds_ld<u32>(xa + 4), R2 = lds_ld<u32>(xa + 8), R3 = lds_ld<u32>(xb), R4 = lds_ld<u32>(xb + 4);
        const u32 ea_ = opaque(kc + 768 + 4 * lane), eb_ = opaque(kc + 1024 + 2 * lane);
        const u32 E0 = lds_ld<u32>(ea_), E1 = lds_ld<u32>(ea_ + 4);
        const u32 f0 = lds_ld<u16>(eb_), f1 = lds_ld<u16>(eb_ + 2), f2 = lds_ld<u16>(eb_ + 128), f3 = lds_ld<u16>(eb_ + 130);
        UW(0u, KOFF); UW(0u, KOFF + KCOPY);
        const u32 k0 = opaque(slot + KOFF + KPAD + lane * 16);
        lds_st<u32x4>(k0 + KCOPY, u32x4{PERM(R4, R3, sA), PERM(R3, R2, sB), PERM(R2, R1, sA), PERM(R1, R0, sB)});
        lds_st<u32x4>(k0, u32x4{PERM(R4, R3, sC), PERM(R3, R2, sD), PERM(R2, R1, sC), PERM(R1, R0, sE)});
        const u32 e00 = PERM(E0, E0, 0x01000100u), e11 = PERM(E0, E0, 0x03020302u);
        lds_st<u32x4>(k0 + KCOPY + 1024, u32x4{PERM(E1, E1, 0x01000100u), e11, PERM(E0, E0, 0x01000302u), e00});
        lds_st<u32x4>(k0 + 1024, u32x4{PERM(E0, E1, 0x07060100u), e11, e00, e00});
#pragma unroll
        for (int rr = 0; rr < 2; ++rr) {
          const u32 e0 = rr ? f2 : f0, e1 = rr ? f3 : f1, t4 = (lane & 1) ? e1 : e0;
          const u32 c00 = e0 | (e0 << 16), c11 = e1 | (e1 << 16);
          lds_st<u32x4>(k0 + KCOPY + 2048 + 1024 * rr, u32x4{c11, e1 | (t4 << 16), c00, c00});
          lds_st<u32x4>(k0 + 2048 + 1024 * rr, u32x4{c11, t4 | (e0 << 16), c00, c00});
        }
      }
#pragma unroll
      for (int k = 0; k < 16; ++k) lds_st<u32>(uw + (k >> 2) * 1024 + 3 * UCOPY + 12 + 4 * (k & 3), uv[k >> 2][k & 3]);
      post(L0 + C_BPREP + 4 * kq, 1u);
      STAMP(2);
#pragma unroll
      for (int A = 0; A < 8; ++A) ya[A] = f32x4{0.f, 0.f, 0.f, 0.f};
      const u32 ta = opaque(slot + KOFF + KPAD + (i16 & 1) * KCOPY + ((i16 >> 1) - g) * 16 - 64);
      const u32 ua = opaque(slot + (i16 & 3) * UCOPY + (i16 >> 2) * 128 + g * 16);
      u32x4 T[2][8], U[2][8];
#define CV_READ(S, st)                                                                                        \
      _Pragma("unroll") for (int D = 0; D < 8; ++D) {                                                         \
        T[S][D] = lds_ld<u32x4>(ta + ((st) >> 1) * 128 + D * 512 + (1 - ((st) & 1)) * 64);                   \
        U[S][D] = lds_ld<u32x4>(ua + UPAD - ((st) >> 1) * 128 + D * 512 + ((st) & 1) * 64);                  \
      }
      CV_READ(0, 0);
      __builtin_amdgcn_s_setprio(2);
#pragma unroll
      for (int st = 0; st < 8; ++st) {
        SB();
        if (st == 6) fhv = ld8(rws, fho);
        if (st < 7) { CV_READ((st + 1) & 1, st + 1); }
#pragma unroll
        for (int S = 0; S < 8; ++S)
#pragma unroll
          for (int D = 0; D <= S; ++D) ya[S] = mfma(T[st & 1][D], U[st & 1][S - D], ya[S]);
        if (st < 7) {
#pragma unroll
          for (int k = 0; k < 16; ++k) {
            __builtin_amdgcn_sched_group_barrier(0x8, 2, 0);
            __builtin_amdgcn_sched_group_barrier(0x100, 1, 0);
          }
          __builtin_amdgcn_sched_group_barrier(0x8, 4, 0);
        }
      }
      SB();
      STAMP(3);
    } else {
      get_tag();
      sw_wait4(L0 + C_FREE, 10);
      post(L0 + C_BPREP + 4 * kq, 1u);
      fhv = ld8(rws, fho);
#pragma unroll
      for (int A = 0; A < 8; ++A) ya[A] = f32x4{0.f, 0.f, 0.f, 0.f};
    }
    sw_wait(ccf, 1);
    G2_REGS(cr, 3, L_A0 + 5 * 10240);
    post(L0 + C_BDONE + 4 * kq, 1u);
    {
      const bool odd = lane & 1;
      const u32 bo = kq < nch ? WS_BT + (u32)ch * 8192 + (i16 >> 2) * 256 + g * 64 + (odd ? 0 : 32) + ((lane & 2) ? 0 : 16) : OOB;
#pragma unroll
      for (int A = 0; A < 8; ++A) {
        const u32 x0 = pk(ya[A].x, ya[A].y), x1 = pk(ya[A].z, ya[A].w);
        const u32 rv = __builtin_amdgcn_update_dpp(0u, odd ? x1 : x0, 0xB1, 0xf, 0xf, true);
        st16(rws, bo + A * 1024, u32x4{odd ? x0 : rv, tag, odd ? rv : x1, tag});
      }
    }
    if (RARE(!flag_ok(fhv, fho, tag))) poll_flags(rws, fho, tag);
    asm volatile("" ::: "memory");
    __builtin_amdgcn_s_setprio(0);
    STAMP(4);
    G2_LOADH();
    G2_INIT(L_B0);
    h_check<0, 6>(hf_, rws, ho);
    STAMP(8);
#pragma unroll
    for (int k = 0; k < 5; ++k) g2_half0<false>(y, cr[k], hf_[k]);
    sw_wait(ccf, 4);
    g2_half<0, 4>(y, P0, cr[5], hf_[5], cab[0][0]);
    STAMP(9);
    h_check<6, 12>(hf_, rws, ho);
    G2_U(3, 0, 5); G2_U(4, 1, 7); G2_U(5, 2, 9);
    h_check<12, 20>(hf_, rws, ho);
    G2_U(6, 3, 11); G2_U(7, 4, 12); G2_U(8, 5, 13); G2_ULAST(9, 6);
    G2_B_IMAGE();
  }

  hwbar();
  STAMP(12);
  if (b == 255 && tid == 0) __hip_atomic_store((GAS u32*)P.ws, tag, __ATOMIC_RELAXED, __HIP_MEMORY_SCOPE_AGENT);
  {
    const int et = tid >> 3, es = tid & 7;
    const u32 ia = opaque(L0 + L_IMG0 + et * 320 + ((es & 4) | ((es ^ (et >> 1)) & 3)) * 16);
    const float gt = lds_ld<float>(opaque(L0 + L_GATE + et * 4));
    GAS char* op = (GAS char*)P.out + (64 * ti + et) * 2560 + dj * 320 + es * 16;
    const f32x4 o0 = lds_ld<f32x4>(ia) + gt * lds_ld<f32x4>(ia + (L_IMG1 - L_IMG0));
    const f32x4 o1 = lds_ld<f32x4>(ia + 128) + gt * lds_ld<f32x4>(ia + 128 + (L_IMG1 - L_IMG0));
    *(GAS f32x4*)op = o0;
    *(GAS f32x4*)(op + 128) = o1;
    if (es < 4) *(GAS f32x4*)(op + 256) = lds_ld<f32x4>(ia + 256) + gt * lds_ld<f32x4>(ia + 256 + (L_IMG1 - L_IMG0));
  }
  DUMP();
  STAMP(13);
}

extern "C" void kernel_launch(void* const* d_in, const int* in_sizes, int n_in, void* d_out, int out_size, void* d_ws,
                              size_t ws_size, hipStream_t stream) {
  Params p;
  p.x = (const u16*)d_in[0];
  p.Lre = (const u16*)d_in[1];
  p.Lim = (const u16*)d_in[2];
  p.Bre = (const u16*)d_in[3];
  p.Bim = (const u16*)d_in[4];
  p.Cre = (const u16*)d_in[5];
  p.Cim = (const u16*)d_in[6];
  p.ldtA = (const u16*)d_in[7];
  p.DA = (const u16*)d_in[8];
  p.Are = (const u16*)d_in[10];
  p.Aim = (const u16*)d_in[11];
  p.sBre = (const u16*)d_in[12];
  p.sBim = (const u16*)d_in[13];
  p.sCre = (const u16*)d_in[14];
  p.sCim = (const u16*)d_in[15];
  p.ldtB = (const u16*)d_in[16];
  p.DB = (const u16*)d_in[17];
  p.gw = (const u16*)d_in[18];
  p.out = (float*)d_out;
  p.ws = (char*)d_ws;
  for (int i = 0; i < 16; ++i) p.seq[i] = i + 1;
  fused_kernel<<<256, 512, 0, stream>>>(p);
}
```

```cpp
#define OAUX 16
#include <hip/hip_runtime.h>
#include <stdint.h>

typedef unsigned short u16;
typedef unsigned int u32;
typedef unsigned long long u64;
typedef __bf16 bf16x8 __attribute__((ext_vector_type(8)));
typedef short s16x4 __attribute__((ext_vector_type(4)));
typedef float f32x4 __attribute__((ext_vector_type(4)));
typedef float f32x2 __attribute__((ext_vector_type(2)));
typedef u32 u32x4 __attribute__((ext_vector_type(4)));
typedef u32 u32x2 __attribute__((ext_vector_type(2)));
typedef u64 u64x8 __attribute__((ext_vector_type(8)));
typedef u64 u64x4 __attribute__((ext_vector_type(4)));
typedef __bf16 bf16x2_t __attribute__((ext_vector_type(2)));
typedef __amdgpu_buffer_rsrc_t rsrc_t;
#define LAS __attribute__((address_space(3)))
#define GAS __attribute__((address_space(1)))
#define DEV __device__ __forceinline__
#define INL __attribute__((always_inline))
#if defined(__HIP_DEVICE_COMPILE__)
#define NOPK __attribute__((target("no-packed-fp32-ops")))
#else
#define NOPK
#endif

constexpr u32 WS_E = 0;
constexpr u32 WS_FH = 4096;
constexpr u32 WS_FX = WS_FH + 256 * 128;
constexpr u32 WS_SG = WS_FX + 1024 * 128;
constexpr u32 WS_GG = WS_SG + 16 * 640 * 16;
constexpr u32 WS_XT = WS_GG + 10 * 2048 * 8;
constexpr u32 WS_BT = WS_XT + 640 * 2048 * 2;
constexpr u32 WS_H = WS_BT + 640 * 1024 * 8;
constexpr u32 WS_END = WS_H + 128 * 40 * 1024;
constexpr u32 WS_DIAG = (WS_END + 4095) & ~4095u;

constexpr u32 STG = 26624;
constexpr u32 L_A0 = 81920;
constexpr u32 L_B0 = 0;
__attribute__((always_inline)) constexpr u32 ring(int s) { return s % 6 < 3 ? L_A0 + (s % 6) * STG : (s % 6 - 3) * STG; }
constexpr u32 BSLOT = 27008;
constexpr u32 L_CNT = 81024;
constexpr u32 C_FREE = L_CNT, C_PRO = L_CNT + 16, C_BAR = L_CNT + 32, C_BDONE = L_CNT + 48, C_C0 = L_CNT + 64, C_BPREP = L_CNT + 80,
              C_STRIP = L_CNT + 96, C_FULL = L_CNT + 112, C_CFULL = L_CNT + 116, C_CLR = L_CNT + 120, L_TAG = L_CNT + 128;
constexpr u32 L_GATE = L_CNT + 256;
constexpr u32 L_RAW = ring(5);
constexpr u32 L_TAB = ring(4);
constexpr u32 L_ERAW = L_A0;
constexpr u32 L_WS = L_A0 + 5120;
constexpr u32 L_HP = L_A0 + 6400;
constexpr u32 L_PT = L_A0 + 7680;
constexpr u32 L_HT = L_A0 + 12800;
constexpr u32 L_BST = L_A0;
constexpr u32 L_IMG0 = L_A0 + 10240, L_IMG1 = L_A0 + 30720;
constexpr u32 UCOPY = 4640, UPAD = 384, KOFF = 4 * UCOPY, KCOPY = 4224, KPAD = 112;
constexpr u32 KC_OFF = 16640;
constexpr u32 TROW = 288;

constexpr int SPIN_CAP = 1 << 16;

template <bool B> struct BoolC { static constexpr bool value = B; };

struct Params {
  const u16 *x, *Lre, *Lim, *Bre, *Bim, *Cre, *Cim, *ldtA, *DA;
  const u16 *Are, *Aim, *sBre, *sBim, *sCre, *sCim, *ldtB, *DB, *gw;
  float* out;
  char* ws;
  u32 seq[16];
};

DEV bool wave_all(bool p) { return __builtin_amdgcn_ballot_w64(!p) == 0; }
DEV float bf2f(u32 v) { return __builtin_bit_cast(float, v << 16); }
DEV float bflo(u32 v) { return __builtin_bit_cast(float, v << 16); }
DEV float bfhi(u32 v) { return __builtin_bit_cast(float, v & 0xffff0000u); }
DEV u32 pk(float a, float b) { bf16x2_t v = {(__bf16)a, (__bf16)b}; return __builtin_bit_cast(u32, v); }
DEV float rbf(float a) { return bflo(pk(a, a)); }
DEV u32 f2u(float a) { return __builtin_bit_cast(u32, a); }
DEV float u2f(u32 a) { return __builtin_bit_cast(float, a); }
DEV float swp(float x) { return u2f(__builtin_amdgcn_update_dpp(0, f2u(x), 0xB1, 0xf, 0xf, true)); }
DEV f32x2 rot(f32x2 z) { return f32x2{-z.y, z.x}; }
DEV f32x2 cmul(f32x2 c, f32x2 z, f32x2 zr) { return __builtin_elementwise_fma(f32x2{c.y, c.y}, zr, f32x2{c.x, c.x} * z); }
DEV f32x2 cmul(f32x2 c, f32x2 z) { return cmul(c, z, rot(z)); }
DEV f32x2 cpowk(float a, float th, float k) {
  const float L2E = 1.44269504088896f, IH = 0.15915494f, IL = 6.4206382e-9f;
  float m = __builtin_amdgcn_exp2f(k * a * L2E);
  float x = k * th;
  float hi = x * IH;
  float lo = __builtin_fmaf(x, IL, __builtin_fmaf(x, IH, -hi));
  float r = __builtin_amdgcn_fractf(hi) + lo;
  return f32x2{m * __builtin_amdgcn_cosf(r), m * __builtin_amdgcn_sinf(r)};
}
template <typename T> DEV T lds_ld(u32 a) { typedef T __attribute__((may_alias)) TA; return *(const LAS TA*)(size_t)a; }
template <typename T> DEV void lds_st(u32 a, T v) { typedef T __attribute__((may_alias)) TA; *(LAS TA*)(size_t)a = v; }
DEV s16x4 lds_tr(u32 a) { return __builtin_amdgcn_ds_read_tr16_b64_v4i16((LAS s16x4*)(size_t)a); }
DEV f32x4 mfma(u32x4 a, u32x4 b, f32x4 c) {
  return __builtin_amdgcn_mfma_f32_16x16x32_bf16(__builtin_bit_cast(bf16x8, a), __builtin_bit_cast(bf16x8, b), c, 0, 0, 0);
}
DEV rsrc_t mksrd(const void* p, u32 n) { return __builtin_amdgcn_make_buffer_rsrc((void*)p, (short)0, (int)n, 0x00020000); }
DEV void dma16(rsrc_t rs, u32 voff, u32 soff, u32 lds) {
  asm volatile("s_mov_b32 m0, %0\n\ts_nop 0\n\tbuffer_load_dwordx4 %1, %2, %3 offen lds"
               :: "s"(lds), "v"(voff), "s"(rs), "s"(soff) : "m0", "memory");
}
DEV void dma64v(rsrc_t rs, u32 v0, u32 v1, u32 v2, u32 v3, u32 so, u32 lds) {
  asm volatile("s_mov_b32 m0, %0\n\ts_nop 0\n\tbuffer_load_dwordx4 %1, %5, %6 offen lds\n\t"
               "buffer_load_dwordx4 %2, %5, %6 offen offset:1024 lds\n\tbuffer_load_dwordx4 %3, %5, %6 offen offset:2048 lds\n\t"
               "buffer_load_dwordx4 %4, %5, %6 offen offset:3072 lds"
               :: "s"(lds), "v"(v0), "v"(v1), "v"(v2), "v"(v3), "s"(rs), "s"(so) : "m0", "memory");
}
DEV void dma32v(rsrc_t rs, u32 v0, u32 v1, u32 so, u32 lds) {
  asm volatile("s_mov_b32 m0, %0\n\ts_nop 0\n\tbuffer_load_dwordx4 %1, %3, %4 offen lds\n\t"
               "buffer_load_dwordx4 %2, %3, %4 offen offset:1024 lds"
               :: "s"(lds), "v"(v0), "v"(v1), "s"(rs), "s"(so) : "m0", "memory");
}
template <int OFF> DEV void dmaq(rsrc_t r0, rsrc_t r1, u32 v0, u32 v1, u32 v2, u32 v3, u32 so, u32 slot) {
  asm volatile("s_add_u32 m0, %0, %8\n\ts_nop 0\n\tbuffer_load_dwordx4 %1, %5, %7 offen lds\n\t"
               "buffer_load_dwordx4 %2, %6, %7 offen offset:1024 lds\n\tbuffer_load_dwordx4 %3, %6, %7 offen offset:2048 lds\n\t"
               "buffer_load_dwordx4 %4, %6, %7 offen offset:3072 lds"
               :: "s"(slot), "v"(v0), "v"(v1), "v"(v2), "v"(v3), "s"(r0), "s"(r1), "s"(so), "n"(OFF) : "m0", "scc", "memory");
}
template <int OFF> DEV void dmad(rsrc_t r0, u32 v0, u32 v1, u32 so, u32 slot) {
  asm volatile("s_add_u32 m0, %0, %5\n\ts_nop 0\n\tbuffer_load_dwordx4 %1, %3, %4 offen lds\n\t"
               "buffer_load_dwordx4 %2, %3, %4 offen offset:1024 lds"
               :: "s"(slot), "v"(v0), "v"(v1), "s"(r0), "s"(so), "n"(OFF) : "m0", "scc", "memory");
}
DEV void dma_mark(rsrc_t rs, u32 so, u32 lds) {
  u64 keep;
  asm volatile("s_mov_b32 m0, %1\n\ts_mov_b64 %0, exec\n\ts_mov_b64 exec, 1\n\tbuffer_load_dword off, %2, %3 lds\n\ts_mov_b64 exec, %0"
               : "=&s"(keep) : "s"(lds), "s"(rs), "s"(so) : "m0", "memory");
}
DEV u32 opaque(u32 v) { asm volatile("" : "+v"(v)); return v; }
#define SB() __builtin_amdgcn_sched_barrier(0)
#define RARE(x) __builtin_expect(!!(x), 0)
template <int N> DEV void waitv() { asm volatile("s_waitcnt vmcnt(%0)" :: "n"(N) : "memory"); }
DEV void waitl() { asm volatile("s_waitcnt lgkmcnt(0)" ::: "memory"); }
DEV void hwbar() { waitl(); __builtin_amdgcn_s_barrier(); asm volatile("" ::: "memory"); }
DEV void post(u32 a, u32 v) {
  asm volatile("" ::: "memory");
  lds_st<u32>(a, v);
  asm volatile("" ::: "memory");
}
DEV u32 min4(u32x4 v) { return (u32)__builtin_amdgcn_readfirstlane(min(min(v.x, v.y), min(v.z, v.w))); }
DEV u32x4 peek4(u32 a) { asm volatile("" ::: "memory"); return lds_ld<u32x4>(a); }
DEV void sw_wait4(u32 a, u32 target) {
#pragma unroll 1
  for (u32 it = 0; it < (1u << 22) && min4(peek4(a)) < target; ++it) {}
  asm volatile("" ::: "memory");
}
DEV void sw_wait(u32 a, u32 target) {
  u32 v, sv, cnt = 1u << 22;
  asm volatile("1:\n\tds_read_b32 %0, %4\n\ts_sub_u32 %2, %2, 1\n\ts_cselect_b32 %3, 0, %3\n\ts_waitcnt lgkmcnt(0)\n\t"
               "v_readfirstlane_b32 %1, %0\n\ts_cmp_lt_u32 %1, %3\n\ts_cbranch_scc1 1b"
               : "=&v"(v), "=&s"(sv), "+s"(cnt), "+s"(target) : "v"(a) : "scc", "memory");
}
DEV u32 peek(u32 a) { return __hip_atomic_load((LAS u32*)(size_t)a, __ATOMIC_RELAXED, __HIP_MEMORY_SCOPE_WORKGROUP); }
DEV u32x4 ld16(rsrc_t rs, u32 off) { return __builtin_amdgcn_raw_buffer_load_b128(rs, off, 0, 16); }
DEV u32x2 ld8(rsrc_t rs, u32 off) { return __builtin_amdgcn_raw_buffer_load_b64(rs, off, 0, 16); }
DEV void st16(rsrc_t rs, u32 off, u32x4 v) { __builtin_amdgcn_raw_buffer_store_b128(v, rs, off, 0, 16); }
DEV void st8(rsrc_t rs, u32 off, u32x2 v) { __builtin_amdgcn_raw_buffer_store_b64(v, rs, off, 0, 16); }
constexpr u32 OOB = 0x7ff00000u;
constexpr u32 FMIX = 0x9E3779B9u;
DEV bool flag_ok(u32x2 v, u32 off, u32 tag) { return wave_all((off == OOB) | ((v.x == tag) & (v.y == (tag ^ FMIX)))); }
DEV void poll_flags(rsrc_t rs, u32 off, u32 tag) {
#pragma unroll 1
  for (int it = 0; it < SPIN_CAP; ++it) {
    asm volatile("" ::: "memory");
    if (flag_ok(ld8(rs, off), off, tag)) break;
    __builtin_amdgcn_s_sleep(1);
  }
  asm volatile("" ::: "memory");
}

#define TR(k) do {} while (0)
#define DUMP() do {} while (0)
#define STAMP(k) do {} while (0)
#define CSTAMP(k) do {} while (0)

#define SCAN_STEP(o, v, h)                                                                                             \
  asm("v_fma_f32 %0, %15, %10, %5\n\tv_fma_f32 %1, %16, %11, %6\n\tv_fma_f32 %2, %17, %12, %7\n\t"                   \
      "v_fma_f32 %3, %18, %13, %8\n\tv_fma_f32 %4, %19, %14, %9\n\t"                                                   \
      "v_fmac_f32_dpp %0, %10, %20 quad_perm:[1,0,3,2] row_mask:0xf bank_mask:0xf\n\t"                                 \
      "v_fmac_f32_dpp %1, %11, %21 quad_perm:[1,0,3,2] row_mask:0xf bank_mask:0xf\n\t"                                 \
      "v_fmac_f32_dpp %2, %12, %22 quad_perm:[1,0,3,2] row_mask:0xf bank_mask:0xf\n\t"                                 \
      "v_fmac_f32_dpp %3, %13, %23 quad_perm:[1,0,3,2] row_mask:0xf bank_mask:0xf\n\t"                                 \
      "v_fmac_f32_dpp %4, %14, %24 quad_perm:[1,0,3,2] row_mask:0xf bank_mask:0xf"                                     \
      : "=&v"(o[0]), "=&v"(o[1]), "=&v"(o[2]), "=&v"(o[3]), "=&v"(o[4])                                                \
      : "v"(v[0]), "v"(v[1]), "v"(v[2]), "v"(v[3]), "v"(v[4]), "v"(h[0]), "v"(h[1]), "v"(h[2]), "v"(h[3]), "v"(h[4]), \
        "v"(ma[0]), "v"(ma[1]), "v"(ma[2]), "v"(ma[3]), "v"(ma[4]), "v"(mb[0]), "v"(mb[1]), "v"(mb[2]), "v"(mb[3]),   \
        "v"(mb[4]))
DEV void scan_step(float (&o)[5], const float (&v)[5], const float (&h)[5], const float (&ma)[5], const float (&mb)[5]) {
  float t[5];
  SCAN_STEP(t, v, h);
#pragma unroll
  for (int i = 0; i < 5; ++i) o[i] = t[i];
}

struct Frag { u32x4 a0, a1, b0, b1, b2, b3, b4; };
#define MF(d, a, b) "v_mfma_f32_16x16x32_bf16 %" #d ", %" #a ", %" #b ", %" #d "\n\t"
#define RA(d, o) "ds_read_b128 %" #d ", %24 offset:" #o "\n\t"
#define RB(d, o) "ds_read_b128 %" #d ", %25 offset:" #o "\n\t"
#define WL(n) "s_waitcnt lgkmcnt(" #n ")\n\t"
#define G1_BODY(w1, w2, w3, w5, w7, w9)                                                                    \
  w1 MF(0, 17, 19) RA(10, 0) w2 MF(5, 18, 19) RB(12, 0) w3 MF(1, 17, 20) RA(11, 2048) MF(6, 18, 20) RB(13, 1024) \
  w5 MF(2, 17, 21) RB(14, 2048) MF(7, 18, 21) RB(15, 3072) w7 MF(3, 17, 22) RB(16, 4096) MF(8, 18, 22)    \
  w9 MF(4, 17, 23) MF(9, 18, 23)
#define G1_ACC(A) "+v"(A[0][0]), "+v"(A[0][1]), "+v"(A[0][2]), "+v"(A[0][3]), "+v"(A[0][4]), "+v"(A[1][0]), "+v"(A[1][1]), "+v"(A[1][2]), "+v"(A[1][3]), "+v"(A[1][4])
#define G1_OUT(N) "=&v"(N.a0), "=&v"(N.a1), "=&v"(N.b0), "=&v"(N.b1), "=&v"(N.b2), "=&v"(N.b3), "=&v"(N.b4)
#define G1_IN(C) "v"(C.a0), "v"(C.a1), "v"(C.b0), "v"(C.b1), "v"(C.b2), "v"(C.b3), "v"(C.b4)
template <bool WAITS> DEV void g1_half(f32x4 (&A)[2][5], Frag& N, const Frag& C, u32 pa, u32 pb) {
  if constexpr (WAITS)
    asm volatile(G1_BODY(WL(5), WL(5), WL(5), WL(6), WL(7), WL(7)) : G1_ACC(A), G1_OUT(N) : G1_IN(C), "v"(pa), "v"(pb) : "memory");
  else
    asm volatile(G1_BODY("", "", "", "", "", "") : G1_ACC(A), G1_OUT(N) : G1_IN(C), "v"(pa), "v"(pb) : "memory");
}
DEV u32 g1_half_peek(f32x4 (&A)[2][5], Frag& N, const Frag& C, u32 pa, u32 pb, u32 pf) {
  u32 t = pf;
  asm volatile("ds_read_b32 %0, %0" : "+v"(t) :: "memory");
  asm volatile(G1_BODY(WL(6), WL(6), WL(6), WL(7), WL(8), WL(8)) WL(7) : G1_ACC(A), G1_OUT(N) : G1_IN(C), "v"(pa), "v"(pb) : "memory");
  u32 r;
  asm volatile("v_readfirstlane_b32 %0, %1" : "=s"(r) : "v"(t) : "memory");
  return r;
}
DEV void g1_last(f32x4 (&A)[2][5], const Frag& C) {
  asm volatile(WL(5) MF(0, 10, 12) WL(4) MF(5, 11, 12) WL(3) MF(1, 10, 13) MF(6, 11, 13) WL(2) MF(2, 10, 14) MF(7, 11, 14)
               WL(1) MF(3, 10, 15) MF(8, 11, 15) WL(0) MF(4, 10, 16) MF(9, 11, 16) "s_nop 7"
               : G1_ACC(A) : G1_IN(C) : "memory");
}

struct CF { u32x4 f[5]; };
DEV u32 peek_issue(u32 a) { asm volatile("ds_read_b32 %0, %0" : "+v"(a) :: "memory"); return a; }
DEV u32 peek_get(u32 t) { u32 r; asm volatile("s_waitcnt lgkmcnt(5)\n\tv_readfirstlane_b32 %0, %1" : "=s"(r) : "v"(t) : "memory"); return r; }
#define M2(d, a) "v_mfma_f32_16x16x32_bf16 %" #d ", %" #a ", %15, %" #d "\n\t"
#define R2(d, o) "ds_read_b128 %" #d ", %16 offset:%17+" #o "\n\t"
template <int OFF, int WT> DEV void g2_half(f32x4 (&y)[5], CF& N, const CF& Cc, u32x4 h, u32 pa) {
  asm volatile("s_waitcnt lgkmcnt(%18)\n\t" M2(0, 10) R2(5, 0) "s_waitcnt lgkmcnt(%18)\n\t" M2(1, 11) R2(6, 2048) "s_waitcnt lgkmcnt(%18)\n\t" M2(2, 12) R2(7, 4096)
               "s_waitcnt lgkmcnt(%18)\n\t" M2(3, 13) R2(8, 6144) "s_waitcnt lgkmcnt(%18)\n\t" M2(4, 14) R2(9, 8192)
               : "+v"(y[0]), "+v"(y[1]), "+v"(y[2]), "+v"(y[3]), "+v"(y[4]), "=&v"(N.f[0]), "=&v"(N.f[1]), "=&v"(N.f[2]), "=&v"(N.f[3]), "=&v"(N.f[4])
               : "v"(Cc.f[0]), "v"(Cc.f[1]), "v"(Cc.f[2]), "v"(Cc.f[3]), "v"(Cc.f[4]), "v"(h), "v"(pa), "n"(OFF), "n"(WT) : "memory");
}
#define M3(d, a) "v_mfma_f32_16x16x32_bf16 %" #d ", %" #a ", %10, %" #d "\n\t"
template <bool LAST> DEV void g2_half0(f32x4 (&y)[5], const CF& Cc, u32x4 h) {
  if constexpr (LAST)
    asm volatile(WL(4) M3(0, 5) WL(3) M3(1, 6) WL(2) M3(2, 7) WL(1) M3(3, 8) WL(0) M3(4, 9) "s_nop 7"
                 : "+v"(y[0]), "+v"(y[1]), "+v"(y[2]), "+v"(y[3]), "+v"(y[4])
                 : "v"(Cc.f[0]), "v"(Cc.f[1]), "v"(Cc.f[2]), "v"(Cc.f[3]), "v"(Cc.f[4]), "v"(h) : "memory");
  else
    asm volatile(M3(0, 5) M3(1, 6) M3(2, 7) M3(3, 8) M3(4, 9)
                 : "+v"(y[0]), "+v"(y[1]), "+v"(y[2]), "+v"(y[3]), "+v"(y[4])
                 : "v"(Cc.f[0]), "v"(Cc.f[1]), "v"(Cc.f[2]), "v"(Cc.f[3]), "v"(Cc.f[4]), "v"(h) : "memory");
}
template <int A, int B> DEV bool h_bad(const u32x4 (&h)[20]) {
  u32 m = 0;
#pragma unroll
  for (int k = A; k < B; ++k) m = max(max(max(h[k].x, h[k].y), h[k].z), max(h[k].w, m));
  return __builtin_amdgcn_ballot_w64(m == 0xffffffffu) != 0;
}
template <int A, int B> DEV void h_check(u32x4 (&h)[20], rsrc_t rs, u32 ho) {
  if (RARE((h_bad<A, B>(h)))) {
#pragma unroll 1
    for (int it = 0; it < SPIN_CAP; ++it) {
      __builtin_amdgcn_s_sleep(2);
      asm volatile("" ::: "memory");
#pragma unroll
      for (int k = A; k < B; ++k) h[k] = ld16(rs, ho + k * 1024);
      if (!h_bad<A, B>(h)) break;
    }
  }
}

NOPK __global__ void __launch_bounds__(512, 2) fused_kernel(Params) {
  __shared__ __attribute__((aligned(16))) char smem[163840];
  asm volatile("" :: "v"((u32)(uintptr_t)(LAS char*)smem));
  constexpr u32 L0 = 0;
  const u64 kap = (u64)__builtin_amdgcn_kernarg_segment_ptr();
  u64x8 k0, k1; u64x4 k2;
  asm volatile("s_load_dwordx16 %0, %3, 0x0\n\ts_load_dwordx16 %1, %3, 0x40\n\ts_load_dwordx8 %2, %3, 0x80"
               : "=&s"(k0), "=&s"(k1), "=&s"(k2) : "s"(kap));
  lds_st<u32>(L0 + L_CNT + (threadIdx.x & 31) * 4, 0u);
  asm volatile("s_waitcnt lgkmcnt(0)\n\ts_barrier" : "+s"(k0), "+s"(k1), "+s"(k2) :: "memory");
  struct { const u16 *x, *Lre, *Lim, *Bre, *Bim, *Cre, *Cim, *ldtA, *DA, *Are, *Aim, *sBre, *sBim, *sCre, *sCim, *ldtB, *DB, *gw; float* out; char* ws; }
  const P = {(const u16*)k0[0], (const u16*)k0[1], (const u16*)k0[2], (const u16*)k0[3], (const u16*)k0[4], (const u16*)k0[5], (const u16*)k0[6],
             (const u16*)k0[7], (const u16*)k1[0], (const u16*)k1[1], (const u16*)k1[2], (const u16*)k1[3], (const u16*)k1[4], (const u16*)k1[5],
             (const u16*)k1[6], (const u16*)k1[7], (const u16*)k2[0], (const u16*)k2[1], (float*)k2[2], (char*)k2[3]};
  const int tid = threadIdx.x, lane = tid & 63;
  const int w = __builtin_amdgcn_readfirstlane(tid >> 6);
  const int b = blockIdx.x, xcd = b & 7, r = b >> 3;
  const int c = 4 * (xcd >> 1) + (r >> 3), j = 8 * (xcd & 1) + (r & 7);
  const int ti = 4 * xcd + (r >> 3), dj = r & 7;
  const int i16 = lane & 15, g = lane >> 4;
  const rsrc_t rws = mksrd(P.ws, WS_END);
  const rsrc_t rx = mksrd(P.x, 2048 * 1280);
  const rsrc_t rCre = mksrd(P.Cre, 640 * 1280), rCim = mksrd(P.Cim, 640 * 1280);
  const rsrc_t rka = mksrd((const void*)kap, sizeof(Params));
  constexpr u32 SEQ = __builtin_offsetof(Params, seq);
  const int kq = w & 3, kh = w >> 2;
  const int rot10 = ((r & 7) + 2 * (r >> 3) + 3 * xcd) % 10;
  u32 tag;
  STAMP(0);
  TR(0);
  const u32 cf0 = i16 * 128 + ((g ^ ((i16 >> 1) & 7)) << 4);
  const u32 ccf = L0 + C_CFULL;
  const u32 fho = lane < 8 ? WS_FH + (u32)((ti >> 1) * 16 + 8 * kh + lane) * 128 : OOB;
  const u32 ho = WS_H + (u32)((4 * ti + kq) * 40 + 20 * kh) * 1024 + lane * 16;
  const u32 ima = L0 + (kh ? L_IMG1 : L_IMG0) + (16 * kq + i16) * 320 + (g ^ ((i16 >> 1) & 3)) * 16;
#define G2_REGS(name, n, at)                                                                                \
  CF name[2 * n];                                                                                           \
  { const u32 a0 = opaque(L0 + (at) + cf0), a1 = opaque(L0 + (at) + (cf0 ^ 64));                            \
    _Pragma("unroll") for (int k = 0; k < 2 * n; ++k)                                                       \
    _Pragma("unroll") for (int nt = 0; nt < 5; ++nt) name[k].f[nt] = lds_ld<u32x4>(((k & 1) ? a1 : a0) + (k >> 1) * 10240 + nt * 2048); \
    _Pragma("unroll") for (int k = 0; k < 2 * n; ++k)                                                       \
      asm volatile("s_waitcnt lgkmcnt(0)" : "+v"(name[k].f[0]), "+v"(name[k].f[1]), "+v"(name[k].f[2]), "+v"(name[k].f[3]), "+v"(name[k].f[4]) :: "memory"); }
#define G2_LOADH()                                                                                          \
  u32x4 hf_[20];                                                                                            \
  _Pragma("unroll") for (int ks = 0; ks < 20; ++ks) hf_[ks] = ld16(rws, ho + ks * 1024);                    \
  SB();
#define G2_INIT(at)                                                                                         \
  f32x4 y[5];                                                                                               \
  _Pragma("unroll") for (int nt = 0; nt < 5; ++nt) y[nt] = f32x4{0.f, 0.f, 0.f, 0.f};                       \
  CF P0, P1;                                                                                                \
  const u32 cab[2][2] = {{opaque(L0 + (at) + cf0), opaque(L0 + (at) + (cf0 ^ 64))},                         \
                         {opaque(L0 + (at) + 40960 + cf0), opaque(L0 + (at) + 40960 + (cf0 ^ 64))}};        \
  asm volatile("s_nop 1" : "+v"(y[0]), "+v"(y[1]), "+v"(y[2]), "+v"(y[3]), "+v"(y[4]));
#define G2_U(uu, slot, need)                                                                                \
  { const u32 t_ = peek_issue(ccf);                                                                         \
    g2_half<((slot) & 3) * 10240, 5>(y, P1, P0, hf_[2 * (uu)], cab[(slot) >> 2][1]);                        \
    if (RARE(peek_get(t_) < (need))) sw_wait(ccf, need);                                                    \
    g2_half<(((slot) + 1) & 3) * 10240, 4>(y, P0, P1, hf_[2 * (uu) + 1], cab[((slot) + 1) >> 2][0]); }
#define G2_UN(uu, slot)                                                                                     \
  g2_half<((slot) & 3) * 10240, 4>(y, P1, P0, hf_[2 * (uu)], cab[(slot) >> 2][1]);                          \
  g2_half<(((slot) + 1) & 3) * 10240, 4>(y, P0, P1, hf_[2 * (uu) + 1], cab[((slot) + 1) >> 2][0]);
#define G2_ULAST(uu, slot)                                                                                  \
  g2_half<((slot) & 3) * 10240, 4>(y, P1, P0, hf_[2 * (uu)], cab[(slot) >> 2][1]);                          \
  g2_half0<true>(y, P1, hf_[2 * (uu) + 1]);
#define G2_B_IMAGE()                                                                                        \
  { STAMP(11);                                                                                              \
    sw_wait4(L0 + C_BAR, 4);                                                                                \
    const u32 ia = opaque(ima);                                                                             \
    _Pragma("unroll") for (int nt = 0; nt < 5; ++nt) lds_st<f32x4>(ia + nt * 64, y[nt]); }
  auto get_tag = [&]() INL { sw_wait(L0 + C_FULL, 1); tag = __builtin_amdgcn_readfirstlane(lds_ld<u32>(opaque(L0 + L_TAG))) + 1u; };

  const int pl = lane < 40 ? lane : 39;
  u32 pLre, pLim, pLdt; u32x4 gw0, gw1;
  if (w < 4) {
#pragma unroll
    for (int k = 0; k < 3; ++k) {
      const int qq = 64 * k + lane, t = (qq * 6554) >> 16, cc = qq - 10 * t;
      if (k < 2 || lane < 32) dma16(rx, (u32)(64 * ti + 16 * w + t) * 1280 + dj * 160 + cc * 16, 0, L0 + L_RAW + w * 2560 + k * 1024);
    }
    dma_mark(rka, SEQ, L0 + C_STRIP + 4 * w);
    pLre = ((const GAS u16*)P.Lre)[40 * j + pl]; pLim = ((const GAS u16*)P.Lim)[40 * j + pl]; pLdt = ((const GAS u16*)P.ldtA)[40 * j + pl];
    gw0 = *(const GAS u32x4*)((const GAS char*)P.gw + rot10 * 128 + g * 16); gw1 = *(const GAS u32x4*)((const GAS char*)P.gw + rot10 * 128 + 64 + g * 16);
    asm volatile("" ::: "memory");
  }
  if (__builtin_expect(w == 7, 1)) {
      __builtin_amdgcn_s_setprio(1);
      const rsrc_t rBre = mksrd(P.Bre, 640 * 1280), rBim = mksrd((const char*)P.Bim - 1024, 640 * 1280 + 1024);
      const int q8 = lane >> 3;
      u32 vx[4], vb[10];
      {
        const u32 x0 = (u32)(128 * c + 8 * (q8 >> 2) + (q8 & 3)) * 1280 + (((lane & 7) ^ (q8 >> 1)) << 4);
        vx[0] = x0; vx[1] = (x0 ^ 64) + 16 * 1280 - 1024; vx[2] = x0 + 4 * 1280 - 2048; vx[3] = (x0 ^ 64) + 20 * 1280 - 3072;
        const u32 b0 = (u32)(40 * j + q8) * 1280 + (((lane & 7) ^ (q8 & 6)) << 4), b1 = (b0 ^ 16) + 1024;
#pragma unroll
        for (int k = 0; k < 10; ++k) vb[k] = (k < 5 ? b0 : b1) + 10240 * (k % 5) - 1024 * (k & 3);
      }
      u32 so = rot10 * 128;
      {
        u64 keep;
        asm volatile("s_mov_b32 m0, %1\n\ts_mov_b64 %0, exec\n\ts_mov_b64 exec, 1\n\tbuffer_load_dword off, %2, 0 sc1 lds\n\ts_mov_b64 exec, %0"
                     : "=&s"(keep) : "s"(L0 + L_TAG), "s"(rws) : "m0", "memory");
      }
      auto head = [&](u32 sl) INL {
        dmaq<0>(rx, rx, vx[0], vx[1], vx[2], vx[3], so, sl);
        dmaq<4096>(rx, rx, vx[0], vx[1], vx[2], vx[3], so + 40960, sl);
        dmaq<8192>(rx, rx, vx[0], vx[1], vx[2], vx[3], so + 81920, sl);
        dmaq<12288>(rx, rx, vx[0], vx[1], vx[2], vx[3], so + 122880, sl);
        dmaq<16384>(rBre, rBre, vb[0], vb[1], vb[2], vb[3], so, sl);
      };
      auto tail = [&](u32 sl) INL {
        dmaq<20480>(rBre, rBim, vb[4], vb[5], vb[6], vb[7], so, sl);
        dmad<24576>(rBim, vb[8], vb[9], so, sl);
        so = so == 1152 ? 0 : so + 128;
      };
#pragma unroll
      for (int s = 0; s < 10; ++s) {
        const u32 sb = L0 + ring(s);
        if (s == 5) sw_wait4(L0 + C_PRO, 1);
        if (s == 6 || s == 8) sw_wait4(L0 + C_FREE, s - 4);
        TR(4 + 4 * s);
        if (s >= 2) waitv<36>();
        TR(5 + 4 * s);
        head(sb);
        tail(sb);
        TR(6 + 4 * s);
        dma_mark(rka, SEQ + 4 * s, L0 + C_FULL);
      }
      STAMP(2);
      u32 vc[4];
#pragma unroll
      for (int k = 0; k < 4; ++k) vc[k] = (u32)(80 * dj + q8) * 1280 + (((lane & 7) ^ (lane >> 4) ^ (4 * (k & 1))) << 4) + 9216 * k;
      auto stageC = [&](int u, u32 dst) INL {
        const rsrc_t rs = (u & 1) ? rCim : rCre;
        const u32 so = (u32)(u >> 1) * 128;
        dma64v(rs, vc[0], vc[1], vc[2], vc[3], so, dst);
        dma64v(rs, vc[0], vc[1], vc[2], vc[3], so + 40960, dst + 4096);
        dma32v(rs, vc[0], vc[1], so + 81920, dst + 8192);
      };
      {
        u32 t0 = 0;
#pragma unroll
        for (int k = 0; k < 7; ++k) {
          const u32 ln = min(r * 400 + k * 64 + lane, r * 400 + 399);
          asm volatile("buffer_load_dword %0, %1, %2, 0 offen" : "+v"(t0) : "v"((ln >= 6400 ? ln - 6400 : ln) * 128), "s"(r >= 16 ? rCim : rCre) : "memory");
        }
        const u32 hb = WS_H + (u32)(8 * c + (lane >> 5)) * 40960 + (g & 1) * 2048 + i16 * 16;
#pragma unroll
        for (int nt = 0; nt < 5; ++nt) {
          const int o = 5 * j + nt;
#pragma unroll
          for (int k = 0; k < 4; ++k)
            st16(rws, hb + k * 81920 + (u32)((o >> 3) * 4096 + ((o >> 2) & 1) * 1024 + (o & 3) * 256), u32x4{~0u, ~0u, ~0u, ~0u});
        }
        get_tag();
        asm volatile("s_waitcnt vmcnt(0)" : "+v"(t0) :: "memory");
        st8(rws, lane == 0 ? WS_FH + (u32)(c * 16 + j) * 128 : OOB, u32x2{tag, tag ^ FMIX});
        post(L0 + C_CLR, 1u);
        post(L0 + C_BPREP + 12, 1u);
      }
      poll_flags(rws, fho, tag);
      sw_wait4(L0 + C_BPREP, 1);
      sw_wait4(L0 + C_BAR, 2);
      stageC(10, L0 + L_A0 + 5 * 10240); stageC(11, L0 + L_A0 + 6 * 10240); stageC(12, L0 + L_A0 + 7 * 10240);
      stageC(0, L0 + L_A0 + 3 * 10240); stageC(1, L0 + L_A0 + 4 * 10240);
      dma_mark(rka, SEQ, ccf);
      stageC(4, L0 + L_A0 + 2 * 10240);
      sw_wait(ccf, 1);
      G2_REGS(cr, 3, L_A0 + 5 * 10240);
      post(L0 + C_BDONE + 12, 1u);
      sw_wait4(L0 + C_BAR, 3);
      stageC(2, L0 + L_A0); stageC(3, L0 + L_A0 + 10240);
      dma_mark(rka, SEQ + 4, ccf);
      STAMP(3);
      sw_wait4(L0 + C_C0, 1);
      waitv<30>();
      stageC(5, L0 + L_A0 + 3 * 10240); stageC(6, L0 + L_A0 + 4 * 10240);
      dma_mark(rka, SEQ + 8, ccf);
      waitv<21>();
      STAMP(8);
      u32x4 hf_[20];
#pragma unroll
      for (int ks = 0; ks < 20; ++ks)
        asm volatile("buffer_load_dwordx4 %0, %1, %2, %3 offen offset:%4 sc1" : "=v"(hf_[ks]) : "v"(ho), "s"(rws), "s"((ks >> 2) * 4096), "n"((ks & 3) * 1024) : "memory");
      G2_INIT(L_B0);
      sw_wait4(L0 + C_BDONE, 1);
      STAMP(4);
#define FILL(q, unit, at) stageC(unit, L0 + (at)); dma_mark(rka, SEQ + 4 * (3 + (q)), ccf);
      FILL(0, 13, L_B0); FILL(1, 14, L_B0 + 10240); waitv<50>(); FILL(2, 7, L_A0 + 5 * 10240);
      asm volatile("s_waitcnt vmcnt(33)" : "+v"(hf_[0]), "+v"(hf_[1]), "+v"(hf_[2]), "+v"(hf_[3]), "+v"(hf_[4]), "+v"(hf_[5]), "+v"(hf_[6]), "+v"(hf_[7]), "+v"(hf_[8]), "+v"(hf_[9]) :: "memory");
      asm volatile("" : "+v"(hf_[10]), "+v"(hf_[11]), "+v"(hf_[12]), "+v"(hf_[13]), "+v"(hf_[14]), "+v"(hf_[15]), "+v"(hf_[16]), "+v"(hf_[17]), "+v"(hf_[18]), "+v"(hf_[19]));
      FILL(3, 15, L_B0 + 2 * 10240);
      h_check<0, 20>(hf_, rws, ho);
      FILL(4, 8, L_A0 + 6 * 10240);
      g2_half0<false>(y, cr[0], hf_[0]); g2_half0<false>(y, cr[1], hf_[1]);
      waitv<40>(); FILL(5, 16, L_B0 + 3 * 10240);
      g2_half0<false>(y, cr[2], hf_[2]); g2_half0<false>(y, cr[3], hf_[3]);
      waitv<40>(); FILL(6, 9, L_A0 + 7 * 10240);
      g2_half0<false>(y, cr[4], hf_[4]); g2_half<0, 4>(y, P0, cr[5], hf_[5], cab[0][0]);
      waitv<40>(); FILL(7, 17, L_B0 + 4 * 10240);
      G2_UN(3, 0);
      waitv<40>(); FILL(8, 18, L_B0 + 5 * 10240);
      G2_UN(4, 1);
      waitv<40>(); FILL(9, 19, L_B0 + 6 * 10240);
      STAMP(5);
      __builtin_amdgcn_s_setprio(0);
      G2_U(5, 2, 9); G2_U(6, 3, 11); G2_U(7, 4, 12); G2_U(8, 5, 13); G2_ULAST(9, 6);
      G2_B_IMAGE();
  } else if (__builtin_expect(w < 4, 1)) {
    __builtin_amdgcn_s_setprio(2);
    f32x4 acc[2][5], accg[2] = {f32x4{0.f, 0.f, 0.f, 0.f}, f32x4{0.f, 0.f, 0.f, 0.f}};
#pragma unroll
    for (int m = 0; m < 2; ++m)
#pragma unroll
      for (int nt = 0; nt < 5; ++nt) acc[m][nt] = f32x4{0.f, 0.f, 0.f, 0.f};
    asm volatile("" : G1_ACC(acc));
    sw_wait4(L0 + C_STRIP, 1);
    STAMP(14);
    u32x2 xq[5];
    {
      const u32 ra = opaque(L0 + L_RAW + (32 * (g & 1) + 8 * (lane & 3) + ((lane >> 2) & 3)) * 160 + 40 * w + 8 * (g >> 1));
      u32x2 lo[3], hi[3];
#pragma unroll
      for (int k = 0; k < 3; ++k) { lo[k] = __builtin_bit_cast(u32x2, lds_tr(ra + k * 16)); hi[k] = __builtin_bit_cast(u32x2, lds_tr(ra + k * 16 + 640)); }
      const u32 xr = opaque(L0 + L_RAW + (16 * w + i16) * 160 + 8 * g);
#pragma unroll
      for (int nt = 0; nt < 5; ++nt) xq[nt] = lds_ld<u32x2>(xr + 32 * nt);
      const u32 xo = WS_XT + (u32)(20 * dj + 5 * w + (g >> 1)) * 16384 + (8 * ti + 4 * (g & 1)) * 64 + i16 * 16;
      waitl();
      dma_mark(rka, SEQ, L0 + C_PRO + 4 * w);
#pragma unroll
      for (int k = 0; k < 3; ++k) st16(rws, (k < 2 || lane < 32) ? xo + k * 32768 : OOB, u32x4{lo[k].x, lo[k].y, hi[k].x, hi[k].y});
    }
    STAMP(15);
    asm volatile("" : "+v"(pLre), "+v"(pLim), "+v"(pLdt));
    const float lre = bf2f(pLre), lim = bf2f(pLim);
    const float dtA = rbf(expf(bf2f(pLdt)));
    const float aA = dtA * lre, tA = dtA * lim;
    const f32x2 lam = cpowk(aA, tA, 1.f), l8 = cpowk(aA, tA, 8.f);
    f32x2 c2;
    {
      const float nr = lam.x - 1.f, ni = lam.y, den = 2.f * __builtin_amdgcn_rcpf(lre * lre + lim * lim);
      c2 = f32x2{(nr * lre + ni * lim) * den, (ni * lre - nr * lim) * den};
    }
    f32x2 zs[4], zh[4];
    zs[3] = cpowk(aA, tA, (float)(96 - 32 * w));
    zh[0] = cmul(cpowk(aA, tA, (float)(32 * w)), c2);
#pragma unroll
    for (int k = 1; k < 4; ++k) { zs[3 - k] = cmul(zs[4 - k], l8); zh[k] = cmul(zh[k - 1], l8); }
    const int sig = 4 * w + g;
    float lr[5], ls[5], sa[5], sb[5], ha[5], hb[5], ca[5], cb[5];
#pragma unroll
    for (int k = 0; k < 4; ++k) { zh[k].y = -zh[k].y; asm volatile("" : "+v"(zs[k]), "+v"(zh[k])); }
    f32x4 q0[5], q1[5];
    const u32 tb = opaque(L0 + L_TAB + w * 3200 + pl * 16), tp = opaque(L0 + L_TAB + w * 3200 + (i16 >> 1) * 16), tg = opaque(tp + g * 640);
    const int nvalid = (c - w + 3) >> 2;
    f32x2 lam4 = cpowk(aA, tA, 512.f);
    f32x2 lame = cpowk(aA, tA, (float)(128 * ((c - 1 - w) & 3)));
    asm volatile("" : "+v"(lam4), "+v"(lame));
    STAMP(1);
    const u32 ax0 = L0 + (32 * w + i16) * 128 + ((g ^ ((i16 >> 1) & 7)) << 4);
    const u32 ab0 = L0 + 16384 + (40 * (i16 & 1) + (i16 >> 1)) * 128 + ((g ^ (2 * (i16 >> 2) + (i16 & 1))) << 4);
    const u32 ax1 = ax0 ^ 64, ab1 = ab0 ^ 64;
    const u32 cfull = opaque(L0 + C_FULL), cfree = opaque(L0 + C_FREE);
    const bool gater = (xcd & 1) ? ((r & 7) >= 2 && (r & 7) < 7) : (r & 7) < 5;
    Frag X, Y;
    sw_wait(cfull, 1);
    const u32 seed = lds_ld<u32>(opaque(L0 + L_TAG));
    {
      const u32 pa = opaque(ax0 + L_A0), pb = opaque(ab0 + L_A0);
      X.a0 = lds_ld<u32x4>(pa); X.b0 = lds_ld<u32x4>(pb); X.a1 = lds_ld<u32x4>(pa + 2048); X.b1 = lds_ld<u32x4>(pb + 1024);
      X.b2 = lds_ld<u32x4>(pb + 2048); X.b3 = lds_ld<u32x4>(pb + 3072); X.b4 = lds_ld<u32x4>(pb + 4096);
    }
    if (gater) {
      accg[0] = mfma(lds_ld<u32x4>(ax1 + L_A0), gw1, mfma(X.a0, gw0, accg[0]));
      accg[1] = mfma(lds_ld<u32x4>(ax1 + L_A0 + 2048), gw1, mfma(X.a1, gw0, accg[1]));
    }
    asm volatile("s_waitcnt lgkmcnt(0)" : "+v"(X.a0), "+v"(X.a1), "+v"(X.b0), "+v"(X.b1), "+v"(X.b2), "+v"(X.b3), "+v"(X.b4));
    tag = __builtin_amdgcn_readfirstlane(seed) + 1u;
    const u32 fxo_ = lane == 0 ? WS_FX + (u32)((ti * 8 + dj) * 4 + w) * 128 : OOB;
    const u32x2 fxv = {tag, tag ^ FMIX};
    u32 so = L_A0;
#pragma unroll 1
    for (int s = 0; s < 8; s += 2) {
      u32 have = g1_half_peek(acc, Y, X, ax1 + so, ab1 + so, cfull);
      so = s == 2 ? 0 : so + STG;
      TR(4 + 4 * s);
      if (RARE(have < (u32)(s + 2))) sw_wait(cfull, s + 2);
      g1_half<true>(acc, X, Y, ax0 + so, ab0 + so);
      have = g1_half_peek(acc, Y, X, ax1 + so, ab1 + so, cfull);
      so = s == 4 ? L_A0 : so + STG;
      TR(5 + 4 * s);
      if (RARE(have < (u32)(s + 3))) sw_wait(cfull, s + 3);
      g1_half<true>(acc, X, Y, ax0 + so, ab0 + so);
      TR(6 + 4 * s);
      dma_mark(rka, SEQ + 4 * s + 4, L0 + C_FREE + 4 * w);
      if (RARE(s == 0)) { waitv<1>(); st8(rws, fxo_, fxv); }
    }
    {
      const u32 have = g1_half_peek(acc, Y, X, ax1 + ring(8), ab1 + ring(8), cfull);
      if (RARE(have < 10u)) sw_wait(cfull, 10);
      sw_wait4(cfree, 6);
#pragma unroll
      for (int k = 0; k < 4; ++k) lds_st<f32x4>(tb + k * 640, f32x4{zs[k].x, zs[k].y, zh[k].x, zh[k].y});
      lds_st<f32x4>(tb + 4 * 640, f32x4{lam.x, lam.y, c2.x, -c2.y});
      g1_half<false>(acc, X, Y, ax0 + ring(9), ab0 + ring(9));
#pragma unroll
      for (int nt = 0; nt < 5; ++nt) { q1[nt] = lds_ld<f32x4>(tp + 4 * 640 + nt * 128); q0[nt] = lds_ld<f32x4>(tg + nt * 128); }
      asm volatile("s_waitcnt lgkmcnt(10)" ::: "memory");
      g1_half<false>(acc, Y, X, ax1 + ring(9), ab1 + ring(9));
      g1_last(acc, Y);
      waitv<0>();
      post(L0 + C_FREE + 4 * w, 10u);
    }
    const u32x4 fr10 = peek4(cfree);
    {
      const float sg = (lane & 1) ? 1.f : -1.f;
#pragma unroll
      for (int nt = 0; nt < 5; ++nt) {
        sa[nt] = q0[nt].x; sb[nt] = sg * q0[nt].y; ha[nt] = -sg * q0[nt].z; hb[nt] = q0[nt].w;
        lr[nt] = q1[nt].x; ls[nt] = sg * q1[nt].y; ca[nt] = -sg * q1[nt].z; cb[nt] = q1[nt].w;
      }
    }
    STAMP(2);
    TR(44);
    float v[8][5];
#pragma unroll
    for (int jj = 0; jj < 8; ++jj)
#pragma unroll
      for (int nt = 0; nt < 5; ++nt) v[jj][nt] = acc[jj >> 2][nt][jj & 3];
    float e[5];
#pragma unroll
    for (int nt = 0; nt < 5; ++nt) e[nt] = v[0][nt];
#pragma unroll
    for (int jj = 1; jj < 8; ++jj) scan_step(e, v[jj], e, lr, ls);
    {
      float z[5] = {0.f, 0.f, 0.f, 0.f, 0.f}, we[5];
      scan_step(we, z, e, sa, sb);
      auto s01 = __builtin_amdgcn_permlane32_swap(f2u(we[0]), f2u(we[1]), false, false);
      auto s23 = __builtin_amdgcn_permlane32_swap(f2u(we[2]), f2u(we[3]), false, false);
      auto s44 = __builtin_amdgcn_permlane32_swap(f2u(we[4]), f2u(we[4]), false, false);
      const float y01 = u2f(s01[0]) + u2f(s01[1]), y23 = u2f(s23[0]) + u2f(s23[1]), y4 = u2f(s44[0]) + u2f(s44[1]);
      auto q03 = __builtin_amdgcn_permlane16_swap(f2u(y01), f2u(y23), false, false);
      auto q44 = __builtin_amdgcn_permlane16_swap(f2u(y4), f2u(y4), false, false);
      if (RARE(min4(fr10) < 10u)) sw_wait4(cfree, 10);
      const u32 wa = opaque(L0 + L_WS + w * 320 + i16 * 4);
      lds_st<float>(wa + (2 * (g & 1) + (g >> 1)) * 64, u2f(q03[0]) + u2f(q03[1]));
      lds_st<float>(wa + 256, u2f(q44[0]) + u2f(q44[1]));
      const u32 ra = opaque(L0 + L_ERAW + sig * 320 + i16 * 4);
#pragma unroll
      for (int nt = 0; nt < 5; ++nt) lds_st<float>(ra + nt * 64, e[nt]);
    }
    post(L0 + C_BAR + 4 * w, 1u);
    sw_wait4(L0 + C_BAR, 1);
    if (w == 2) {
      const u32 ap = opaque(L0 + L_WS + pl * 8);
      const f32x2 se = (lds_ld<f32x2>(ap) + lds_ld<f32x2>(ap + 320)) + (lds_ld<f32x2>(ap + 640) + lds_ld<f32x2>(ap + 960));
      st16(rws, lane < 40 ? WS_SG + (u32)(c * 640 + 40 * j + lane) * 16 : OOB, u32x4{f2u(se.x), tag, f2u(se.y), tag});
    }
    STAMP(3);
    if (gater) {
      float gl = accg[0][0];
#pragma unroll
      for (int k = 1; k < 8; ++k) gl = (i16 == k) ? accg[k >> 2][k & 3] : gl;
      st8(rws, i16 < 8 ? WS_GG + (u32)(rot10 * 2048 + 128 * c + 32 * w + 8 * g + i16) * 8 : OOB, u32x2{f2u(gl), tag});
    }
    float pf[5];
    {
      u32 era = opaque(L0 + L_ERAW + pl * 8);
      const f32x2 l8r = rot(l8);
      f32x2 pp = {0.f, 0.f};
#pragma unroll 1
      for (int s2 = 0; s2 < w; ++s2, era += 1280) {
        const f32x2 e0 = lds_ld<f32x2>(era), e1 = lds_ld<f32x2>(era + 320), e2 = lds_ld<f32x2>(era + 640), e3 = lds_ld<f32x2>(era + 960);
        pp = cmul(pp, l8, l8r) + e0; pp = cmul(pp, l8, l8r) + e1; pp = cmul(pp, l8, l8r) + e2; pp = cmul(pp, l8, l8r) + e3;
      }
      const f32x2 e0 = lds_ld<f32x2>(era), e1 = lds_ld<f32x2>(era + 320), e2 = lds_ld<f32x2>(era + 640);
      const u32 ptb = L0 + L_PT + w * 1280;
      const u32 pw = opaque(ptb + pl * 8);
#pragma unroll
      for (int k = 0; k < 4; ++k) {
        const f32x2 q = cmul(pp, c2);
        lds_st<f32x2>(pw + k * 320, f32x2{q.x, -q.y});
        if (k < 3) pp = cmul(pp, l8, l8r) + (k == 0 ? e0 : k == 1 ? e1 : e2);
      }
      const u32 pr_ = opaque(ptb + g * 320 + i16 * 4);
#pragma unroll
      for (int nt = 0; nt < 5; ++nt) pf[nt] = lds_ld<float>(pr_ + nt * 64);
      const float z[5] = {0.f, 0.f, 0.f, 0.f, 0.f};
#pragma unroll
      for (int jj = 0; jj < 8; ++jj) scan_step(v[jj], z, v[jj], ca, cb);
#pragma unroll
      for (int nt = 0; nt < 5; ++nt) ls[nt] = -ls[nt];
    }
    {
      u32 off[4];
#pragma unroll
      for (int k = 0; k < 4; ++k) off[k] = ((k < nvalid || c + w + k == 0) && lane < 40) ? WS_SG + (u32)((w + 4 * k) * 640 + 40 * j + lane) * 16 : OOB;
      u32x4 sv[4];
#pragma unroll 1
      for (int it = 0; it < SPIN_CAP; ++it) {
        asm volatile("" ::: "memory");
        bool ok = true;
#pragma unroll
        for (int k = 0; k < 4; ++k) sv[k] = ld16(rws, off[k]);
#pragma unroll
        for (int k = 0; k < 4; ++k) ok &= (off[k] == OOB) | ((sv[k].y == tag) & (sv[k].w == tag));
        if (wave_all(ok)) break;
        __builtin_amdgcn_s_sleep(1);
      }
      asm volatile("" ::: "memory");
      STAMP(4);
      f32x2 hsum = {0.f, 0.f};
      const f32x2 l4r = rot(lam4);
#pragma unroll
      for (int k = 0; k < 4; ++k)
        if (k < nvalid) hsum = cmul(hsum, lam4, l4r) + f32x2{u2f(sv[k].x), u2f(sv[k].z)};
      hsum = cmul(hsum, lame);
      const u32 hp = opaque(L0 + L_HP + lane * 32 + w * 4);
      if (lane < 40) {
        lds_st<float>(hp, hsum.x);
        lds_st<float>(hp + 16, hsum.y);
      }
    }
    post(L0 + C_BAR + 4 * w, 2u);
    const u32 clr = peek(L0 + C_CLR);
    sw_wait4(L0 + C_BAR, 2);
    {
      float hin[5], h0[5];
      const u32 hp = opaque(L0 + L_HP + i16 * 16);
#pragma unroll
      for (int nt = 0; nt < 5; ++nt) {
        const f32x4 q = lds_ld<f32x4>(hp + nt * 256);
        hin[nt] = (q.x + q.y) + (q.z + q.w);
      }
      scan_step(h0, pf, hin, ha, hb);
      scan_step(v[0], v[0], h0, lr, ls);
#pragma unroll
      for (int jj = 1; jj < 8; ++jj) scan_step(v[jj], v[jj], v[jj - 1], lr, ls);
    }
    {
      const u32 img = L0 + L_HT + w * 1280;
      const u32 wa = opaque(img + i16 * 80 + g * 16);
      const u32 ra = opaque(img + (2 * ((lane >> 2) & 3) + (g & 1)) * 80 + (g >> 1) * 32 + (lane & 3) * 8);
      const u32 hb0 = WS_H + (u32)(8 * c + 2 * w + (g >> 1)) * 40960 + (g & 1) * 2048 + i16 * 16;
      u32x2 lo[5], hi[5];
#pragma unroll
      for (int nt = 0; nt < 5; ++nt) {
        lds_st<u32x4>(wa, u32x4{pk(v[0][nt], v[1][nt]), pk(v[2][nt], v[3][nt]), pk(v[4][nt], v[5][nt]), pk(v[6][nt], v[7][nt])});
        lo[nt] = __builtin_bit_cast(u32x2, lds_tr(ra));
        hi[nt] = __builtin_bit_cast(u32x2, lds_tr(ra + 640));
      }
      SB();
      if (RARE(__builtin_amdgcn_readfirstlane(clr) == 0)) sw_wait(L0 + C_CLR, 1);
#pragma unroll
      for (int nt = 0; nt < 5; ++nt) {
        const int o = 5 * j + nt;
        st16(rws, hb0 + (u32)((o >> 3) * 4096 + ((o >> 2) & 1) * 1024 + (o & 3) * 256), u32x4{lo[nt].x, lo[nt].y, hi[nt].x, hi[nt].y});
      }
    }
    post(L0 + C_BAR + 4 * w, 3u);
    STAMP(5);
    u32x2 eda[5], edb[5];
#pragma unroll
    for (int nt = 0; nt < 5; ++nt) {
      eda[nt] = *(const GAS u32x2*)((const GAS u16*)P.DA + 80 * dj + 16 * nt + 4 * g);
      edb[nt] = *(const GAS u32x2*)((const GAS u16*)P.DB + 80 * dj + 16 * nt + 4 * g);
    }
    const u32 go = WS_GG + (u32)(g * 2048 + 64 * ti + 16 * kq + i16) * 8, go2 = g < 2 ? go + 8 * 2048 * 8 : OOB;
    u32x2 g0 = ld8(rws, go), g1 = ld8(rws, go + 4 * 2048 * 8), g2 = ld8(rws, go2);
    const u32x2 fhv = ld8(rws, fho);
    sw_wait(ccf, 1);
    G2_REGS(cr, 2, L_A0 + 3 * 10240);
    dma_mark(rka, SEQ, L0 + C_C0 + 4 * w);
#pragma unroll 1
    for (int it = 0; it < SPIN_CAP && RARE(!wave_all((g0.y == tag) & (g1.y == tag) & ((g2.y == tag) | (g >= 2)))); ++it) {
      __builtin_amdgcn_s_sleep(1);
      asm volatile("" ::: "memory");
      g0 = ld8(rws, go); g1 = ld8(rws, go + 4 * 2048 * 8); g2 = ld8(rws, go2);
    }
    float gate, ogate;
    {
      const float s3 = (u2f(g0.x) + u2f(g1.x)) + u2f(g2.x);
      auto sa_ = __builtin_amdgcn_permlane32_swap(f2u(s3), f2u(s3), false, false);
      const float s2 = u2f(sa_[0]) + u2f(sa_[1]);
      auto sb_ = __builtin_amdgcn_permlane16_swap(f2u(s2), f2u(s2), false, false);
      gate = rbf(1.f / (1.f + expf(-rbf(u2f(sb_[0]) + u2f(sb_[1])))));
      ogate = rbf(1.f - gate);
    }
    u32x2 ska[5], skb[5];
#pragma unroll
    for (int nt = 0; nt < 5; ++nt) {
#pragma unroll
      for (int e = 0; e < 2; ++e) {
        const float x0 = bflo(xq[nt][e]), x1 = bfhi(xq[nt][e]);
        ska[nt][e] = pk(x0 * bflo(eda[nt][e]), x1 * bfhi(eda[nt][e]));
        skb[nt][e] = pk(x0 * bflo(edb[nt][e]), x1 * bfhi(edb[nt][e]));
      }
      asm volatile("" : "+v"(ska[nt]), "+v"(skb[nt]));
    }
    waitv<1>();
    if (RARE(!flag_ok(fhv, fho, tag))) poll_flags(rws, fho, tag);
    asm volatile("" ::: "memory");
    __builtin_amdgcn_s_setprio(0);
    STAMP(6);
    G2_LOADH();
    STAMP(7);
    G2_INIT(L_A0);
    h_check<0, 4>(hf_, rws, ho);
    g2_half0<false>(y, cr[0], hf_[0]); g2_half0<false>(y, cr[1], hf_[1]); g2_half0<false>(y, cr[2], hf_[2]);
    sw_wait(ccf, 2);
    g2_half<0, 4>(y, P0, cr[3], hf_[3], cab[0][0]);
    STAMP(8);
    h_check<4, 10>(hf_, rws, ho);
    G2_UN(2, 0); G2_UN(3, 1); G2_U(4, 2, 3);
    h_check<10, 14>(hf_, rws, ho);
    G2_UN(5, 3);
    h_check<14, 20>(hf_, rws, ho);
    STAMP(9);
    G2_U(6, 4, 6);
    lds_st<float>(opaque(L0 + L_GATE + (16 * w + i16) * 4), gate);
    post(L0 + C_BAR + 4 * w, 4u);
    waitl();
    const u32 bo = WS_BT + (u32)(80 * dj + (lane >> 2)) * 8192 + (64 * ti + 16 * w) * 4 + (lane & 3) * 16;
    u32x4 bv[5];
#pragma unroll
    for (int k = 0; k < 5; ++k) bv[k] = ld16(rws, bo + k * 16 * 8192);
    SB();
    G2_U(7, 5, 8); G2_U(8, 6, 10); G2_ULAST(9, 7);
    STAMP(10);
#pragma unroll 1
    for (int it = 0; it < SPIN_CAP; ++it) {
      bool ok = true;
#pragma unroll
      for (int k = 0; k < 5; ++k) ok &= (bv[k].y == tag) & (bv[k].w == tag);
      if (!RARE(!wave_all(ok))) break;
      __builtin_amdgcn_s_sleep(2);
      asm volatile("" ::: "memory");
#pragma unroll
      for (int k = 0; k < 5; ++k) bv[k] = ld16(rws, bo + k * 16 * 8192);
    }
    STAMP(11);
    sw_wait4(L0 + C_BAR, 4);
    {
      const u32 ba = opaque(L0 + L_BST + w * 2560 + lane * 8), ia = opaque(ima);
#pragma unroll
      for (int k = 0; k < 5; ++k) lds_st<u32x2>(ba + k * 512, u32x2{bv[k].x, bv[k].z});
#pragma unroll
      for (int nt = 0; nt < 5; ++nt) {
        const u32x2 yb = __builtin_bit_cast(u32x2, lds_tr(ba + nt * 512));
        f32x4 o;
#pragma unroll
        for (int e = 0; e < 2; ++e) {
          const u32 bb = pk(bflo(yb[e]) + bflo(skb[nt][e]), bfhi(yb[e]) + bfhi(skb[nt][e]));
          const u32 ob = pk(ogate * bflo(bb), ogate * bfhi(bb));
          o[2 * e] = gate * (y[nt][2 * e] + bflo(ska[nt][e])) + bflo(ob);
          o[2 * e + 1] = gate * (y[nt][2 * e + 1] + bfhi(ska[nt][e])) + bfhi(ob);
        }
        lds_st<f32x4>(ia + nt * 64, o);
      }
    }
  } else {
    const int pr = xcd * 16 + (r >> 1);
    const int nch = (r & 1) ? 2 : 3, ch = 5 * pr + ((r & 1) ? 3 : 0) + kq;
    const u32 slot = opaque(L0 + L_B0 + kq * BSLOT) ;
    u32x2 fhv;
    f32x4 ya[8];
    if (__builtin_expect(kq < nch, 1)) {
      const GAS u32* pa = (const GAS u32*)P.Are + ch * 64 + lane;
      const u32 wAre = *pa, wAim = ((const GAS u32*)P.Aim)[ch * 64 + lane];
      const u32 wBre = ((const GAS u32*)P.sBre)[ch * 64 + lane], wBim = ((const GAS u32*)P.sBim)[ch * 64 + lane];
      const u32 wCre = ((const GAS u32*)P.sCre)[ch * 64 + lane], wCim = ((const GAS u32*)P.sCim)[ch * 64 + lane];
      const u32 wdt = ((const GAS u16*)P.ldtB)[ch];
      const float dt = rbf(expf(bf2f(wdt)));
      f32x4 ka[3] = {f32x4{0.f, 0.f, 0.f, 0.f}, f32x4{0.f, 0.f, 0.f, 0.f}, f32x4{0.f, 0.f, 0.f, 0.f}};
      const u32 tr = opaque(slot + i16 * TROW + g * 16);
      const u32 fxo = lane < 32 ? WS_FX + (u32)((lane * 8 + ch / 80) * 4 + (ch % 80) / 20) * 128 : OOB;
      u32 tv[56], tu[56];
#pragma unroll 1
      for (int rd = 0; rd < 2; ++rd) {
#pragma unroll
        for (int k = 0; k < 56; ++k) tu[k] = tv[k];
        const u32 sh = rd ? 0u : 16u;
        const float a = dt * bfhi(wAre << sh), t = dt * bfhi(wAim << sh);
        const f32x2 db = {dt * bfhi(wBre << sh), dt * bfhi(wBim << sh)}, cc = {bfhi(wCre << sh), bfhi(wCim << sh)};
        const f32x2 W = cmul(cc, db);
        const f32x2 z1 = cpowk(a, t, 1.f), z8 = cpowk(a, t, 8.f), z128 = cpowk(a, t, 128.f);
        {
          f32x2 cv = z8;
          tv[41] = pk(cv.x, cv.y);
#pragma unroll
          for (int jj = 2; jj < 16; ++jj) { cv = cmul(cv, z8); tv[40 + jj] = pk(cv.x, cv.y); }
        }
        {
          const f32x2 y1 = {z1.x, -z1.y}, yh = {z128.x, -z128.y};
          f32x2 R[8];
          R[0] = f32x2{W.x, -W.y};
#pragma unroll
          for (int k = 1; k < 8; ++k) R[k] = cmul(R[k - 1], y1);
#pragma unroll
          for (int k = 0; k < 8; ++k) tv[k] = pk(R[k].x, R[k].y);
#pragma unroll
          for (int k = 0; k < 8; ++k) { R[k] = cmul(R[k], yh); tv[8 + k] = pk(R[k].x, R[k].y); }
#pragma unroll
          for (int hh = 0; hh < 2; ++hh)
#pragma unroll
            for (int k = 0; k < 4; ++k) { R[2 * k] = cmul(R[2 * k], yh); tv[16 + 4 * hh + k] = pk(R[2 * k].x, R[2 * k].y); }
#pragma unroll
          for (int hh = 0; hh < 4; ++hh)
#pragma unroll
            for (int k = 0; k < 2; ++k) { R[4 * k] = cmul(R[4 * k], yh); tv[24 + 2 * hh + k] = pk(R[4 * k].x, R[4 * k].y); }
#pragma unroll
          for (int hh = 0; hh < 9; ++hh) { R[0] = cmul(R[0], yh); tv[32 + hh] = pk(R[0].x, R[0].y); }
        }
      }
      STAMP(1);
      get_tag();
      poll_flags(rws, fxo, tag);
      u32x4 uv[4];
#pragma unroll
      for (int k = 0; k < 4; ++k) uv[k] = ld16(rws, WS_XT + (u32)(ch >> 2) * 16384 + (ch & 3) * 16 + (64 * k + lane) * 64);
      const u32 uw = opaque(slot + UPAD + lane * 16);
      const u32 kc = slot + KC_OFF;
      const bool r1 = lane < 32;
      const u32 xa = opaque(kc - 4 + (r1 ? 16 * lane : 256 + 8 * lane)), xb = opaque(xa + (r1 ? 12 : 8));
      const u32 sA = opaque(r1 ? 0x05040706u : 0x03020504u), sB = opaque(r1 ? 0x05040706u : 0x05040504u), sC = opaque(r1 ? 0x03020504u : 0x01000302u),
                sD = opaque(r1 ? 0x03020504u : 0x05040504u), sE = opaque(lane <= 32 ? 0x03020504u : 0x05040504u);
      const u32 ka0 = opaque(kc + (128 * (g >> 1) + 8 * i16 + 4 * (g & 1)) * 2);
      const u32 ka1 = opaque(kc + (g < 2 ? 256 + 64 * g + 4 * i16 : 256 + 64 * g + 2 * i16) * 2), ka1b = opaque(ka1 + (g < 2 ? 4 : 64));
      const u32 ka2 = opaque(kc + (512 + 64 * min(g, 2) + i16) * 2);
      const u32 sbase = __builtin_amdgcn_readfirstlane(slot);
      sw_wait4(L0 + C_FREE, 10);
      STAMP(6);
#define TW(v, row) asm volatile("ds_write_addtid_b32 %0 offset:%1" :: "v"(v), "n"((row) * TROW) : "memory")
      asm volatile("s_mov_b32 m0, %0\n\ts_nop 0" :: "s"(sbase) : "m0", "memory");
      TW(0x00003f80u, 41);
#pragma unroll
      for (int rd = 0; rd < 2; ++rd) {
#pragma unroll
        for (int k = 0; k < 56; ++k) TW(rd ? tv[k] : tu[k], k < 41 ? k : k + 1);
#pragma unroll
        for (int ks = 0; ks < 4; ++ks) {
          const u32x4 bb = lds_ld<u32x4>(tr + 41 * TROW + ks * 64);
#pragma unroll
          for (int m = 0; m < 3; ++m) ka[m] = mfma(lds_ld<u32x4>(tr + 16 * m * TROW + ks * 64), bb, ka[m]);
        }
      }
#define UW(v, off) asm volatile("ds_write_addtid_b32 %0 offset:%1" :: "v"(v), "n"(off) : "memory")
#pragma unroll
      for (int cc = 0; cc < 4; ++cc) { UW(0u, cc * UCOPY); UW(0u, cc * UCOPY + 144); }
#pragma unroll
      for (int k = 0; k < 4; ++k) {
        lds_st<u32x4>(uw + k * 1024, uv[k]);
#pragma unroll
        for (int e = 0; e < 4; ++e) lds_st<u32>(uw + k * 1024 + UCOPY + 4 + 4 * e, uv[k][e]);
        lds_st<u32x2>(uw + k * 1024 + 2 * UCOPY + 8, u32x2{uv[k].x, uv[k].y});
        lds_st<u32x2>(uw + k * 1024 + 2 * UCOPY + 16, u32x2{uv[k].z, uv[k].w});
      }
      STAMP(5);
      {
        const u32 q0 = pk(ka[2].x, ka[2].y), q1 = pk(ka[2].z, ka[2].w);
        if (g < 3) { lds_st<u16>(ka2, (u16)q0); lds_st<u16>(ka2 + 32, (u16)(q0 >> 16)); lds_st<u16>(ka2 + 64, (u16)q1); lds_st<u16>(ka2 + 96, (u16)(q1 >> 16)); }
        lds_st<u32x2>(ka0, u32x2{pk(ka[0].x, ka[0].y), pk(ka[0].z, ka[0].w)});
        lds_st<u32>(ka1, pk(ka[1].x, ka[1].y));
        lds_st<u32>(ka1b, pk(ka[1].z, ka[1].w));
        lds_st<u32>(kc - 4, 0u);
      }
#define PERM(hi, lo, sel) __builtin_amdgcn_perm(hi, lo, sel)
      {
        const u32 R0 = lds_ld<u32>(xa), R1 = lds_ld<u32>(xa + 4), R2 = lds_ld<u32>(xa + 8), R3 = lds_ld<u32>(xb), R4 = lds_ld<u32>(xb + 4);
        const u32 ea_ = opaque(kc + 768 + 4 * lane), eb_ = opaque(kc + 1024 + 2 * lane);
        const u32 E0 = lds_ld<u32>(ea_), E1 = lds_ld<u32>(ea_ + 4);
        const u32 f0 = lds_ld<u16>(eb_), f1 = lds_ld<u16>(eb_ + 2), f2 = lds_ld<u16>(eb_ + 128), f3 = lds_ld<u16>(eb_ + 130);
        UW(0u, KOFF); UW(0u, KOFF + KCOPY);
        const u32 k0 = opaque(slot + KOFF + KPAD + lane * 16);
        lds_st<u32x4>(k0 + KCOPY, u32x4{PERM(R4, R3, sA), PERM(R3, R2, sB), PERM(R2, R1, sA), PERM(R1, R0, sB)});
        lds_st<u32x4>(k0, u32x4{PERM(R4, R3, sC), PERM(R3, R2, sD), PERM(R2, R1, sC), PERM(R1, R0, sE)});
        const u32 e00 = PERM(E0, E0, 0x01000100u), e11 = PERM(E0, E0, 0x03020302u);
        lds_st<u32x4>(k0 + KCOPY + 1024, u32x4{PERM(E1, E1, 0x01000100u), e11, PERM(E0, E0, 0x01000302u), e00});
        lds_st<u32x4>(k0 + 1024, u32x4{PERM(E0, E1, 0x07060100u), e11, e00, e00});
#pragma unroll
        for (int rr = 0; rr < 2; ++rr) {
          const u32 e0 = rr ? f2 : f0, e1 = rr ? f3 : f1, t4 = (lane & 1) ? e1 : e0;
          const u32 c00 = e0 | (e0 << 16), c11 = e1 | (e1 << 16);
          lds_st<u32x4>(k0 + KCOPY + 2048 + 1024 * rr, u32x4{c11, e1 | (t4 << 16), c00, c00});
          lds_st<u32x4>(k0 + 2048 + 1024 * rr, u32x4{c11, t4 | (e0 << 16), c00, c00});
        }
      }
#pragma unroll
      for (int k = 0; k < 16; ++k) lds_st<u32>(uw + (k >> 2) * 1024 + 3 * UCOPY + 12 + 4 * (k & 3), uv[k >> 2][k & 3]);
      post(L0 + C_BPREP + 4 * kq, 1u);
      STAMP(2);
#pragma unroll
      for (int A = 0; A < 8; ++A) ya[A] = f32x4{0.f, 0.f, 0.f, 0.f};
      const u32 ta = opaque(slot + KOFF + KPAD + (i16 & 1) * KCOPY + ((i16 >> 1) - g) * 16 - 64);
      const u32 ua = opaque(slot + (i16 & 3) * UCOPY + (i16 >> 2) * 128 + g * 16);
      u32x4 T[2][8], U[2][8];
#define CV_READ(S, st)                                                                                        \
      _Pragma("unroll") for (int D = 0; D < 8; ++D) {                                                         \
        T[S][D] = lds_ld<u32x4>(ta + ((st) >> 1) * 128 + D * 512 + (1 - ((st) & 1)) * 64);                   \
        U[S][D] = lds_ld<u32x4>(ua + UPAD - ((st) >> 1) * 128 + D * 512 + ((st) & 1) * 64);                  \
      }
      CV_READ(0, 0);
      __builtin_amdgcn_s_setprio(2);
#pragma unroll
      for (int st = 0; st < 8; ++st) {
        SB();
        if (st == 6) fhv = ld8(rws, fho);
        if (st < 7) { CV_READ((st + 1) & 1, st + 1); }
#pragma unroll
        for (int S = 0; S < 8; ++S)
#pragma unroll
          for (int D = 0; D <= S; ++D) ya[S] = mfma(T[st & 1][D], U[st & 1][S - D], ya[S]);
        if (st < 7) {
#pragma unroll
          for (int k = 0; k < 16; ++k) {
            __builtin_amdgcn_sched_group_barrier(0x8, 2, 0);
            __builtin_amdgcn_sched_group_barrier(0x100, 1, 0);
          }
          __builtin_amdgcn_sched_group_barrier(0x8, 4, 0);
        }
      }
      SB();
      STAMP(3);
    } else {
      get_tag();
      sw_wait4(L0 + C_FREE, 10);
      post(L0 + C_BPREP + 4 * kq, 1u);
      fhv = ld8(rws, fho);
#pragma unroll
      for (int A = 0; A < 8; ++A) ya[A] = f32x4{0.f, 0.f, 0.f, 0.f};
    }
    sw_wait(ccf, 1);
    G2_REGS(cr, 3, L_A0 + 5 * 10240);
    post(L0 + C_BDONE + 4 * kq, 1u);
    {
      const bool odd = lane & 1;
      const u32 bo = kq < nch ? WS_BT + (u32)ch * 8192 + (i16 >> 2) * 256 + g * 64 + (odd ? 0 : 32) + ((lane & 2) ? 0 : 16) : OOB;
#pragma unroll
      for (int A = 0; A < 8; ++A) {
        const u32 x0 = pk(ya[A].x, ya[A].y), x1 = pk(ya[A].z, ya[A].w);
        const u32 rv = __builtin_amdgcn_update_dpp(0u, odd ? x1 : x0, 0xB1, 0xf, 0xf, true);
        st16(rws, bo + A * 1024, u32x4{odd ? x0 : rv, tag, odd ? rv : x1, tag});
      }
    }
    if (RARE(!flag_ok(fhv, fho, tag))) poll_flags(rws, fho, tag);
    asm volatile("" ::: "memory");
    __builtin_amdgcn_s_setprio(0);
    STAMP(4);
    G2_LOADH();
    G2_INIT(L_B0);
    h_check<0, 6>(hf_, rws, ho);
    STAMP(8);
#pragma unroll
    for (int k = 0; k < 5; ++k) g2_half0<false>(y, cr[k], hf_[k]);
    sw_wait(ccf, 4);
    g2_half<0, 4>(y, P0, cr[5], hf_[5], cab[0][0]);
    STAMP(9);
    h_check<6, 12>(hf_, rws, ho);
    G2_U(3, 0, 5); G2_U(4, 1, 7); G2_U(5, 2, 9);
    h_check<12, 20>(hf_, rws, ho);
    G2_U(6, 3, 11); G2_U(7, 4, 12); G2_U(8, 5, 13); G2_ULAST(9, 6);
    G2_B_IMAGE();
  }

  hwbar();
  STAMP(12);
  if (b == 255 && tid == 0) __hip_atomic_store((GAS u32*)P.ws, tag, __ATOMIC_RELAXED, __HIP_MEMORY_SCOPE_AGENT);
  {
    const int et = tid >> 3, es = tid & 7;
    const u32 ia = opaque(L0 + L_IMG0 + et * 320 + ((es & 4) | ((es ^ (et >> 1)) & 3)) * 16);
    const float gt = lds_ld<float>(opaque(L0 + L_GATE + et * 4));
    const rsrc_t ro = mksrd(P.out, 2048 * 2560);
    const u32 oo = (64 * ti + et) * 2560 + dj * 320 + es * 16;
    const f32x4 o0 = lds_ld<f32x4>(ia) + gt * lds_ld<f32x4>(ia + (L_IMG1 - L_IMG0));
    const f32x4 o1 = lds_ld<f32x4>(ia + 128) + gt * lds_ld<f32x4>(ia + 128 + (L_IMG1 - L_IMG0));
    const f32x4 o2 = lds_ld<f32x4>(ia + 256) + gt * lds_ld<f32x4>(ia + 256 + (L_IMG1 - L_IMG0));
    __builtin_amdgcn_raw_buffer_store_b128(__builtin_bit_cast(u32x4, o0), ro, oo, 0, OAUX);
    __builtin_amdgcn_raw_buffer_store_b128(__builtin_bit_cast(u32x4, o1), ro, oo + 128, 0, OAUX);
    __builtin_amdgcn_raw_buffer_store_b128(__builtin_bit_cast(u32x4, o2), ro, es < 4 ? oo + 256 : OOB, 0, OAUX);
  }
  DUMP();
  STAMP(13);
}

extern "C" void kernel_launch(void* const* d_in, const int* in_sizes, int n_in, void* d_out, int out_size, void* d_ws,
                              size_t ws_size, hipStream_t stream) {
  Params p;
  p.x = (const u16*)d_in[0];
  p.Lre = (const u16*)d_in[1];
  p.Lim = (const u16*)d_in[2];
  p.Bre = (const u16*)d_in[3];
  p.Bim = (const u16*)d_in[4];
  p.Cre = (const u16*)d_in[5];
  p.Cim = (const u16*)d_in[6];
  p.ldtA = (const u16*)d_in[7];
  p.DA = (const u16*)d_in[8];
  p.Are = (const u16*)d_in[10];
  p.Aim = (const u16*)d_in[11];
  p.sBre = (const u16*)d_in[12];
  p.sBim = (const u16*)d_in[13];
  p.sCre = (const u16*)d_in[14];
  p.sCim = (const u16*)d_in[15];
  p.ldtB = (const u16*)d_in[16];
  p.DB = (const u16*)d_in[17];
  p.gw = (const u16*)d_in[18];
  p.out = (float*)d_out;
  p.ws = (char*)d_ws;
  for (int i = 0; i < 16; ++i) p.seq[i] = i + 1;
  fused_kernel<<<256, 512, 0, stream>>>(p);
}
```

```cpp
#include <hip/hip_runtime.h>
#include <stdint.h>

typedef unsigned short u16;
typedef unsigned int u32;
typedef unsigned long long u64;
typedef __bf16 bf16x8 __attribute__((ext_vector_type(8)));
typedef short s16x4 __attribute__((ext_vector_type(4)));
typedef float f32x4 __attribute__((ext_vector_type(4)));
typedef float f32x2 __attribute__((ext_vector_type(2)));
typedef u32 u32x4 __attribute__((ext_vector_type(4)));
typedef u32 u32x2 __attribute__((ext_vector_type(2)));
typedef u64 u64x8 __attribute__((ext_vector_type(8)));
typedef u64 u64x4 __attribute__((ext_vector_type(4)));
typedef __bf16 bf16x2_t __attribute__((ext_vector_type(2)));
typedef __amdgpu_buffer_rsrc_t rsrc_t;
#define LAS __attribute__((address_space(3)))
#define GAS __attribute__((address_space(1)))
#define DEV __device__ __forceinline__
#define INL __attribute__((always_inline))
#if defined(__HIP_DEVICE_COMPILE__)
#define NOPK __attribute__((target("no-packed-fp32-ops")))
#else
#define NOPK
#endif

constexpr u32 WS_E = 0;
constexpr u32 WS_FH = 4096;
constexpr u32 WS_FX = WS_FH + 256 * 128;
constexpr u32 WS_SG = WS_FX + 1024 * 128;
constexpr u32 WS_GG = WS_SG + 16 * 640 * 16;
constexpr u32 WS_XT = WS_GG + 10 * 2048 * 8;
constexpr u32 WS_BT = WS_XT + 640 * 2048 * 2;
constexpr u32 WS_H = WS_BT + 640 * 1024 * 8;
constexpr u32 WS_END = WS_H + 128 * 40 * 1024;
constexpr u32 WS_DIAG = (WS_END + 4095) & ~4095u;

constexpr u32 STG = 26624;
constexpr u32 L_A0 = 81920;
constexpr u32 L_B0 = 0;
__attribute__((always_inline)) constexpr u32 ring(int s) { return s % 6 < 3 ? L_A0 + (s % 6) * STG : (s % 6 - 3) * STG; }
constexpr u32 BSLOT = 27008;
constexpr u32 L_CNT = 81024;
constexpr u32 C_FREE = L_CNT, C_PRO = L_CNT + 16, C_BAR = L_CNT + 32, C_BDONE = L_CNT + 48, C_C0 = L_CNT + 64, C_BPREP = L_CNT + 80,
              C_STRIP = L_CNT + 96, C_FULL = L_CNT + 112, C_CFULL = L_CNT + 116, C_CLR = L_CNT + 120, L_TAG = L_CNT + 128;
constexpr u32 L_GATE = L_CNT + 256;
constexpr u32 L_RAW = ring(5);
constexpr u32 L_TAB = ring(4);
constexpr u32 L_ERAW = L_A0;
constexpr u32 L_WS = L_A0 + 5120;
constexpr u32 L_HP = L_A0 + 6400;
constexpr u32 L_PT = L_A0 + 7680;
constexpr u32 L_HT = L_A0 + 12800;
constexpr u32 L_BST = L_A0;
constexpr u32 L_IMG0 = L_A0 + 10240, L_IMG1 = L_A0 + 30720;
constexpr u32 UCOPY = 4640, UPAD = 384, KOFF = 4 * UCOPY, KCOPY = 4224, KPAD = 112;
constexpr u32 KC_OFF = 16640;
constexpr u32 TROW = 288;

constexpr int SPIN_CAP = 1 << 16;

template <bool B> struct BoolC { static constexpr bool value = B; };

struct Params {
  const u16 *x, *Lre, *Lim, *Bre, *Bim, *Cre, *Cim, *ldtA, *DA;
  const u16 *Are, *Aim, *sBre, *sBim, *sCre, *sCim, *ldtB, *DB, *gw;
  float* out;
  char* ws;
  u32 seq[16];
};

DEV bool wave_all(bool p) { return __builtin_amdgcn_ballot_w64(!p) == 0; }
DEV float bf2f(u32 v) { return __builtin_bit_cast(float, v << 16); }
DEV float bflo(u32 v) { return __builtin_bit_cast(float, v << 16); }
DEV float bfhi(u32 v) { return __builtin_bit_cast(float, v & 0xffff0000u); }
DEV u32 pk(float a, float b) { bf16x2_t v = {(__bf16)a, (__bf16)b}; return __builtin_bit_cast(u32, v); }
DEV float rbf(float a) { return bflo(pk(a, a)); }
DEV u32 f2u(float a) { return __builtin_bit_cast(u32, a); }
DEV float u2f(u32 a) { return __builtin_bit_cast(float, a); }
DEV float swp(float x) { return u2f(__builtin_amdgcn_update_dpp(0, f2u(x), 0xB1, 0xf, 0xf, true)); }
DEV f32x2 rot(f32x2 z) { return f32x2{-z.y, z.x}; }
DEV f32x2 cmul(f32x2 c, f32x2 z, f32x2 zr) { return __builtin_elementwise_fma(f32x2{c.y, c.y}, zr, f32x2{c.x, c.x} * z); }
DEV f32x2 cmul(f32x2 c, f32x2 z) { return cmul(c, z, rot(z)); }
DEV f32x2 cpowk(float a, float th, float k) {
  const float L2E = 1.44269504088896f, IH = 0.15915494f, IL = 6.4206382e-9f;
  float m = __builtin_amdgcn_exp2f(k * a * L2E);
  float x = k * th;
  float hi = x * IH;
  float lo = __builtin_fmaf(x, IL, __builtin_fmaf(x, IH, -hi));
  float r = __builtin_amdgcn_fractf(hi) + lo;
  return f32x2{m * __builtin_amdgcn_cosf(r), m * __builtin_amdgcn_sinf(r)};
}
template <typename T> DEV T lds_ld(u32 a) { typedef T __attribute__((may_alias)) TA; return *(const LAS TA*)(size_t)a; }
template <typename T> DEV void lds_st(u32 a, T v) { typedef T __attribute__((may_alias)) TA; *(LAS TA*)(size_t)a = v; }
DEV s16x4 lds_tr(u32 a) { return __builtin_amdgcn_ds_read_tr16_b64_v4i16((LAS s16x4*)(size_t)a); }
DEV f32x4 mfma(u32x4 a, u32x4 b, f32x4 c) {
  return __builtin_amdgcn_mfma_f32_16x16x32_bf16(__builtin_bit_cast(bf16x8, a), __builtin_bit_cast(bf16x8, b), c, 0, 0, 0);
}
DEV rsrc_t mksrd(const void* p, u32 n) { return __builtin_amdgcn_make_buffer_rsrc((void*)p, (short)0, (int)n, 0x00020000); }
DEV void dma16(rsrc_t rs, u32 voff, u32 soff, u32 lds) {
  asm volatile("s_mov_b32 m0, %0\n\ts_nop 0\n\tbuffer_load_dwordx4 %1, %2, %3 offen lds"
               :: "s"(lds), "v"(voff), "s"(rs), "s"(soff) : "m0", "memory");
}
DEV void dma64v(rsrc_t rs, u32 v0, u32 v1, u32 v2, u32 v3, u32 so, u32 lds) {
  asm volatile("s_mov_b32 m0, %0\n\ts_nop 0\n\tbuffer_load_dwordx4 %1, %5, %6 offen lds\n\t"
               "buffer_load_dwordx4 %2, %5, %6 offen offset:1024 lds\n\tbuffer_load_dwordx4 %3, %5, %6 offen offset:2048 lds\n\t"
               "buffer_load_dwordx4 %4, %5, %6 offen offset:3072 lds"
               :: "s"(lds), "v"(v0), "v"(v1), "v"(v2), "v"(v3), "s"(rs), "s"(so) : "m0", "memory");
}
DEV void dma32v(rsrc_t rs, u32 v0, u32 v1, u32 so, u32 lds) {
  asm volatile("s_mov_b32 m0, %0\n\ts_nop 0\n\tbuffer_load_dwordx4 %1, %3, %4 offen lds\n\t"
               "buffer_load_dwordx4 %2, %3, %4 offen offset:1024 lds"
               :: "s"(lds), "v"(v0), "v"(v1), "s"(rs), "s"(so) : "m0", "memory");
}
template <int OFF> DEV void dmaq(rsrc_t r0, rsrc_t r1, u32 v0, u32 v1, u32 v2, u32 v3, u32 so, u32 slot) {
  asm volatile("s_add_u32 m0, %0, %8\n\ts_nop 0\n\tbuffer_load_dwordx4 %1, %5, %7 offen lds\n\t"
               "buffer_load_dwordx4 %2, %6, %7 offen offset:1024 lds\n\tbuffer_load_dwordx4 %3, %6, %7 offen offset:2048 lds\n\t"
               "buffer_load_dwordx4 %4, %6, %7 offen offset:3072 lds"
               :: "s"(slot), "v"(v0), "v"(v1), "v"(v2), "v"(v3), "s"(r0), "s"(r1), "s"(so), "n"(OFF) : "m0", "scc", "memory");
}
template <int OFF> DEV void dmad(rsrc_t r0, u32 v0, u32 v1, u32 so, u32 slot) {
  asm volatile("s_add_u32 m0, %0, %5\n\ts_nop 0\n\tbuffer_load_dwordx4 %1, %3, %4 offen lds\n\t"
               "buffer_load_dwordx4 %2, %3, %4 offen offset:1024 lds"
               :: "s"(slot), "v"(v0), "v"(v1), "s"(r0), "s"(so), "n"(OFF) : "m0", "scc", "memory");
}
DEV void dma_mark(rsrc_t rs, u32 so, u32 lds) {
  u64 keep;
  asm volatile("s_mov_b32 m0, %1\n\ts_mov_b64 %0, exec\n\ts_mov_b64 exec, 1\n\tbuffer_load_dword off, %2, %3 lds\n\ts_mov_b64 exec, %0"
               : "=&s"(keep) : "s"(lds), "s"(rs), "s"(so) : "m0", "memory");
}
DEV u32 opaque(u32 v) { asm volatile("" : "+v"(v)); return v; }
#define SB() __builtin_amdgcn_sched_barrier(0)
#define RARE(x) __builtin_expect(!!(x), 0)
template <int N> DEV void waitv() { asm volatile("s_waitcnt vmcnt(%0)" :: "n"(N) : "memory"); }
DEV void waitl() { asm volatile("s_waitcnt lgkmcnt(0)" ::: "memory"); }
DEV void hwbar() { waitl(); __builtin_amdgcn_s_barrier(); asm volatile("" ::: "memory"); }
DEV void post(u32 a, u32 v) {
  asm volatile("" ::: "memory");
  lds_st<u32>(a, v);
  asm volatile("" ::: "memory");
}
DEV u32 min4(u32x4 v) { return (u32)__builtin_amdgcn_readfirstlane(min(min(v.x, v.y), min(v.z, v.w))); }
DEV u32x4 peek4(u32 a) { asm volatile("" ::: "memory"); return lds_ld<u32x4>(a); }
DEV void sw_wait4(u32 a, u32 target) {
#pragma unroll 1
  for (u32 it = 0; it < (1u << 22) && min4(peek4(a)) < target; ++it) {}
  asm volatile("" ::: "memory");
}
DEV void sw_wait(u32 a, u32 target) {
  u32 v, sv, cnt = 1u << 22;
  asm volatile("1:\n\tds_read_b32 %0, %4\n\ts_sub_u32 %2, %2, 1\n\ts_cselect_b32 %3, 0, %3\n\ts_waitcnt lgkmcnt(0)\n\t"
               "v_readfirstlane_b32 %1, %0\n\ts_cmp_lt_u32 %1, %3\n\ts_cbranch_scc1 1b"
               : "=&v"(v), "=&s"(sv), "+s"(cnt), "+s"(target) : "v"(a) : "scc", "memory");
}
DEV u32 peek(u32 a) { return __hip_atomic_load((LAS u32*)(size_t)a, __ATOMIC_RELAXED, __HIP_MEMORY_SCOPE_WORKGROUP); }
DEV u32x4 ld16(rsrc_t rs, u32 off) { return __builtin_amdgcn_raw_buffer_load_b128(rs, off, 0, 16); }
DEV u32x2 ld8(rsrc_t rs, u32 off) { return __builtin_amdgcn_raw_buffer_load_b64(rs, off, 0, 16); }
DEV void st16(rsrc_t rs, u32 off, u32x4 v) { __builtin_amdgcn_raw_buffer_store_b128(v, rs, off, 0, 16); }
DEV void st8(rsrc_t rs, u32 off, u32x2 v) { __builtin_amdgcn_raw_buffer_store_b64(v, rs, off, 0, 16); }
constexpr u32 OOB = 0x7ff00000u;
constexpr u32 FMIX = 0x9E3779B9u;
DEV bool flag_ok(u32x2 v, u32 off, u32 tag) { return wave_all((off == OOB) | ((v.x == tag) & (v.y == (tag ^ FMIX)))); }
DEV void poll_flags(rsrc_t rs, u32 off, u32 tag) {
#pragma unroll 1
  for (int it = 0; it < SPIN_CAP; ++it) {
    asm volatile("" ::: "memory");
    if (flag_ok(ld8(rs, off), off, tag)) break;
    __builtin_amdgcn_s_sleep(1);
  }
  asm volatile("" ::: "memory");
}

#define TR(k) do {} while (0)
#define DUMP() do {} while (0)
#define STAMP(k) do {} while (0)
#define CSTAMP(k) do {} while (0)

#define SCAN_STEP(o, v, h)                                                                                             \
  asm("v_fma_f32 %0, %15, %10, %5\n\tv_fma_f32 %1, %16, %11, %6\n\tv_fma_f32 %2, %17, %12, %7\n\t"                   \
      "v_fma_f32 %3, %18, %13, %8\n\tv_fma_f32 %4, %19, %14, %9\n\t"                                                   \
      "v_fmac_f32_dpp %0, %10, %20 quad_perm:[1,0,3,2] row_mask:0xf bank_mask:0xf\n\t"                                 \
      "v_fmac_f32_dpp %1, %11, %21 quad_perm:[1,0,3,2] row_mask:0xf bank_mask:0xf\n\t"                                 \
      "v_fmac_f32_dpp %2, %12, %22 quad_perm:[1,0,3,2] row_mask:0xf bank_mask:0xf\n\t"                                 \
      "v_fmac_f32_dpp %3, %13, %23 quad_perm:[1,0,3,2] row_mask:0xf bank_mask:0xf\n\t"                                 \
      "v_fmac_f32_dpp %4, %14, %24 quad_perm:[1,0,3,2] row_mask:0xf bank_mask:0xf"                                     \
      : "=&v"(o[0]), "=&v"(o[1]), "=&v"(o[2]), "=&v"(o[3]), "=&v"(o[4])                                                \
      : "v"(v[0]), "v"(v[1]), "v"(v[2]), "v"(v[3]), "v"(v[4]), "v"(h[0]), "v"(h[1]), "v"(h[2]), "v"(h[3]), "v"(h[4]), \
        "v"(ma[0]), "v"(ma[1]), "v"(ma[2]), "v"(ma[3]), "v"(ma[4]), "v"(mb[0]), "v"(mb[1]), "v"(mb[2]), "v"(mb[3]),   \
        "v"(mb[4]))
DEV void scan_step(float (&o)[5], const float (&v)[5], const float (&h)[5], const float (&ma)[5], const float (&mb)[5]) {
  float t[5];
  SCAN_STEP(t, v, h);
#pragma unroll
  for (int i = 0; i < 5; ++i) o[i] = t[i];
}

struct Frag { u32x4 a0, a1, b0, b1, b2, b3, b4; };
#define MF(d, a, b) "v_mfma_f32_16x16x32_bf16 %" #d ", %" #a ", %" #b ", %" #d "\n\t"
#define RA(d, o) "ds_read_b128 %" #d ", %24 offset:" #o "\n\t"
#define RB(d, o) "ds_read_b128 %" #d ", %25 offset:" #o "\n\t"
#define WL(n) "s_waitcnt lgkmcnt(" #n ")\n\t"
#define G1_BODY(w1, w2, w3, w5, w7, w9)                                                                    \
  w1 MF(0, 17, 19) RA(10, 0) w2 MF(5, 18, 19) RB(12, 0) w3 MF(1, 17, 20) RA(11, 2048) MF(6, 18, 20) RB(13, 1024) \
  w5 MF(2, 17, 21) RB(14, 2048) MF(7, 18, 21) RB(15, 3072) w7 MF(3, 17, 22) RB(16, 4096) MF(8, 18, 22)    \
  w9 MF(4, 17, 23) MF(9, 18, 23)
#define G1_ACC(A) "+v"(A[0][0]), "+v"(A[0][1]), "+v"(A[0][2]), "+v"(A[0][3]), "+v"(A[0][4]), "+v"(A[1][0]), "+v"(A[1][1]), "+v"(A[1][2]), "+v"(A[1][3]), "+v"(A[1][4])
#define G1_OUT(N) "=&v"(N.a0), "=&v"(N.a1), "=&v"(N.b0), "=&v"(N.b1), "=&v"(N.b2), "=&v"(N.b3), "=&v"(N.b4)
#define G1_IN(C) "v"(C.a0), "v"(C.a1), "v"(C.b0), "v"(C.b1), "v"(C.b2), "v"(C.b3), "v"(C.b4)
template <bool WAITS> DEV void g1_half(f32x4 (&A)[2][5], Frag& N, const Frag& C, u32 pa, u32 pb) {
  if constexpr (WAITS)
    asm volatile(G1_BODY(WL(5), WL(5), WL(5), WL(6), WL(7), WL(7)) : G1_ACC(A), G1_OUT(N) : G1_IN(C), "v"(pa), "v"(pb) : "memory");
  else
    asm volatile(G1_BODY("", "", "", "", "", "") : G1_ACC(A), G1_OUT(N) : G1_IN(C), "v"(pa), "v"(pb) : "memory");
}
DEV u32 g1_half_peek(f32x4 (&A)[2][5], Frag& N, const Frag& C, u32 pa, u32 pb, u32 pf) {
  u32 t = pf;
  asm volatile("ds_read_b32 %0, %0" : "+v"(t) :: "memory");
  asm volatile(G1_BODY(WL(6), WL(6), WL(6), WL(7), WL(8), WL(8)) WL(7) : G1_ACC(A), G1_OUT(N) : G1_IN(C), "v"(pa), "v"(pb) : "memory");
  u32 r;
  asm volatile("v_readfirstlane_b32 %0, %1" : "=s"(r) : "v"(t) : "memory");
  return r;
}
DEV void g1_last(f32x4 (&A)[2][5], const Frag& C) {
  asm volatile(WL(5) MF(0, 10, 12) WL(4) MF(5, 11, 12) WL(3) MF(1, 10, 13) MF(6, 11, 13) WL(2) MF(2, 10, 14) MF(7, 11, 14)
               WL(1) MF(3, 10, 15) MF(8, 11, 15) WL(0) MF(4, 10, 16) MF(9, 11, 16) "s_nop 7"
               : G1_ACC(A) : G1_IN(C) : "memory");
}

struct CF { u32x4 f[5]; };
DEV u32 peek_issue(u32 a) { asm volatile("ds_read_b32 %0, %0" : "+v"(a) :: "memory"); return a; }
DEV u32 peek_get(u32 t) { u32 r; asm volatile("s_waitcnt lgkmcnt(5)\n\tv_readfirstlane_b32 %0, %1" : "=s"(r) : "v"(t) : "memory"); return r; }
#define M2(d, a) "v_mfma_f32_16x16x32_bf16 %" #d ", %" #a ", %15, %" #d "\n\t"
#define R2(d, o) "ds_read_b128 %" #d ", %16 offset:%17+" #o "\n\t"
template <int OFF, int WT> DEV void g2_half(f32x4 (&y)[5], CF& N, const CF& Cc, u32x4 h, u32 pa) {
  asm volatile("s_waitcnt lgkmcnt(%18)\n\t" M2(0, 10) R2(5, 0) "s_waitcnt lgkmcnt(%18)\n\t" M2(1, 11) R2(6, 2048) "s_waitcnt lgkmcnt(%18)\n\t" M2(2, 12) R2(7, 4096)
               "s_waitcnt lgkmcnt(%18)\n\t" M2(3, 13) R2(8, 6144) "s_waitcnt lgkmcnt(%18)\n\t" M2(4, 14) R2(9, 8192)
               : "+v"(y[0]), "+v"(y[1]), "+v"(y[2]), "+v"(y[3]), "+v"(y[4]), "=&v"(N.f[0]), "=&v"(N.f[1]), "=&v"(N.f[2]), "=&v"(N.f[3]), "=&v"(N.f[4])
               : "v"(Cc.f[0]), "v"(Cc.f[1]), "v"(Cc.f[2]), "v"(Cc.f[3]), "v"(Cc.f[4]), "v"(h), "v"(pa), "n"(OFF), "n"(WT) : "memory");
}
#define M3(d, a) "v_mfma_f32_16x16x32_bf16 %" #d ", %" #a ", %10, %" #d "\n\t"
template <bool LAST> DEV void g2_half0(f32x4 (&y)[5], const CF& Cc, u32x4 h) {
  if constexpr (LAST)
    asm volatile(WL(4) M3(0, 5) WL(3) M3(1, 6) WL(2) M3(2, 7) WL(1) M3(3, 8) WL(0) M3(4, 9) "s_nop 7"
                 : "+v"(y[0]), "+v"(y[1]), "+v"(y[2]), "+v"(y[3]), "+v"(y[4])
                 : "v"(Cc.f[0]), "v"(Cc.f[1]), "v"(Cc.f[2]), "v"(Cc.f[3]), "v"(Cc.f[4]), "v"(h) : "memory");
  else
    asm volatile(M3(0, 5) M3(1, 6) M3(2, 7) M3(3, 8) M3(4, 9)
                 : "+v"(y[0]), "+v"(y[1]), "+v"(y[2]), "+v"(y[3]), "+v"(y[4])
                 : "v"(Cc.f[0]), "v"(Cc.f[1]), "v"(Cc.f[2]), "v"(Cc.f[3]), "v"(Cc.f[4]), "v"(h) : "memory");
}
template <int A, int B> DEV bool h_bad(const u32x4 (&h)[20]) {
  u32 m = 0;
#pragma unroll
  for (int k = A; k < B; ++k) m = max(max(max(h[k].x, h[k].y), h[k].z), max(h[k].w, m));
  return __builtin_amdgcn_ballot_w64(m == 0xffffffffu) != 0;
}
template <int A, int B> DEV void h_check(u32x4 (&h)[20], rsrc_t rs, u32 ho) {
  if (RARE((h_bad<A, B>(h)))) {
#pragma unroll 1
    for (int it = 0; it < SPIN_CAP; ++it) {
      __builtin_amdgcn_s_sleep(2);
      asm volatile("" ::: "memory");
#pragma unroll
      for (int k = A; k < B; ++k) h[k] = ld16(rs, ho + k * 1024);
      if (!h_bad<A, B>(h)) break;
    }
  }
}

NOPK __global__ void __launch_bounds__(512, 2) fused_kernel(Params) {
  __shared__ __attribute__((aligned(16))) char smem[163840];
  asm volatile("" :: "v"((u32)(uintptr_t)(LAS char*)smem));
  constexpr u32 L0 = 0;
  const u64 kap = (u64)__builtin_amdgcn_kernarg_segment_ptr();
  u64x8 k0, k1; u64x4 k2;
  asm volatile("s_load_dwordx16 %0, %3, 0x0\n\ts_load_dwordx16 %1, %3, 0x40\n\ts_load_dwordx8 %2, %3, 0x80"
               : "=&s"(k0), "=&s"(k1), "=&s"(k2) : "s"(kap));
  lds_st<u32>(L0 + L_CNT + (threadIdx.x & 31) * 4, 0u);
  asm volatile("s_waitcnt lgkmcnt(0)\n\ts_barrier" : "+s"(k0), "+s"(k1), "+s"(k2) :: "memory");
  struct { const u16 *x, *Lre, *Lim, *Bre, *Bim, *Cre, *Cim, *ldtA, *DA, *Are, *Aim, *sBre, *sBim, *sCre, *sCim, *ldtB, *DB, *gw; float* out; char* ws; }
  const P = {(const u16*)k0[0], (const u16*)k0[1], (const u16*)k0[2], (const u16*)k0[3], (const u16*)k0[4], (const u16*)k0[5], (const u16*)k0[6],
             (const u16*)k0[7], (const u16*)k1[0], (const u16*)k1[1], (const u16*)k1[2], (const u16*)k1[3], (const u16*)k1[4], (const u16*)k1[5],
             (const u16*)k1[6], (const u16*)k1[7], (const u16*)k2[0], (const u16*)k2[1], (float*)k2[2], (char*)k2[3]};
  const int tid = threadIdx.x, lane = tid & 63;
  const int w = __builtin_amdgcn_readfirstlane(tid >> 6);
  const int b = blockIdx.x, xcd = b & 7, r = b >> 3;
  const int c = 4 * (xcd >> 1) + (r >> 3), j = 8 * (xcd & 1) + (r & 7);
  const int ti = 4 * xcd + (r >> 3), dj = r & 7;
  const int i16 = lane & 15, g = lane >> 4;
  const rsrc_t rws = mksrd(P.ws, WS_END);
  const rsrc_t rx = mksrd(P.x, 2048 * 1280);
  const rsrc_t rCre = mksrd(P.Cre, 640 * 1280), rCim = mksrd(P.Cim, 640 * 1280);
  const rsrc_t rka = mksrd((const void*)kap, sizeof(Params));
  constexpr u32 SEQ = __builtin_offsetof(Params, seq);
  const int kq = w & 3, kh = w >> 2;
  const int rot10 = ((r & 7) + 2 * (r >> 3) + 3 * xcd) % 10;
  u32 tag;
  STAMP(0);
  TR(0);
  const u32 cf0 = i16 * 128 + ((g ^ ((i16 >> 1) & 7)) << 4);
  const u32 ccf = L0 + C_CFULL;
  const u32 fho = lane < 8 ? WS_FH + (u32)((ti >> 1) * 16 + 8 * kh + lane) * 128 : OOB;
  const u32 ho = WS_H + (u32)((4 * ti + kq) * 40 + 20 * kh) * 1024 + lane * 16;
  const u32 ima = L0 + (kh ? L_IMG1 : L_IMG0) + (16 * kq + i16) * 320 + (g ^ ((i16 >> 1) & 3)) * 16;
#define G2_REGS(name, n, at)                                                                                \
  CF name[2 * n];                                                                                           \
  { const u32 a0 = opaque(L0 + (at) + cf0), a1 = opaque(L0 + (at) + (cf0 ^ 64));                            \
    _Pragma("unroll") for (int k = 0; k < 2 * n; ++k)                                                       \
    _Pragma("unroll") for (int nt = 0; nt < 5; ++nt) name[k].f[nt] = lds_ld<u32x4>(((k & 1) ? a1 : a0) + (k >> 1) * 10240 + nt * 2048); \
    _Pragma("unroll") for (int k = 0; k < 2 * n; ++k)                                                       \
      asm volatile("s_waitcnt lgkmcnt(0)" : "+v"(name[k].f[0]), "+v"(name[k].f[1]), "+v"(name[k].f[2]), "+v"(name[k].f[3]), "+v"(name[k].f[4]) :: "memory"); }
#define G2_LOADH()                                                                                          \
  u32x4 hf_[20];                                                                                            \
  _Pragma("unroll") for (int ks = 0; ks < 20; ++ks) hf_[ks] = ld16(rws, ho + ks * 1024);                    \
  SB();
#define G2_INIT(at)                                                                                         \
  f32x4 y[5];                                                                                               \
  _Pragma("unroll") for (int nt = 0; nt < 5; ++nt) y[nt] = f32x4{0.f, 0.f, 0.f, 0.f};                       \
  CF P0, P1;                                                                                                \
  const u32 cab[2][2] = {{opaque(L0 + (at) + cf0), opaque(L0 + (at) + (cf0 ^ 64))},                         \
                         {opaque(L0 + (at) + 40960 + cf0), opaque(L0 + (at) + 40960 + (cf0 ^ 64))}};        \
  asm volatile("s_nop 1" : "+v"(y[0]), "+v"(y[1]), "+v"(y[2]), "+v"(y[3]), "+v"(y[4]));
#define G2_U(uu, slot, need)                                                                                \
  { const u32 t_ = peek_issue(ccf);                                                                         \
    g2_half<((slot) & 3) * 10240, 5>(y, P1, P0, hf_[2 * (uu)], cab[(slot) >> 2][1]);                        \
    if (RARE(peek_get(t_) < (need))) sw_wait(ccf, need);                                                    \
    g2_half<(((slot) + 1) & 3) * 10240, 4>(y, P0, P1, hf_[2 * (uu) + 1], cab[((slot) + 1) >> 2][0]); }
#define G2_UN(uu, slot)                                                                                     \
  g2_half<((slot) & 3) * 10240, 4>(y, P1, P0, hf_[2 * (uu)], cab[(slot) >> 2][1]);                          \
  g2_half<(((slot) + 1) & 3) * 10240, 4>(y, P0, P1, hf_[2 * (uu) + 1], cab[((slot) + 1) >> 2][0]);
#define G2_ULAST(uu, slot)                                                                                  \
  g2_half<((slot) & 3) * 10240, 4>(y, P1, P0, hf_[2 * (uu)], cab[(slot) >> 2][1]);                          \
  g2_half0<true>(y, P1, hf_[2 * (uu) + 1]);
#define G2_B_IMAGE()                                                                                        \
  { STAMP(11);                                                                                              \
    sw_wait4(L0 + C_BAR, 4);                                                                                \
    const u32 ia = opaque(ima);                                                                             \
    _Pragma("unroll") for (int nt = 0; nt < 5; ++nt) lds_st<f32x4>(ia + nt * 64, y[nt]); }
  auto get_tag = [&]() INL { sw_wait(L0 + C_FULL, 1); tag = __builtin_amdgcn_readfirstlane(lds_ld<u32>(opaque(L0 + L_TAG))) + 1u; };

  const int pl = lane < 40 ? lane : 39;
  u32 pLre, pLim, pLdt; u32x4 gw0, gw1;
  if (w < 4) {
#pragma unroll
    for (int k = 0; k < 3; ++k) {
      const int qq = 64 * k + lane, t = (qq * 6554) >> 16, cc = qq - 10 * t;
      if (k < 2 || lane < 32) dma16(rx, (u32)(64 * ti + 16 * w + t) * 1280 + dj * 160 + cc * 16, 0, L0 + L_RAW + w * 2560 + k * 1024);
    }
    dma_mark(rka, SEQ, L0 + C_STRIP + 4 * w);
    pLre = ((const GAS u16*)P.Lre)[40 * j + pl]; pLim = ((const GAS u16*)P.Lim)[40 * j + pl]; pLdt = ((const GAS u16*)P.ldtA)[40 * j + pl];
    gw0 = *(const GAS u32x4*)((const GAS char*)P.gw + rot10 * 128 + g * 16); gw1 = *(const GAS u32x4*)((const GAS char*)P.gw + rot10 * 128 + 64 + g * 16);
    asm volatile("" ::: "memory");
  }
  if (__builtin_expect(w == 7, 1)) {
      __builtin_amdgcn_s_setprio(1);
      const rsrc_t rBre = mksrd(P.Bre, 640 * 1280), rBim = mksrd((const char*)P.Bim - 1024, 640 * 1280 + 1024);
      const int q8 = lane >> 3;
      u32 vx[4], vb[10];
      {
        const u32 x0 = (u32)(128 * c + 8 * (q8 >> 2) + (q8 & 3)) * 1280 + (((lane & 7) ^ (q8 >> 1)) << 4);
        vx[0] = x0; vx[1] = (x0 ^ 64) + 16 * 1280 - 1024; vx[2] = x0 + 4 * 1280 - 2048; vx[3] = (x0 ^ 64) + 20 * 1280 - 3072;
        const u32 b0 = (u32)(40 * j + q8) * 1280 + (((lane & 7) ^ (q8 & 6)) << 4), b1 = (b0 ^ 16) + 1024;
#pragma unroll
        for (int k = 0; k < 10; ++k) vb[k] = (k < 5 ? b0 : b1) + 10240 * (k % 5) - 1024 * (k & 3);
      }
      u32 so = rot10 * 128;
      {
        u64 keep;
        asm volatile("s_mov_b32 m0, %1\n\ts_mov_b64 %0, exec\n\ts_mov_b64 exec, 1\n\tbuffer_load_dword off, %2, 0 sc1 lds\n\ts_mov_b64 exec, %0"
                     : "=&s"(keep) : "s"(L0 + L_TAG), "s"(rws) : "m0", "memory");
      }
      auto head = [&](u32 sl) INL {
        dmaq<0>(rx, rx, vx[0], vx[1], vx[2], vx[3], so, sl);
        dmaq<4096>(rx, rx, vx[0], vx[1], vx[2], vx[3], so + 40960, sl);
        dmaq<8192>(rx, rx, vx[0], vx[1], vx[2], vx[3], so + 81920, sl);
        dmaq<12288>(rx, rx, vx[0], vx[1], vx[2], vx[3], so + 122880, sl);
        dmaq<16384>(rBre, rBre, vb[0], vb[1], vb[2], vb[3], so, sl);
      };
      auto tail = [&](u32 sl) INL {
        dmaq<20480>(rBre, rBim, vb[4], vb[5], vb[6], vb[7], so, sl);
        dmad<24576>(rBim, vb[8], vb[9], so, sl);
        so = so == 1152 ? 0 : so + 128;
      };
#pragma unroll
      for (int s = 0; s < 10; ++s) {
        const u32 sb = L0 + ring(s);
        if (s == 5) sw_wait4(L0 + C_PRO, 1);
        if (s == 6 || s == 8) sw_wait4(L0 + C_FREE, s - 4);
        TR(4 + 4 * s);
        if (s >= 2) waitv<36>();
        TR(5 + 4 * s);
        head(sb);
        tail(sb);
        TR(6 + 4 * s);
        dma_mark(rka, SEQ + 4 * s, L0 + C_FULL);
      }
      STAMP(2);
      u32 vc[4];
#pragma unroll
      for (int k = 0; k < 4; ++k) vc[k] = (u32)(80 * dj + q8) * 1280 + (((lane & 7) ^ (lane >> 4) ^ (4 * (k & 1))) << 4) + 9216 * k;
      auto stageC = [&](int u, u32 dst) INL {
        const rsrc_t rs = (u & 1) ? rCim : rCre;
        const u32 so = (u32)(u >> 1) * 128;
        dma64v(rs, vc[0], vc[1], vc[2], vc[3], so, dst);
        dma64v(rs, vc[0], vc[1], vc[2], vc[3], so + 40960, dst + 4096);
        dma32v(rs, vc[0], vc[1], so + 81920, dst + 8192);
      };
      {
        u32 t0 = 0;
#pragma unroll
        for (int k = 0; k < 7; ++k) {
          const u32 ln = min(r * 400 + k * 64 + lane, r * 400 + 399);
          asm volatile("buffer_load_dword %0, %1, %2, 0 offen" : "+v"(t0) : "v"((ln >= 6400 ? ln - 6400 : ln) * 128), "s"(r >= 16 ? rCim : rCre) : "memory");
        }
        const u32 hb = WS_H + (u32)(8 * c + (lane >> 5)) * 40960 + (g & 1) * 2048 + i16 * 16;
#pragma unroll
        for (int nt = 0; nt < 5; ++nt) {
          const int o = 5 * j + nt;
#pragma unroll
          for (int k = 0; k < 4; ++k)
            st16(rws, hb + k * 81920 + (u32)((o >> 3) * 4096 + ((o >> 2) & 1) * 1024 + (o & 3) * 256), u32x4{~0u, ~0u, ~0u, ~0u});
        }
        get_tag();
        asm volatile("s_waitcnt vmcnt(0)" : "+v"(t0) :: "memory");
        st8(rws, lane == 0 ? WS_FH + (u32)(c * 16 + j) * 128 : OOB, u32x2{tag, tag ^ FMIX});
        post(L0 + C_CLR, 1u);
        post(L0 + C_BPREP + 12, 1u);
      }
      poll_flags(rws, fho, tag);
      sw_wait4(L0 + C_BPREP, 1);
      sw_wait4(L0 + C_BAR, 2);
      stageC(10, L0 + L_A0 + 5 * 10240); stageC(11, L0 + L_A0 + 6 * 10240); stageC(12, L0 + L_A0 + 7 * 10240);
      stageC(0, L0 + L_A0 + 3 * 10240); stageC(1, L0 + L_A0 + 4 * 10240);
      dma_mark(rka, SEQ, ccf);
      stageC(4, L0 + L_A0 + 2 * 10240);
      sw_wait(ccf, 1);
      G2_REGS(cr, 3, L_A0 + 5 * 10240);
      post(L0 + C_BDONE + 12, 1u);
      sw_wait4(L0 + C_BAR, 3);
      stageC(2, L0 + L_A0); stageC(3, L0 + L_A0 + 10240);
      dma_mark(rka, SEQ + 4, ccf);
      STAMP(3);
      sw_wait4(L0 + C_C0, 1);
      waitv<30>();
      stageC(5, L0 + L_A0 + 3 * 10240); stageC(6, L0 + L_A0 + 4 * 10240);
      dma_mark(rka, SEQ + 8, ccf);
      waitv<21>();
      STAMP(8);
      u32x4 hf_[20];
#pragma unroll
      for (int ks = 0; ks < 20; ++ks)
        asm volatile("buffer_load_dwordx4 %0, %1, %2, %3 offen offset:%4 sc1" : "=v"(hf_[ks]) : "v"(ho), "s"(rws), "s"((ks >> 2) * 4096), "n"((ks & 3) * 1024) : "memory");
      G2_INIT(L_B0);
      sw_wait4(L0 + C_BDONE, 1);
      STAMP(4);
#define FILL(q, unit, at) stageC(unit, L0 + (at)); dma_mark(rka, SEQ + 4 * (3 + (q)), ccf);
      FILL(0, 13, L_B0); FILL(1, 14, L_B0 + 10240); waitv<50>(); FILL(2, 7, L_A0 + 5 * 10240);
      asm volatile("s_waitcnt vmcnt(33)" : "+v"(hf_[0]), "+v"(hf_[1]), "+v"(hf_[2]), "+v"(hf_[3]), "+v"(hf_[4]), "+v"(hf_[5]), "+v"(hf_[6]), "+v"(hf_[7]), "+v"(hf_[8]), "+v"(hf_[9]) :: "memory");
      asm volatile("" : "+v"(hf_[10]), "+v"(hf_[11]), "+v"(hf_[12]), "+v"(hf_[13]), "+v"(hf_[14]), "+v"(hf_[15]), "+v"(hf_[16]), "+v"(hf_[17]), "+v"(hf_[18]), "+v"(hf_[19]));
      FILL(3, 15, L_B0 + 2 * 10240);
      h_check<0, 20>(hf_, rws, ho);
      FILL(4, 8, L_A0 + 6 * 10240);
      g2_half0<false>(y, cr[0], hf_[0]); g2_half0<false>(y, cr[1], hf_[1]);
      waitv<40>(); FILL(5, 16, L_B0 + 3 * 10240);
      g2_half0<false>(y, cr[2], hf_[2]); g2_half0<false>(y, cr[3], hf_[3]);
      waitv<40>(); FILL(6, 9, L_A0 + 7 * 10240);
      g2_half0<false>(y, cr[4], hf_[4]); g2_half<0, 4>(y, P0, cr[5], hf_[5], cab[0][0]);
      waitv<40>(); FILL(7, 17, L_B0 + 4 * 10240);
      G2_UN(3, 0);
      waitv<40>(); FILL(8, 18, L_B0 + 5 * 10240);
      G2_UN(4, 1);
      waitv<40>(); FILL(9, 19, L_B0 + 6 * 10240);
      STAMP(5);
      __builtin_amdgcn_s_setprio(0);
      G2_U(5, 2, 9); G2_U(6, 3, 11); G2_U(7, 4, 12); G2_U(8, 5, 13); G2_ULAST(9, 6);
      G2_B_IMAGE();
  } else if (__builtin_expect(w < 4, 1)) {
    __builtin_amdgcn_s_setprio(2);
    f32x4 acc[2][5], accg[2] = {f32x4{0.f, 0.f, 0.f, 0.f}, f32x4{0.f, 0.f, 0.f, 0.f}};
#pragma unroll
    for (int m = 0; m < 2; ++m)
#pragma unroll
      for (int nt = 0; nt < 5; ++nt) acc[m][nt] = f32x4{0.f, 0.f, 0.f, 0.f};
    asm volatile("" : G1_ACC(acc));
    sw_wait4(L0 + C_STRIP, 1);
    STAMP(14);
    u32x2 xq[5];
    {
      const u32 ra = opaque(L0 + L_RAW + (32 * (g & 1) + 8 * (lane & 3) + ((lane >> 2) & 3)) * 160 + 40 * w + 8 * (g >> 1));
      u32x2 lo[3], hi[3];
#pragma unroll
      for (int k = 0; k < 3; ++k) { lo[k] = __builtin_bit_cast(u32x2, lds_tr(ra + k * 16)); hi[k] = __builtin_bit_cast(u32x2, lds_tr(ra + k * 16 + 640)); }
      const u32 xr = opaque(L0 + L_RAW + (16 * w + i16) * 160 + 8 * g);
#pragma unroll
      for (int nt = 0; nt < 5; ++nt) xq[nt] = lds_ld<u32x2>(xr + 32 * nt);
      const u32 xo = WS_XT + (u32)(20 * dj + 5 * w + (g >> 1)) * 16384 + (8 * ti + 4 * (g & 1)) * 64 + i16 * 16;
      waitl();
      dma_mark(rka, SEQ, L0 + C_PRO + 4 * w);
#pragma unroll
      for (int k = 0; k < 3; ++k) st16(rws, (k < 2 || lane < 32) ? xo + k * 32768 : OOB, u32x4{lo[k].x, lo[k].y, hi[k].x, hi[k].y});
    }
    STAMP(15);
    asm volatile("" : "+v"(pLre), "+v"(pLim), "+v"(pLdt));
    const float lre = bf2f(pLre), lim = bf2f(pLim);
    const float dtA = rbf(expf(bf2f(pLdt)));
    const float aA = dtA * lre, tA = dtA * lim;
    const f32x2 lam = cpowk(aA, tA, 1.f), l8 = cpowk(aA, tA, 8.f);
    f32x2 c2;
    {
      const float nr = lam.x - 1.f, ni = lam.y, den = 2.f * __builtin_amdgcn_rcpf(lre * lre + lim * lim);
      c2 = f32x2{(nr * lre + ni * lim) * den, (ni * lre - nr * lim) * den};
    }
    f32x2 zs[4], zh[4];
    zs[3] = cpowk(aA, tA, (float)(96 - 32 * w));
    zh[0] = cmul(cpowk(aA, tA, (float)(32 * w)), c2);
#pragma unroll
    for (int k = 1; k < 4; ++k) { zs[3 - k] = cmul(zs[4 - k], l8); zh[k] = cmul(zh[k - 1], l8); }
    const int sig = 4 * w + g;
    float lr[5], ls[5], sa[5], sb[5], ha[5], hb[5], ca[5], cb[5];
#pragma unroll
    for (int k = 0; k < 4; ++k) { zh[k].y = -zh[k].y; asm volatile("" : "+v"(zs[k]), "+v"(zh[k])); }
    f32x4 q0[5], q1[5];
    const u32 tb = opaque(L0 + L_TAB + w * 3200 + pl * 16), tp = opaque(L0 + L_TAB + w * 3200 + (i16 >> 1) * 16), tg = opaque(tp + g * 640);
    const int nvalid = (c - w + 3) >> 2;
    f32x2 lam4 = cpowk(aA, tA, 512.f);
    f32x2 lame = cpowk(aA, tA, (float)(128 * ((c - 1 - w) & 3)));
    asm volatile("" : "+v"(lam4), "+v"(lame));
    STAMP(1);
    const u32 ax0 = L0 + (32 * w + i16) * 128 + ((g ^ ((i16 >> 1) & 7)) << 4);
    const u32 ab0 = L0 + 16384 + (40 * (i16 & 1) + (i16 >> 1)) * 128 + ((g ^ (2 * (i16 >> 2) + (i16 & 1))) << 4);
    const u32 ax1 = ax0 ^ 64, ab1 = ab0 ^ 64;
    const u32 cfull = opaque(L0 + C_FULL), cfree = opaque(L0 + C_FREE);
    const bool gater = (xcd & 1) ? ((r & 7) >= 2 && (r & 7) < 7) : (r & 7) < 5;
    Frag X, Y;
    sw_wait(cfull, 1);
    const u32 seed = lds_ld<u32>(opaque(L0 + L_TAG));
    {
      const u32 pa = opaque(ax0 + L_A0), pb = opaque(ab0 + L_A0);
      X.a0 = lds_ld<u32x4>(pa); X.b0 = lds_ld<u32x4>(pb); X.a1 = lds_ld<u32x4>(pa + 2048); X.b1 = lds_ld<u32x4>(pb + 1024);
      X.b2 = lds_ld<u32x4>(pb + 2048); X.b3 = lds_ld<u32x4>(pb + 3072); X.b4 = lds_ld<u32x4>(pb + 4096);
    }
    if (gater) {
      accg[0] = mfma(lds_ld<u32x4>(ax1 + L_A0), gw1, mfma(X.a0, gw0, accg[0]));
      accg[1] = mfma(lds_ld<u32x4>(ax1 + L_A0 + 2048), gw1, mfma(X.a1, gw0, accg[1]));
    }
    asm volatile("s_waitcnt lgkmcnt(0)" : "+v"(X.a0), "+v"(X.a1), "+v"(X.b0), "+v"(X.b1), "+v"(X.b2), "+v"(X.b3), "+v"(X.b4));
    tag = __builtin_amdgcn_readfirstlane(seed) + 1u;
    const u32 fxo_ = lane == 0 ? WS_FX + (u32)((ti * 8 + dj) * 4 + w) * 128 : OOB;
    const u32x2 fxv = {tag, tag ^ FMIX};
    u32 so = L_A0;
#pragma unroll 1
    for (int s = 0; s < 8; s += 2) {
      u32 have = g1_half_peek(acc, Y, X, ax1 + so, ab1 + so, cfull);
      so = s == 2 ? 0 : so + STG;
      TR(4 + 4 * s);
      if (RARE(have < (u32)(s + 2))) sw_wait(cfull, s + 2);
      g1_half<true>(acc, X, Y, ax0 + so, ab0 + so);
      have = g1_half_peek(acc, Y, X, ax1 + so, ab1 + so, cfull);
      so = s == 4 ? L_A0 : so + STG;
      TR(5 + 4 * s);
      if (RARE(have < (u32)(s + 3))) sw_wait(cfull, s + 3);
      g1_half<true>(acc, X, Y, ax0 + so, ab0 + so);
      TR(6 + 4 * s);
      dma_mark(rka, SEQ + 4 * s + 4, L0 + C_FREE + 4 * w);
      if (RARE(s == 0)) { waitv<1>(); st8(rws, fxo_, fxv); }
    }
    {
      const u32 have = g1_half_peek(acc, Y, X, ax1 + ring(8), ab1 + ring(8), cfull);
      if (RARE(have < 10u)) sw_wait(cfull, 10);
      sw_wait4(cfree, 6);
#pragma unroll
      for (int k = 0; k < 4; ++k) lds_st<f32x4>(tb + k * 640, f32x4{zs[k].x, zs[k].y, zh[k].x, zh[k].y});
      lds_st<f32x4>(tb + 4 * 640, f32x4{lam.x, lam.y, c2.x, -c2.y});
      g1_half<false>(acc, X, Y, ax0 + ring(9), ab0 + ring(9));
#pragma unroll
      for (int nt = 0; nt < 5; ++nt) { q1[nt] = lds_ld<f32x4>(tp + 4 * 640 + nt * 128); q0[nt] = lds_ld<f32x4>(tg + nt * 128); }
      asm volatile("s_waitcnt lgkmcnt(10)" ::: "memory");
      g1_half<false>(acc, Y, X, ax1 + ring(9), ab1 + ring(9));
      g1_last(acc, Y);
      waitv<0>();
      post(L0 + C_FREE + 4 * w, 10u);
    }
    const u32x4 fr10 = peek4(cfree);
    {
      const float sg = (lane & 1) ? 1.f : -1.f;
#pragma unroll
      for (int nt = 0; nt < 5; ++nt) {
        sa[nt] = q0[nt].x; sb[nt] = sg * q0[nt].y; ha[nt] = -sg * q0[nt].z; hb[nt] = q0[nt].w;
        lr[nt] = q1[nt].x; ls[nt] = sg * q1[nt].y; ca[nt] = -sg * q1[nt].z; cb[nt] = q1[nt].w;
      }
    }
    STAMP(2);
    TR(44);
    float v[8][5];
#pragma unroll
    for (int jj = 0; jj < 8; ++jj)
#pragma unroll
      for (int nt = 0; nt < 5; ++nt) v[jj][nt] = acc[jj >> 2][nt][jj & 3];
    float e[5];
#pragma unroll
    for (int nt = 0; nt < 5; ++nt) e[nt] = v[0][nt];
#pragma unroll
    for (int jj = 1; jj < 8; ++jj) scan_step(e, v[jj], e, lr, ls);
    {
      float z[5] = {0.f, 0.f, 0.f, 0.f, 0.f}, we[5];
      scan_step(we, z, e, sa, sb);
      auto s01 = __builtin_amdgcn_permlane32_swap(f2u(we[0]), f2u(we[1]), false, false);
      auto s23 = __builtin_amdgcn_permlane32_swap(f2u(we[2]), f2u(we[3]), false, false);
      auto s44 = __builtin_amdgcn_permlane32_swap(f2u(we[4]), f2u(we[4]), false, false);
      const float y01 = u2f(s01[0]) + u2f(s01[1]), y23 = u2f(s23[0]) + u2f(s23[1]), y4 = u2f(s44[0]) + u2f(s44[1]);
      auto q03 = __builtin_amdgcn_permlane16_swap(f2u(y01), f2u(y23), false, false);
      auto q44 = __builtin_amdgcn_permlane16_swap(f2u(y4), f2u(y4), false, false);
      if (RARE(min4(fr10) < 10u)) sw_wait4(cfree, 10);
      const u32 wa = opaque(L0 + L_WS + w * 320 + i16 * 4);
      lds_st<float>(wa + (2 * (g & 1) + (g >> 1)) * 64, u2f(q03[0]) + u2f(q03[1]));
      lds_st<float>(wa + 256, u2f(q44[0]) + u2f(q44[1]));
      const u32 ra = opaque(L0 + L_ERAW + sig * 320 + i16 * 4);
#pragma unroll
      for (int nt = 0; nt < 5; ++nt) lds_st<float>(ra + nt * 64, e[nt]);
    }
    post(L0 + C_BAR + 4 * w, 1u);
    sw_wait4(L0 + C_BAR, 1);
    if (w == 2) {
      const u32 ap = opaque(L0 + L_WS + pl * 8);
      const f32x2 se = (lds_ld<f32x2>(ap) + lds_ld<f32x2>(ap + 320)) + (lds_ld<f32x2>(ap + 640) + lds_ld<f32x2>(ap + 960));
      st16(rws, lane < 40 ? WS_SG + (u32)(c * 640 + 40 * j + lane) * 16 : OOB, u32x4{f2u(se.x), tag, f2u(se.y), tag});
    }
    STAMP(3);
    if (gater) {
      float gl = accg[0][0];
#pragma unroll
      for (int k = 1; k < 8; ++k) gl = (i16 == k) ? accg[k >> 2][k & 3] : gl;
      st8(rws, i16 < 8 ? WS_GG + (u32)(rot10 * 2048 + 128 * c + 32 * w + 8 * g + i16) * 8 : OOB, u32x2{f2u(gl), tag});
    }
    float pf[5];
    {
      u32 era = opaque(L0 + L_ERAW + pl * 8);
      const f32x2 l8r = rot(l8);
      f32x2 pp = {0.f, 0.f};
#pragma unroll 1
      for (int s2 = 0; s2 < w; ++s2, era += 1280) {
        const f32x2 e0 = lds_ld<f32x2>(era), e1 = lds_ld<f32x2>(era + 320), e2 = lds_ld<f32x2>(era + 640), e3 = lds_ld<f32x2>(era + 960);
        pp = cmul(pp, l8, l8r) + e0; pp = cmul(pp, l8, l8r) + e1; pp = cmul(pp, l8, l8r) + e2; pp = cmul(pp, l8, l8r) + e3;
      }
      const f32x2 e0 = lds_ld<f32x2>(era), e1 = lds_ld<f32x2>(era + 320), e2 = lds_ld<f32x2>(era + 640);
      const u32 ptb = L0 + L_PT + w * 1280;
      const u32 pw = opaque(ptb + pl * 8);
#pragma unroll
      for (int k = 0; k < 4; ++k) {
        const f32x2 q = cmul(pp, c2);
        lds_st<f32x2>(pw + k * 320, f32x2{q.x, -q.y});
        if (k < 3) pp = cmul(pp, l8, l8r) + (k == 0 ? e0 : k == 1 ? e1 : e2);
      }
      const u32 pr_ = opaque(ptb + g * 320 + i16 * 4);
#pragma unroll
      for (int nt = 0; nt < 5; ++nt) pf[nt] = lds_ld<float>(pr_ + nt * 64);
      const float z[5] = {0.f, 0.f, 0.f, 0.f, 0.f};
#pragma unroll
      for (int jj = 0; jj < 8; ++jj) scan_step(v[jj], z, v[jj], ca, cb);
#pragma unroll
      for (int nt = 0; nt < 5; ++nt) ls[nt] = -ls[nt];
    }
    {
      u32 off[4];
#pragma unroll
      for (int k = 0; k < 4; ++k) off[k] = ((k < nvalid || c + w + k == 0) && lane < 40) ? WS_SG + (u32)((w + 4 * k) * 640 + 40 * j + lane) * 16 : OOB;
      u32x4 sv[4];
#pragma unroll 1
      for (int it = 0; it < SPIN_CAP; ++it) {
        asm volatile("" ::: "memory");
        bool ok = true;
#pragma unroll
        for (int k = 0; k < 4; ++k) sv[k] = ld16(rws, off[k]);
#pragma unroll
        for (int k = 0; k < 4; ++k) ok &= (off[k] == OOB) | ((sv[k].y == tag) & (sv[k].w == tag));
        if (wave_all(ok)) break;
        __builtin_amdgcn_s_sleep(1);
      }
      asm volatile("" ::: "memory");
      STAMP(4);
      f32x2 hsum = {0.f, 0.f};
      const f32x2 l4r = rot(lam4);
#pragma unroll
      for (int k = 0; k < 4; ++k)
        if (k < nvalid) hsum = cmul(hsum, lam4, l4r) + f32x2{u2f(sv[k].x), u2f(sv[k].z)};
      hsum = cmul(hsum, lame);
      const u32 hp = opaque(L0 + L_HP + lane * 32 + w * 4);
      if (lane < 40) {
        lds_st<float>(hp, hsum.x);
        lds_st<float>(hp + 16, hsum.y);
      }
    }
    post(L0 + C_BAR + 4 * w, 2u);
    const u32 clr = peek(L0 + C_CLR);
    sw_wait4(L0 + C_BAR, 2);
    {
      float hin[5], h0[5];
      const u32 hp = opaque(L0 + L_HP + i16 * 16);
#pragma unroll
      for (int nt = 0; nt < 5; ++nt) {
        const f32x4 q = lds_ld<f32x4>(hp + nt * 256);
        hin[nt] = (q.x + q.y) + (q.z + q.w);
      }
      scan_step(h0, pf, hin, ha, hb);
      scan_step(v[0], v[0], h0, lr, ls);
#pragma unroll
      for (int jj = 1; jj < 8; ++jj) scan_step(v[jj], v[jj], v[jj - 1], lr, ls);
    }
    {
      const u32 img = L0 + L_HT + w * 1280;
      const u32 wa = opaque(img + i16 * 80 + g * 16);
      const u32 ra = opaque(img + (2 * ((lane >> 2) & 3) + (g & 1)) * 80 + (g >> 1) * 32 + (lane & 3) * 8);
      const u32 hb0 = WS_H + (u32)(8 * c + 2 * w + (g >> 1)) * 40960 + (g & 1) * 2048 + i16 * 16;
      u32x2 lo[5], hi[5];
#pragma unroll
      for (int nt = 0; nt < 5; ++nt) {
        lds_st<u32x4>(wa, u32x4{pk(v[0][nt], v[1][nt]), pk(v[2][nt], v[3][nt]), pk(v[4][nt], v[5][nt]), pk(v[6][nt], v[7][nt])});
        lo[nt] = __builtin_bit_cast(u32x2, lds_tr(ra));
        hi[nt] = __builtin_bit_cast(u32x2, lds_tr(ra + 640));
      }
      SB();
      if (RARE(__builtin_amdgcn_readfirstlane(clr) == 0)) sw_wait(L0 + C_CLR, 1);
#pragma unroll
      for (int nt = 0; nt < 5; ++nt) {
        const int o = 5 * j + nt;
        st16(rws, hb0 + (u32)((o >> 3) * 4096 + ((o >> 2) & 1) * 1024 + (o & 3) * 256), u32x4{lo[nt].x, lo[nt].y, hi[nt].x, hi[nt].y});
      }
    }
    post(L0 + C_BAR + 4 * w, 3u);
    STAMP(5);
    u32x2 eda[5], edb[5];
#pragma unroll
    for (int nt = 0; nt < 5; ++nt) {
      eda[nt] = *(const GAS u32x2*)((const GAS u16*)P.DA + 80 * dj + 16 * nt + 4 * g);
      edb[nt] = *(const GAS u32x2*)((const GAS u16*)P.DB + 80 * dj + 16 * nt + 4 * g);
    }
    const u32 go = WS_GG + (u32)(g * 2048 + 64 * ti + 16 * kq + i16) * 8, go2 = g < 2 ? go + 8 * 2048 * 8 : OOB;
    u32x2 g0 = ld8(rws, go), g1 = ld8(rws, go + 4 * 2048 * 8), g2 = ld8(rws, go2);
    const u32x2 fhv = ld8(rws, fho);
    sw_wait(ccf, 1);
    G2_REGS(cr, 2, L_A0 + 3 * 10240);
    dma_mark(rka, SEQ, L0 + C_C0 + 4 * w);
#pragma unroll 1
    for (int it = 0; it < SPIN_CAP && RARE(!wave_all((g0.y == tag) & (g1.y == tag) & ((g2.y == tag) | (g >= 2)))); ++it) {
      __builtin_amdgcn_s_sleep(1);
      asm volatile("" ::: "memory");
      g0 = ld8(rws, go); g1 = ld8(rws, go + 4 * 2048 * 8); g2 = ld8(rws, go2);
    }
    float gate, ogate;
    {
      const float s3 = (u2f(g0.x) + u2f(g1.x)) + u2f(g2.x);
      auto sa_ = __builtin_amdgcn_permlane32_swap(f2u(s3), f2u(s3), false, false);
      const float s2 = u2f(sa_[0]) + u2f(sa_[1]);
      auto sb_ = __builtin_amdgcn_permlane16_swap(f2u(s2), f2u(s2), false, false);
      gate = rbf(1.f / (1.f + expf(-rbf(u2f(sb_[0]) + u2f(sb_[1])))));
      ogate = rbf(1.f - gate);
    }
    u32x2 ska[5], skb[5];
#pragma unroll
    for (int nt = 0; nt < 5; ++nt) {
#pragma unroll
      for (int e = 0; e < 2; ++e) {
        const float x0 = bflo(xq[nt][e]), x1 = bfhi(xq[nt][e]);
        ska[nt][e] = pk(x0 * bflo(eda[nt][e]), x1 * bfhi(eda[nt][e]));
        skb[nt][e] = pk(x0 * bflo(edb[nt][e]), x1 * bfhi(edb[nt][e]));
      }
      asm volatile("" : "+v"(ska[nt]), "+v"(skb[nt]));
    }
    waitv<1>();
    if (RARE(!flag_ok(fhv, fho, tag))) poll_flags(rws, fho, tag);
    asm volatile("" ::: "memory");
    __builtin_amdgcn_s_setprio(0);
    STAMP(6);
    G2_LOADH();
    STAMP(7);
    G2_INIT(L_A0);
    h_check<0, 4>(hf_, rws, ho);
    g2_half0<false>(y, cr[0], hf_[0]); g2_half0<false>(y, cr[1], hf_[1]); g2_half0<false>(y, cr[2], hf_[2]);
    sw_wait(ccf, 2);
    g2_half<0, 4>(y, P0, cr[3], hf_[3], cab[0][0]);
    STAMP(8);
    h_check<4, 10>(hf_, rws, ho);
    G2_UN(2, 0); G2_UN(3, 1); G2_U(4, 2, 3);
    h_check<10, 14>(hf_, rws, ho);
    G2_UN(5, 3);
    h_check<14, 20>(hf_, rws, ho);
    STAMP(9);
    G2_U(6, 4, 6);
    lds_st<float>(opaque(L0 + L_GATE + (16 * w + i16) * 4), gate);
    post(L0 + C_BAR + 4 * w, 4u);
    waitl();
    const u32 bo = WS_BT + (u32)(80 * dj + (lane >> 2)) * 8192 + (64 * ti + 16 * w) * 4 + (lane & 3) * 16;
    u32x4 bv[5];
#pragma unroll
    for (int k = 0; k < 5; ++k) bv[k] = ld16(rws, bo + k * 16 * 8192);
    SB();
    G2_U(7, 5, 8); G2_U(8, 6, 10); G2_ULAST(9, 7);
    STAMP(10);
#pragma unroll 1
    for (int it = 0; it < SPIN_CAP; ++it) {
      bool ok = true;
#pragma unroll
      for (int k = 0; k < 5; ++k) ok &= (bv[k].y == tag) & (bv[k].w == tag);
      if (!RARE(!wave_all(ok))) break;
      __builtin_amdgcn_s_sleep(2);
      asm volatile("" ::: "memory");
#pragma unroll
      for (int k = 0; k < 5; ++k) bv[k] = ld16(rws, bo + k * 16 * 8192);
    }
    STAMP(11);
    sw_wait4(L0 + C_BAR, 4);
    {
      const u32 ba = opaque(L0 + L_BST + w * 2560 + lane * 8), ia = opaque(ima);
#pragma unroll
      for (int k = 0; k < 5; ++k) lds_st<u32x2>(ba + k * 512, u32x2{bv[k].x, bv[k].z});
#pragma unroll
      for (int nt = 0; nt < 5; ++nt) {
        const u32x2 yb = __builtin_bit_cast(u32x2, lds_tr(ba + nt * 512));
        f32x4 o;
#pragma unroll
        for (int e = 0; e < 2; ++e) {
          const u32 bb = pk(bflo(yb[e]) + bflo(skb[nt][e]), bfhi(yb[e]) + bfhi(skb[nt][e]));
          const u32 ob = pk(ogate * bflo(bb), ogate * bfhi(bb));
          o[2 * e] = gate * (y[nt][2 * e] + bflo(ska[nt][e])) + bflo(ob);
          o[2 * e + 1] = gate * (y[nt][2 * e + 1] + bfhi(ska[nt][e])) + bfhi(ob);
        }
        lds_st<f32x4>(ia + nt * 64, o);
      }
    }
  } else {
    const int pr = xcd * 16 + (r >> 1);
    const int nch = (r & 1) ? 2 : 3, ch = 5 * pr + ((r & 1) ? 3 : 0) + kq;
    const u32 slot = opaque(L0 + L_B0 + kq * BSLOT) ;
    u32x2 fhv;
    f32x4 ya[8];
    if (__builtin_expect(kq < nch, 1)) {
      const GAS u32* pa = (const GAS u32*)P.Are + ch * 64 + lane;
      const u32 wAre = *pa, wAim = ((const GAS u32*)P.Aim)[ch * 64 + lane];
      const u32 wBre = ((const GAS u32*)P.sBre)[ch * 64 + lane], wBim = ((const GAS u32*)P.sBim)[ch * 64 + lane];
      const u32 wCre = ((const GAS u32*)P.sCre)[ch * 64 + lane], wCim = ((const GAS u32*)P.sCim)[ch * 64 + lane];
      const u32 wdt = ((const GAS u16*)P.ldtB)[ch];
      const float dt = rbf(expf(bf2f(wdt)));
      f32x4 ka[3] = {f32x4{0.f, 0.f, 0.f, 0.f}, f32x4{0.f, 0.f, 0.f, 0.f}, f32x4{0.f, 0.f, 0.f, 0.f}};
      const u32 tr = opaque(slot + i16 * TROW + g * 16);
      const u32 fxo = lane < 32 ? WS_FX + (u32)((lane * 8 + ch / 80) * 4 + (ch % 80) / 20) * 128 : OOB;
      u32 tv[56], tu[56];
#pragma unroll 1
      for (int rd = 0; rd < 2; ++rd) {
#pragma unroll
        for (int k = 0; k < 56; ++k) tu[k] = tv[k];
        const u32 sh = rd ? 0u : 16u;
        const float a = dt * bfhi(wAre << sh), t = dt * bfhi(wAim << sh);
        const f32x2 db = {dt * bfhi(wBre << sh), dt * bfhi(wBim << sh)}, cc = {bfhi(wCre << sh), bfhi(wCim << sh)};
        const f32x2 W = cmul(cc, db);
        const f32x2 z1 = cpowk(a, t, 1.f), z8 = cpowk(a, t, 8.f), z128 = cpowk(a, t, 128.f);
        {
          f32x2 cv = z8;
          tv[41] = pk(cv.x, cv.y);
#pragma unroll
          for (int jj = 2; jj < 16; ++jj) { cv = cmul(cv, z8); tv[40 + jj] = pk(cv.x, cv.y); }
        }
        {
          const f32x2 y1 = {z1.x, -z1.y}, yh = {z128.x, -z128.y};
          f32x2 R[8];
          R[0] = f32x2{W.x, -W.y};
#pragma unroll
          for (int k = 1; k < 8; ++k) R[k] = cmul(R[k - 1], y1);
#pragma unroll
          for (int k = 0; k < 8; ++k) tv[k] = pk(R[k].x, R[k].y);
#pragma unroll
          for (int k = 0; k < 8; ++k) { R[k] = cmul(R[k], yh); tv[8 + k] = pk(R[k].x, R[k].y); }
#pragma unroll
          for (int hh = 0; hh < 2; ++hh)
#pragma unroll
            for (int k = 0; k < 4; ++k) { R[2 * k] = cmul(R[2 * k], yh); tv[16 + 4 * hh + k] = pk(R[2 * k].x, R[2 * k].y); }
#pragma unroll
          for (int hh = 0; hh < 4; ++hh)
#pragma unroll
            for (int k = 0; k < 2; ++k) { R[4 * k] = cmul(R[4 * k], yh); tv[24 + 2 * hh + k] = pk(R[4 * k].x, R[4 * k].y); }
#pragma unroll
          for (int hh = 0; hh < 9; ++hh) { R[0] = cmul(R[0], yh); tv[32 + hh] = pk(R[0].x, R[0].y); }
        }
      }
      STAMP(1);
      get_tag();
      poll_flags(rws, fxo, tag);
      u32x4 uv[4];
#pragma unroll
      for (int k = 0; k < 4; ++k) uv[k] = ld16(rws, WS_XT + (u32)(ch >> 2) * 16384 + (ch & 3) * 16 + (64 * k + lane) * 64);
      const u32 uw = opaque(slot + UPAD + lane * 16);
      const u32 kc = slot + KC_OFF;
      const bool r1 = lane < 32;
      const u32 xa = opaque(kc - 4 + (r1 ? 16 * lane : 256 + 8 * lane)), xb = opaque(xa + (r1 ? 12 : 8));
      const u32 sA = opaque(r1 ? 0x05040706u : 0x03020504u), sB = opaque(r1 ? 0x05040706u : 0x05040504u), sC = opaque(r1 ? 0x03020504u : 0x01000302u),
                sD = opaque(r1 ? 0x03020504u : 0x05040504u), sE = opaque(lane <= 32 ? 0x03020504u : 0x05040504u);
      const u32 ka0 = opaque(kc + (128 * (g >> 1) + 8 * i16 + 4 * (g & 1)) * 2);
      const u32 ka1 = opaque(kc + (g < 2 ? 256 + 64 * g + 4 * i16 : 256 + 64 * g + 2 * i16) * 2), ka1b = opaque(ka1 + (g < 2 ? 4 : 64));
      const u32 ka2 = opaque(kc + (512 + 64 * min(g, 2) + i16) * 2);
      const u32 sbase = __builtin_amdgcn_readfirstlane(slot);
      sw_wait4(L0 + C_FREE, 10);
      STAMP(6);
#define TW(v, row) asm volatile("ds_write_addtid_b32 %0 offset:%1" :: "v"(v), "n"((row) * TROW) : "memory")
      asm volatile("s_mov_b32 m0, %0\n\ts_nop 0" :: "s"(sbase) : "m0", "memory");
      TW(0x00003f80u, 41);
#pragma unroll
      for (int rd = 0; rd < 2; ++rd) {
#pragma unroll
        for (int k = 0; k < 56; ++k) TW(rd ? tv[k] : tu[k], k < 41 ? k : k + 1);
#pragma unroll
        for (int ks = 0; ks < 4; ++ks) {
          const u32x4 bb = lds_ld<u32x4>(tr + 41 * TROW + ks * 64);
#pragma unroll
          for (int m = 0; m < 3; ++m) ka[m] = mfma(lds_ld<u32x4>(tr + 16 * m * TROW + ks * 64), bb, ka[m]);
        }
      }
#define UW(v, off) asm volatile("ds_write_addtid_b32 %0 offset:%1" :: "v"(v), "n"(off) : "memory")
#pragma unroll
      for (int cc = 0; cc < 4; ++cc) { UW(0u, cc * UCOPY); UW(0u, cc * UCOPY + 144); }
#pragma unroll
      for (int k = 0; k < 4; ++k) {
        lds_st<u32x4>(uw + k * 1024, uv[k]);
#pragma unroll
        for (int e = 0; e < 4; ++e) lds_st<u32>(uw + k * 1024 + UCOPY + 4 + 4 * e, uv[k][e]);
        lds_st<u32x2>(uw + k * 1024 + 2 * UCOPY + 8, u32x2{uv[k].x, uv[k].y});
        lds_st<u32x2>(uw + k * 1024 + 2 * UCOPY + 16, u32x2{uv[k].z, uv[k].w});
      }
      STAMP(5);
      {
        const u32 q0 = pk(ka[2].x, ka[2].y), q1 = pk(ka[2].z, ka[2].w);
        if (g < 3) { lds_st<u16>(ka2, (u16)q0); lds_st<u16>(ka2 + 32, (u16)(q0 >> 16)); lds_st<u16>(ka2 + 64, (u16)q1); lds_st<u16>(ka2 + 96, (u16)(q1 >> 16)); }
        lds_st<u32x2>(ka0, u32x2{pk(ka[0].x, ka[0].y), pk(ka[0].z, ka[0].w)});
        lds_st<u32>(ka1, pk(ka[1].x, ka[1].y));
        lds_st<u32>(ka1b, pk(ka[1].z, ka[1].w));
        lds_st<u32>(kc - 4, 0u);
      }
#define PERM(hi, lo, sel) __builtin_amdgcn_perm(hi, lo, sel)
      {
        const u32 R0 = lds_ld<u32>(xa), R1 = lds_ld<u32>(xa + 4), R2 = lds_ld<u32>(xa + 8), R3 = lds_ld<u32>(xb), R4 = lds_ld<u32>(xb + 4);
        const u32 ea_ = opaque(kc + 768 + 4 * lane), eb_ = opaque(kc + 1024 + 2 * lane);
        const u32 E0 = lds_ld<u32>(ea_), E1 = lds_ld<u32>(ea_ + 4);
        const u32 f0 = lds_ld<u16>(eb_), f1 = lds_ld<u16>(eb_ + 2), f2 = lds_ld<u16>(eb_ + 128), f3 = lds_ld<u16>(eb_ + 130);
        UW(0u, KOFF); UW(0u, KOFF + KCOPY);
        const u32 k0 = opaque(slot + KOFF + KPAD + lane * 16);
        lds_st<u32x4>(k0 + KCOPY, u32x4{PERM(R4, R3, sA), PERM(R3, R2, sB), PERM(R2, R1, sA), PERM(R1, R0, sB)});
        lds_st<u32x4>(k0, u32x4{PERM(R4, R3, sC), PERM(R3, R2, sD), PERM(R2, R1, sC), PERM(R1, R0, sE)});
        const u32 e00 = PERM(E0, E0, 0x01000100u), e11 = PERM(E0, E0, 0x03020302u);
        lds_st<u32x4>(k0 + KCOPY + 1024, u32x4{PERM(E1, E1, 0x01000100u), e11, PERM(E0, E0, 0x01000302u), e00});
        lds_st<u32x4>(k0 + 1024, u32x4{PERM(E0, E1, 0x07060100u), e11, e00, e00});
#pragma unroll
        for (int rr = 0; rr < 2; ++rr) {
          const u32 e0 = rr ? f2 : f0, e1 = rr ? f3 : f1, t4 = (lane & 1) ? e1 : e0;
          const u32 c00 = e0 | (e0 << 16), c11 = e1 | (e1 << 16);
          lds_st<u32x4>(k0 + KCOPY + 2048 + 1024 * rr, u32x4{c11, e1 | (t4 << 16), c00, c00});
          lds_st<u32x4>(k0 + 2048 + 1024 * rr, u32x4{c11, t4 | (e0 << 16), c00, c00});
        }
      }
#pragma unroll
      for (int k = 0; k < 16; ++k) lds_st<u32>(uw + (k >> 2) * 1024 + 3 * UCOPY + 12 + 4 * (k & 3), uv[k >> 2][k & 3]);
      post(L0 + C_BPREP + 4 * kq, 1u);
      STAMP(2);
#pragma unroll
      for (int A = 0; A < 8; ++A) ya[A] = f32x4{0.f, 0.f, 0.f, 0.f};
      const u32 ta = opaque(slot + KOFF + KPAD + (i16 & 1) * KCOPY + ((i16 >> 1) - g) * 16 - 64);
      const u32 ua = opaque(slot + (i16 & 3) * UCOPY + (i16 >> 2) * 128 + g * 16);
      u32x4 T[2][8], U[2][8];
#define CV_READ(S, st)                                                                                        \
      _Pragma("unroll") for (int D = 0; D < 8; ++D) {                                                         \
        T[S][D] = lds_ld<u32x4>(ta + ((st) >> 1) * 128 + D * 512 + (1 - ((st) & 1)) * 64);                   \
        U[S][D] = lds_ld<u32x4>(ua + UPAD - ((st) >> 1) * 128 + D * 512 + ((st) & 1) * 64);                  \
      }
      CV_READ(0, 0);
      __builtin_amdgcn_s_setprio(2);
#pragma unroll
      for (int st = 0; st < 8; ++st) {
        SB();
        if (st == 6) fhv = ld8(rws, fho);
        if (st < 7) { CV_READ((st + 1) & 1, st + 1); }
#pragma unroll
        for (int S = 0; S < 8; ++S)
#pragma unroll
          for (int D = 0; D <= S; ++D) ya[S] = mfma(T[st & 1][D], U[st & 1][S - D], ya[S]);
        if (st < 7) {
#pragma unroll
          for (int k = 0; k < 16; ++k) {
            __builtin_amdgcn_sched_group_barrier(0x8, 2, 0);
            __builtin_amdgcn_sched_group_barrier(0x100, 1, 0);
          }
          __builtin_amdgcn_sched_group_barrier(0x8, 4, 0);
        }
      }
      SB();
      STAMP(3);
    } else {
      get_tag();
      sw_wait4(L0 + C_FREE, 10);
      post(L0 + C_BPREP + 4 * kq, 1u);
      fhv = ld8(rws, fho);
#pragma unroll
      for (int A = 0; A < 8; ++A) ya[A] = f32x4{0.f, 0.f, 0.f, 0.f};
    }
    sw_wait(ccf, 1);
    G2_REGS(cr, 3, L_A0 + 5 * 10240);
    post(L0 + C_BDONE + 4 * kq, 1u);
    {
      const bool odd = lane & 1;
      const u32 bo = kq < nch ? WS_BT + (u32)ch * 8192 + (i16 >> 2) * 256 + g * 64 + (odd ? 0 : 32) + ((lane & 2) ? 0 : 16) : OOB;
#pragma unroll
      for (int A = 0; A < 8; ++A) {
        const u32 x0 = pk(ya[A].x, ya[A].y), x1 = pk(ya[A].z, ya[A].w);
        const u32 rv = __builtin_amdgcn_update_dpp(0u, odd ? x1 : x0, 0xB1, 0xf, 0xf, true);
        st16(rws, bo + A * 1024, u32x4{odd ? x0 : rv, tag, odd ? rv : x1, tag});
      }
    }
    if (RARE(!flag_ok(fhv, fho, tag))) poll_flags(rws, fho, tag);
    asm volatile("" ::: "memory");
    __builtin_amdgcn_s_setprio(0);
    STAMP(4);
    G2_LOADH();
    G2_INIT(L_B0);
    h_check<0, 6>(hf_, rws, ho);
    STAMP(8);
#pragma unroll
    for (int k = 0; k < 5; ++k) g2_half0<false>(y, cr[k], hf_[k]);
    sw_wait(ccf, 4);
    g2_half<0, 4>(y, P0, cr[5], hf_[5], cab[0][0]);
    STAMP(9);
    h_check<6, 12>(hf_, rws, ho);
    G2_U(3, 0, 5); G2_U(4, 1, 7); G2_U(5, 2, 9);
    h_check<12, 20>(hf_, rws, ho);
    G2_U(6, 3, 11); G2_U(7, 4, 12); G2_U(8, 5, 13); G2_ULAST(9, 6);
    G2_B_IMAGE();
  }

  hwbar();
  STAMP(12);
  if (b == 255 && tid == 0) __hip_atomic_store((GAS u32*)P.ws, tag, __ATOMIC_RELAXED, __HIP_MEMORY_SCOPE_AGENT);
  {
    const int et = tid >> 3, es = tid & 7;
    const u32 ia = opaque(L0 + L_IMG0 + et * 320 + ((es & 4) | ((es ^ (et >> 1)) & 3)) * 16);
    const float gt = lds_ld<float>(opaque(L0 + L_GATE + et * 4));
    const rsrc_t ro = mksrd(P.out, 2048 * 2560);
    const u32 oo = (64 * ti + et) * 2560 + dj * 320 + es * 16;
    const f32x4 o0 = lds_ld<f32x4>(ia) + gt * lds_ld<f32x4>(ia + (L_IMG1 - L_IMG0));
    const f32x4 o1 = lds_ld<f32x4>(ia + 128) + gt * lds_ld<f32x4>(ia + 128 + (L_IMG1 - L_IMG0));
    const f32x4 o2 = lds_ld<f32x4>(ia + 256) + gt * lds_ld<f32x4>(ia + 256 + (L_IMG1 - L_IMG0));
    st16(ro, oo, __builtin_bit_cast(u32x4, o0));
    st16(ro, oo + 128, __builtin_bit_cast(u32x4, o1));
    st16(ro, es < 4 ? oo + 256 : OOB, __builtin_bit_cast(u32x4, o2));
  }
  DUMP();
  STAMP(13);
}

extern "C" void kernel_launch(void* const* d_in, const int* in_sizes, int n_in, void* d_out, int out_size, void* d_ws,
                              size_t ws_size, hipStream_t stream) {
  Params p;
  p.x = (const u16*)d_in[0];
  p.Lre = (const u16*)d_in[1];
  p.Lim = (const u16*)d_in[2];
  p.Bre = (const u16*)d_in[3];
  p.Bim = (const u16*)d_in[4];
  p.Cre = (const u16*)d_in[5];
  p.Cim = (const u16*)d_in[6];
  p.ldtA = (const u16*)d_in[7];
  p.DA = (const u16*)d_in[8];
  p.Are = (const u16*)d_in[10];
  p.Aim = (const u16*)d_in[11];
  p.sBre = (const u16*)d_in[12];
  p.sBim = (const u16*)d_in[13];
  p.sCre = (const u16*)d_in[14];
  p.sCim = (const u16*)d_in[15];
  p.ldtB = (const u16*)d_in[16];
  p.DB = (const u16*)d_in[17];
  p.gw = (const u16*)d_in[18];
  p.out = (float*)d_out;
  p.ws = (char*)d_ws;
  for (int i = 0; i < 16; ++i) p.seq[i] = i + 1;
  fused_kernel<<<256, 512, 0, stream>>>(p);
}
```

```cpp
#include <hip/hip_runtime.h>
#include <stdint.h>

typedef unsigned short u16;
typedef unsigned int u32;
typedef unsigned long long u64;
typedef __bf16 bf16x8 __attribute__((ext_vector_type(8)));
typedef short s16x4 __attribute__((ext_vector_type(4)));
typedef float f32x4 __attribute__((ext_vector_type(4)));
typedef float f32x2 __attribute__((ext_vector_type(2)));
typedef u32 u32x4 __attribute__((ext_vector_type(4)));
typedef u32 u32x2 __attribute__((ext_vector_type(2)));
typedef u64 u64x8 __attribute__((ext_vector_type(8)));
typedef u64 u64x4 __attribute__((ext_vector_type(4)));
typedef __bf16 bf16x2_t __attribute__((ext_vector_type(2)));
typedef __amdgpu_buffer_rsrc_t rsrc_t;
#define LAS __attribute__((address_space(3)))
#define GAS __attribute__((address_space(1)))
#define DEV __device__ __forceinline__
#define INL __attribute__((always_inline))
#if defined(__HIP_DEVICE_COMPILE__)
#define NOPK __attribute__((target("no-packed-fp32-ops")))
#else
#define NOPK
#endif

constexpr u32 WS_E = 0;
constexpr u32 WS_FH = 4096;
constexpr u32 WS_FX = WS_FH + 1024 * 128;
constexpr u32 WS_SG = WS_FX + 1024 * 128;
constexpr u32 WS_GG = WS_SG + 16 * 640 * 16;
constexpr u32 WS_XT = WS_GG + 10 * 2048 * 8;
constexpr u32 WS_BT = WS_XT + 640 * 2048 * 2;
constexpr u32 WS_H = WS_BT + 640 * 1024 * 8;
constexpr u32 WS_END = WS_H + 128 * 40 * 1024;
constexpr u32 WS_DIAG = (WS_END + 4095) & ~4095u;

constexpr u32 STG = 26624;
constexpr u32 L_A0 = 81920;
constexpr u32 L_B0 = 0;
__attribute__((always_inline)) constexpr u32 ring(int s) { return s % 6 < 3 ? L_A0 + (s % 6) * STG : (s % 6 - 3) * STG; }
constexpr u32 BSLOT = 27008;
constexpr u32 L_CNT = 81024;
constexpr u32 C_FREE = L_CNT, C_PRO = L_CNT + 16, C_BAR = L_CNT + 32, C_BDONE = L_CNT + 48, C_C0 = L_CNT + 64, C_BPREP = L_CNT + 80,
              C_STRIP = L_CNT + 96, C_FULL = L_CNT + 112, C_CFULL = L_CNT + 116,
              C_G1 = L_CNT + 128, L_TAG = L_CNT + 160;
constexpr u32 L_GATE = L_CNT + 256;
constexpr u32 L_DSK = L_A0 + 3 * STG;
constexpr u32 L_GW = L_DSK + 320;
constexpr u32 L_RAW = ring(5);
constexpr u32 L_TAB = L_RAW + 10240;
constexpr u32 L_ERAW = L_A0;
constexpr u32 L_WS = L_A0 + 5120;
constexpr u32 L_HP = L_A0 + 6400;
constexpr u32 L_PT = L_A0 + 7936;
constexpr u32 L_HT = L_A0 + 13056;
constexpr u32 L_BST = L_A0;
constexpr u32 L_IMG0 = L_A0 + 10240, L_IMG1 = L_A0 + 30720;
constexpr u32 UCOPY = 4640, UPAD = 384, KOFF = 4 * UCOPY, KCOPY = 4224, KPAD = 112;
constexpr u32 KC_OFF = 16640;
constexpr u32 TROW = 288;

constexpr int SPIN_CAP = 1 << 16;

static __device__ const __attribute__((aligned(64))) u32 seq_tab[16] = {1, 2, 3, 4, 5, 6, 7, 8, 9, 10, 11, 12, 13, 14, 15, 16};

template <bool B> struct BoolC { static constexpr bool value = B; };

struct Params {
  const u16 *x, *Lre, *Lim, *Bre, *Bim, *Cre, *Cim, *ldtA, *DA;
  const u16 *Are, *Aim, *sBre, *sBim, *sCre, *sCim, *ldtB, *DB, *gw;
  float* out;
  char* ws;
};

DEV bool wave_all(bool p) { return __builtin_amdgcn_ballot_w64(!p) == 0; }
DEV float bf2f(u32 v) { return __builtin_bit_cast(float, v << 16); }
DEV float bflo(u32 v) { return __builtin_bit_cast(float, v << 16); }
DEV float bfhi(u32 v) { return __builtin_bit_cast(float, v & 0xffff0000u); }
DEV u32 pk(float a, float b) { bf16x2_t v = {(__bf16)a, (__bf16)b}; return __builtin_bit_cast(u32, v); }
DEV float rbf(float a) { return bflo(pk(a, a)); }
DEV u32 f2u(float a) { return __builtin_bit_cast(u32, a); }
DEV float u2f(u32 a) { return __builtin_bit_cast(float, a); }
DEV float swp(float x) { return u2f(__builtin_amdgcn_update_dpp(0, f2u(x), 0xB1, 0xf, 0xf, true)); }
DEV f32x2 rot(f32x2 z) { return f32x2{-z.y, z.x}; }
DEV f32x2 cmul(f32x2 c, f32x2 z, f32x2 zr) { return __builtin_elementwise_fma(f32x2{c.y, c.y}, zr, f32x2{c.x, c.x} * z); }
DEV f32x2 cmul(f32x2 c, f32x2 z) { return cmul(c, z, rot(z)); }
DEV f32x2 cpowk(float a, float th, float k) {
  const float L2E = 1.44269504088896f, IH = 0.15915494f, IL = 6.4206382e-9f;
  float m = __builtin_amdgcn_exp2f(k * a * L2E);
  float x = k * th;
  float hi = x * IH;
  float lo = __builtin_fmaf(x, IL, __builtin_fmaf(x, IH, -hi));
  float r = __builtin_amdgcn_fractf(hi) + lo;
  return f32x2{m * __builtin_amdgcn_cosf(r), m * __builtin_amdgcn_sinf(r)};
}
template <typename T> DEV T lds_ld(u32 a) { typedef T __attribute__((may_alias)) TA; return *(const LAS TA*)(size_t)a; }
template <typename T> DEV void lds_st(u32 a, T v) { typedef T __attribute__((may_alias)) TA; *(LAS TA*)(size_t)a = v; }
DEV s16x4 lds_tr(u32 a) { return __builtin_amdgcn_ds_read_tr16_b64_v4i16((LAS s16x4*)(size_t)a); }
DEV f32x4 mfma(u32x4 a, u32x4 b, f32x4 c) {
  return __builtin_amdgcn_mfma_f32_16x16x32_bf16(__builtin_bit_cast(bf16x8, a), __builtin_bit_cast(bf16x8, b), c, 0, 0, 0);
}
DEV rsrc_t mksrd(const void* p, u32 n) { return __builtin_amdgcn_make_buffer_rsrc((void*)p, (short)0, (int)n, 0x00020000); }
DEV void dma16(rsrc_t rs, u32 voff, u32 soff, u32 lds) {
  asm volatile("s_mov_b32 m0, %0\n\ts_nop 0\n\tbuffer_load_dwordx4 %1, %2, %3 offen lds"
               :: "s"(lds), "v"(voff), "s"(rs), "s"(soff) : "m0", "memory");
}
DEV void dma64v(rsrc_t rs, u32 v0, u32 v1, u32 v2, u32 v3, u32 so, u32 lds) {
  asm volatile("s_mov_b32 m0, %0\n\ts_nop 0\n\tbuffer_load_dwordx4 %1, %5, %6 offen lds\n\t"
               "buffer_load_dwordx4 %2, %5, %6 offen offset:1024 lds\n\tbuffer_load_dwordx4 %3, %5, %6 offen offset:2048 lds\n\t"
               "buffer_load_dwordx4 %4, %5, %6 offen offset:3072 lds"
               :: "s"(lds), "v"(v0), "v"(v1), "v"(v2), "v"(v3), "s"(rs), "s"(so) : "m0", "memory");
}
DEV void dma32v(rsrc_t rs, u32 v0, u32 v1, u32 so, u32 lds) {
  asm volatile("s_mov_b32 m0, %0\n\ts_nop 0\n\tbuffer_load_dwordx4 %1, %3, %4 offen lds\n\t"
               "buffer_load_dwordx4 %2, %3, %4 offen offset:1024 lds"
               :: "s"(lds), "v"(v0), "v"(v1), "s"(rs), "s"(so) : "m0", "memory");
}
template <int OFF> DEV void dmaq(rsrc_t r0, rsrc_t r1, u32 v0, u32 v1, u32 v2, u32 v3, u32 so, u32 slot) {
  asm volatile("s_add_u32 m0, %0, %8\n\ts_nop 0\n\tbuffer_load_dwordx4 %1, %5, %7 offen lds\n\t"
               "buffer_load_dwordx4 %2, %6, %7 offen offset:1024 lds\n\tbuffer_load_dwordx4 %3, %6, %7 offen offset:2048 lds\n\t"
               "buffer_load_dwordx4 %4, %6, %7 offen offset:3072 lds"
               :: "s"(slot), "v"(v0), "v"(v1), "v"(v2), "v"(v3), "s"(r0), "s"(r1), "s"(so), "n"(OFF) : "m0", "scc", "memory");
}
template <int OFF> DEV void dmad(rsrc_t r0, u32 v0, u32 v1, u32 so, u32 slot) {
  asm volatile("s_add_u32 m0, %0, %5\n\ts_nop 0\n\tbuffer_load_dwordx4 %1, %3, %4 offen lds\n\t"
               "buffer_load_dwordx4 %2, %3, %4 offen offset:1024 lds"
               :: "s"(slot), "v"(v0), "v"(v1), "s"(r0), "s"(so), "n"(OFF) : "m0", "scc", "memory");
}
DEV void dma_mark(rsrc_t rs, u32 so, u32 lds) {
  u64 keep;
  asm volatile("s_mov_b32 m0, %1\n\ts_mov_b64 %0, exec\n\ts_mov_b64 exec, 1\n\tbuffer_load_dword off, %2, %3 lds\n\ts_mov_b64 exec, %0"
               : "=&s"(keep) : "s"(lds), "s"(rs), "s"(so) : "m0", "memory");
}
DEV u32 opaque(u32 v) { asm volatile("" : "+v"(v)); return v; }
#define SB() __builtin_amdgcn_sched_barrier(0)
#define RARE(x) __builtin_expect(!!(x), 0)
template <int N> DEV void waitv() { asm volatile("s_waitcnt vmcnt(%0)" :: "n"(N) : "memory"); }
DEV void waitl() { asm volatile("s_waitcnt lgkmcnt(0)" ::: "memory"); }
DEV void hwbar() { waitl(); __builtin_amdgcn_s_barrier(); asm volatile("" ::: "memory"); }
DEV void post(u32 a, u32 v) {
  asm volatile("" ::: "memory");
  lds_st<u32>(a, v);
  asm volatile("" ::: "memory");
}
DEV u32 min4(u32x4 v) { return (u32)__builtin_amdgcn_readfirstlane(min(min(v.x, v.y), min(v.z, v.w))); }
DEV u32x4 peek4(u32 a) { asm volatile("" ::: "memory"); return lds_ld<u32x4>(a); }
DEV void sw_wait4(u32 a, u32 target) {
#pragma unroll 1
  for (u32 it = 0; it < (1u << 22) && min4(peek4(a)) < target; ++it) {}
  asm volatile("" ::: "memory");
}
DEV void sw_wait(u32 a, u32 target) {
  u32 v, sv, cnt = 1u << 22;
  asm volatile("1:\n\tds_read_b32 %0, %4\n\ts_sub_u32 %2, %2, 1\n\ts_cselect_b32 %3, 0, %3\n\ts_waitcnt lgkmcnt(0)\n\t"
               "v_readfirstlane_b32 %1, %0\n\ts_cmp_lt_u32 %1, %3\n\ts_cbranch_scc1 1b"
               : "=&v"(v), "=&s"(sv), "+s"(cnt), "+s"(target) : "v"(a) : "scc", "memory");
}
DEV u32 peek(u32 a) { return __hip_atomic_load((LAS u32*)(size_t)a, __ATOMIC_RELAXED, __HIP_MEMORY_SCOPE_WORKGROUP); }
DEV u32x4 ld16(rsrc_t rs, u32 off) { return __builtin_amdgcn_raw_buffer_load_b128(rs, off, 0, 16); }
DEV u32x2 ld8(rsrc_t rs, u32 off) { return __builtin_amdgcn_raw_buffer_load_b64(rs, off, 0, 16); }
DEV void st16(rsrc_t rs, u32 off, u32x4 v) { __builtin_amdgcn_raw_buffer_store_b128(v, rs, off, 0, 16); }
DEV void st8(rsrc_t rs, u32 off, u32x2 v) { __builtin_amdgcn_raw_buffer_store_b64(v, rs, off, 0, 16); }
constexpr u32 OOB = 0x7ff00000u;
constexpr u32 FMIX = 0x9E3779B9u;
DEV bool flag_ok(u32x2 v, u32 off, u32 tag) { return wave_all((off == OOB) | ((v.x == tag) & (v.y == (tag ^ FMIX)))); }
DEV void poll_flags(rsrc_t rs, u32 off, u32 tag) {
#pragma unroll 1
  for (int it = 0; it < SPIN_CAP; ++it) {
    asm volatile("" ::: "memory");
    if (flag_ok(ld8(rs, off), off, tag)) break;
    __builtin_amdgcn_s_sleep(1);
  }
  asm volatile("" ::: "memory");
}

#define TR(k) do {} while (0)
#define DUMP() do {} while (0)
#define STAMP(k) do {} while (0)
#define CSTAMP(k) do {} while (0)

#define SCAN_STEP(o, v, h)                                                                                             \
  asm("v_fma_f32 %0, %15, %10, %5\n\tv_fma_f32 %1, %16, %11, %6\n\tv_fma_f32 %2, %17, %12, %7\n\t"                   \
      "v_fma_f32 %3, %18, %13, %8\n\tv_fma_f32 %4, %19, %14, %9\n\t"                                                   \
      "v_fmac_f32_dpp %0, %10, %20 quad_perm:[1,0,3,2] row_mask:0xf bank_mask:0xf\n\t"                                 \
      "v_fmac_f32_dpp %1, %11, %21 quad_perm:[1,0,3,2] row_mask:0xf bank_mask:0xf\n\t"                                 \
      "v_fmac_f32_dpp %2, %12, %22 quad_perm:[1,0,3,2] row_mask:0xf bank_mask:0xf\n\t"                                 \
      "v_fmac_f32_dpp %3, %13, %23 quad_perm:[1,0,3,2] row_mask:0xf bank_mask:0xf\n\t"                                 \
      "v_fmac_f32_dpp %4, %14, %24 quad_perm:[1,0,3,2] row_mask:0xf bank_mask:0xf"                                     \
      : "=&v"(o[0]), "=&v"(o[1]), "=&v"(o[2]), "=&v"(o[3]), "=&v"(o[4])                                                \
      : "v"(v[0]), "v"(v[1]), "v"(v[2]), "v"(v[3]), "v"(v[4]), "v"(h[0]), "v"(h[1]), "v"(h[2]), "v"(h[3]), "v"(h[4]), \
        "v"(ma[0]), "v"(ma[1]), "v"(ma[2]), "v"(ma[3]), "v"(ma[4]), "v"(mb[0]), "v"(mb[1]), "v"(mb[2]), "v"(mb[3]),   \
        "v"(mb[4]))
DEV void scan_step(float (&o)[5], const float (&v)[5], const float (&h)[5], const float (&ma)[5], const float (&mb)[5]) {
  float t[5];
  SCAN_STEP(t, v, h);
#pragma unroll
  for (int i = 0; i < 5; ++i) o[i] = t[i];
}

struct Frag { u32x4 a0, a1, b0, b1, b2, b3, b4; };
#define MF(d, a, b) "v_mfma_f32_16x16x32_bf16 %" #d ", %" #a ", %" #b ", %" #d "\n\t"
#define RA(d, o) "ds_read_b128 %" #d ", %24 offset:" #o "\n\t"
#define RB(d, o) "ds_read_b128 %" #d ", %25 offset:" #o "\n\t"
#define WL(n) "s_waitcnt lgkmcnt(" #n ")\n\t"
#define G1_BODY(w1, w2, w3, w5, w7, w9)                                                                    \
  w1 MF(0, 17, 19) RA(10, 0) w2 MF(5, 18, 19) RB(12, 0) w3 MF(1, 17, 20) RA(11, 2048) MF(6, 18, 20) RB(13, 1024) \
  w5 MF(2, 17, 21) RB(14, 2048) MF(7, 18, 21) RB(15, 3072) w7 MF(3, 17, 22) RB(16, 4096) MF(8, 18, 22)    \
  w9 MF(4, 17, 23) MF(9, 18, 23)
#define G1_ACC(A) "+v"(A[0][0]), "+v"(A[0][1]), "+v"(A[0][2]), "+v"(A[0][3]), "+v"(A[0][4]), "+v"(A[1][0]), "+v"(A[1][1]), "+v"(A[1][2]), "+v"(A[1][3]), "+v"(A[1][4])
#define G1_OUT(N) "=&v"(N.a0), "=&v"(N.a1), "=&v"(N.b0), "=&v"(N.b1), "=&v"(N.b2), "=&v"(N.b3), "=&v"(N.b4)
#define G1_IN(C) "v"(C.a0), "v"(C.a1), "v"(C.b0), "v"(C.b1), "v"(C.b2), "v"(C.b3), "v"(C.b4)
template <bool WAITS> DEV void g1_half(f32x4 (&A)[2][5], Frag& N, const Frag& C, u32 pa, u32 pb) {
  if constexpr (WAITS)
    asm volatile(G1_BODY(WL(5), WL(5), WL(5), WL(6), WL(7), WL(7)) : G1_ACC(A), G1_OUT(N) : G1_IN(C), "v"(pa), "v"(pb) : "memory");
  else
    asm volatile(G1_BODY("", "", "", "", "", "") : G1_ACC(A), G1_OUT(N) : G1_IN(C), "v"(pa), "v"(pb) : "memory");
}
DEV u32 g1_half_peek(f32x4 (&A)[2][5], Frag& N, const Frag& C, u32 pa, u32 pb, u32 pf) {
  u32 t = pf;
  asm volatile("ds_read_b32 %0, %0" : "+v"(t) :: "memory");
  asm volatile(G1_BODY(WL(6), WL(6), WL(6), WL(7), WL(8), WL(8)) WL(7) : G1_ACC(A), G1_OUT(N) : G1_IN(C), "v"(pa), "v"(pb) : "memory");
  u32 r;
  asm volatile("v_readfirstlane_b32 %0, %1" : "=s"(r) : "v"(t) : "memory");
  return r;
}
DEV void g1_last(f32x4 (&A)[2][5], const Frag& C) {
  asm volatile(WL(5) MF(0, 10, 12) WL(4) MF(5, 11, 12) WL(3) MF(1, 10, 13) MF(6, 11, 13) WL(2) MF(2, 10, 14) MF(7, 11, 14)
               WL(1) MF(3, 10, 15) MF(8, 11, 15) WL(0) MF(4, 10, 16) MF(9, 11, 16) "s_nop 7"
               : G1_ACC(A) : G1_IN(C) : "memory");
}

struct CF { u32x4 f[5]; };
DEV u32 peek_issue(u32 a) { asm volatile("ds_read_b32 %0, %0" : "+v"(a) :: "memory"); return a; }
DEV u32 peek_get(u32 t) { u32 r; asm volatile("s_waitcnt lgkmcnt(5)\n\tv_readfirstlane_b32 %0, %1" : "=s"(r) : "v"(t) : "memory"); return r; }
#define M2(d, a) "v_mfma_f32_16x16x32_bf16 %" #d ", %" #a ", %15, %" #d "\n\t"
#define R2(d, o) "ds_read_b128 %" #d ", %16 offset:%17+" #o "\n\t"
template <int OFF, int WT> DEV void g2_half(f32x4 (&y)[5], CF& N, const CF& Cc, u32x4 h, u32 pa) {
  asm volatile("s_waitcnt lgkmcnt(%18)\n\t" M2(0, 10) R2(5, 0) "s_waitcnt lgkmcnt(%18)\n\t" M2(1, 11) R2(6, 2048) "s_waitcnt lgkmcnt(%18)\n\t" M2(2, 12) R2(7, 4096)
               "s_waitcnt lgkmcnt(%18)\n\t" M2(3, 13) R2(8, 6144) "s_waitcnt lgkmcnt(%18)\n\t" M2(4, 14) R2(9, 8192)
               : "+v"(y[0]), "+v"(y[1]), "+v"(y[2]), "+v"(y[3]), "+v"(y[4]), "=&v"(N.f[0]), "=&v"(N.f[1]), "=&v"(N.f[2]), "=&v"(N.f[3]), "=&v"(N.f[4])
               : "v"(Cc.f[0]), "v"(Cc.f[1]), "v"(Cc.f[2]), "v"(Cc.f[3]), "v"(Cc.f[4]), "v"(h), "v"(pa), "n"(OFF), "n"(WT) : "memory");
}
#define M3(d, a) "v_mfma_f32_16x16x32_bf16 %" #d ", %" #a ", %10, %" #d "\n\t"
template <bool LAST> DEV void g2_half0(f32x4 (&y)[5], const CF& Cc, u32x4 h) {
  if constexpr (LAST)
    asm volatile(WL(4) M3(0, 5) WL(3) M3(1, 6) WL(2) M3(2, 7) WL(1) M3(3, 8) WL(0) M3(4, 9) "s_nop 7"
                 : "+v"(y[0]), "+v"(y[1]), "+v"(y[2]), "+v"(y[3]), "+v"(y[4])
                 : "v"(Cc.f[0]), "v"(Cc.f[1]), "v"(Cc.f[2]), "v"(Cc.f[3]), "v"(Cc.f[4]), "v"(h) : "memory");
  else
    asm volatile(M3(0, 5) M3(1, 6) M3(2, 7) M3(3, 8) M3(4, 9)
                 : "+v"(y[0]), "+v"(y[1]), "+v"(y[2]), "+v"(y[3]), "+v"(y[4])
                 : "v"(Cc.f[0]), "v"(Cc.f[1]), "v"(Cc.f[2]), "v"(Cc.f[3]), "v"(Cc.f[4]), "v"(h) : "memory");
}

NOPK __global__ void __launch_bounds__(512, 2) fused_kernel(Params) {
  __shared__ __attribute__((aligned(16))) char smem[163840];
  asm volatile("" :: "v"((u32)(uintptr_t)(LAS char*)smem));
  constexpr u32 L0 = 0;
  const u64 kap = (u64)__builtin_amdgcn_kernarg_segment_ptr();
  u64x8 k0, k1; u64x4 k2;
  asm volatile("s_load_dwordx16 %0, %3, 0x0\n\ts_load_dwordx16 %1, %3, 0x40\n\ts_load_dwordx8 %2, %3, 0x80"
               : "=&s"(k0), "=&s"(k1), "=&s"(k2) : "s"(kap));
  lds_st<u32>(L0 + L_CNT + min(threadIdx.x & 63, 39u) * 4, 0u);
  asm volatile("s_waitcnt lgkmcnt(0)\n\ts_barrier" : "+s"(k0), "+s"(k1), "+s"(k2) :: "memory");
  struct { const u16 *x, *Lre, *Lim, *Bre, *Bim, *Cre, *Cim, *ldtA, *DA, *Are, *Aim, *sBre, *sBim, *sCre, *sCim, *ldtB, *DB, *gw; float* out; char* ws; }
  const P = {(const u16*)k0[0], (const u16*)k0[1], (const u16*)k0[2], (const u16*)k0[3], (const u16*)k0[4], (const u16*)k0[5], (const u16*)k0[6],
             (const u16*)k0[7], (const u16*)k1[0], (const u16*)k1[1], (const u16*)k1[2], (const u16*)k1[3], (const u16*)k1[4], (const u16*)k1[5],
             (const u16*)k1[6], (const u16*)k1[7], (const u16*)k2[0], (const u16*)k2[1], (float*)k2[2], (char*)k2[3]};
  const int tid = threadIdx.x, lane = tid & 63;
  const int w = __builtin_amdgcn_readfirstlane(tid >> 6);
  const int b = blockIdx.x, xcd = b & 7, r = b >> 3;
  const int c = 4 * (xcd >> 1) + (r >> 3), j = 8 * (xcd & 1) + (r & 7);
  const int ti = 4 * xcd + (r >> 3), dj = r & 7;
  const int i16 = lane & 15, g = lane >> 4;
  const rsrc_t rws = mksrd(P.ws, WS_END);
  const rsrc_t rx = mksrd(P.x, 2048 * 1280);
  const rsrc_t rCre = mksrd(P.Cre, 640 * 1280), rCim = mksrd(P.Cim, 640 * 1280);
  const rsrc_t rka = mksrd(seq_tab, 64);
  constexpr u32 SEQ = 0;
  const int kq = w & 3, kh = w >> 2;
  const int rot10 = ((r & 7) + 2 * (r >> 3) + 3 * xcd) % 10;
  u32 tag;
  STAMP(0);
  TR(0);
  const u32 cf0 = i16 * 128 + ((g ^ ((i16 >> 1) & 7)) << 4);
  const u32 ccf = L0 + C_CFULL;
  const u32 fho = lane < 8 ? WS_FH + (u32)((2 * ti + (kq >> 1)) * 16 + 8 * kh + lane) * 128 : OOB;
  const u32 ho = WS_H + (u32)((4 * ti + kq) * 40 + 20 * kh) * 1024 + lane * 16;
  const u32 ima = L0 + (kh ? L_IMG1 : L_IMG0) + (16 * kq + i16) * 320 + (g ^ ((i16 >> 1) & 3)) * 16;
#define G2_REGS(name, n, at)                                                                                \
  CF name[2 * n];                                                                                           \
  { const u32 a0 = opaque(L0 + (at) + cf0), a1 = opaque(L0 + (at) + (cf0 ^ 64));                            \
    _Pragma("unroll") for (int k = 0; k < 2 * n; ++k)                                                       \
    _Pragma("unroll") for (int nt = 0; nt < 5; ++nt) name[k].f[nt] = lds_ld<u32x4>(((k & 1) ? a1 : a0) + (k >> 1) * 10240 + nt * 2048); \
    _Pragma("unroll") for (int k = 0; k < 2 * n; ++k)                                                       \
      asm volatile("s_waitcnt lgkmcnt(0)" : "+v"(name[k].f[0]), "+v"(name[k].f[1]), "+v"(name[k].f[2]), "+v"(name[k].f[3]), "+v"(name[k].f[4]) :: "memory"); }
#define G2_LOADH()                                                                                          \
  u32x4 hf_[20];                                                                                            \
  _Pragma("unroll") for (int ks = 0; ks < 20; ++ks) hf_[ks] = ld16(rws, ho + ks * 1024);                    \
  SB();
#define G2_INIT(at)                                                                                         \
  f32x4 y[5];                                                                                               \
  _Pragma("unroll") for (int nt = 0; nt < 5; ++nt) y[nt] = f32x4{0.f, 0.f, 0.f, 0.f};                       \
  CF P0, P1;                                                                                                \
  const u32 cab[2][2] = {{opaque(L0 + (at) + cf0), opaque(L0 + (at) + (cf0 ^ 64))},                         \
                         {opaque(L0 + (at) + 40960 + cf0), opaque(L0 + (at) + 40960 + (cf0 ^ 64))}};        \
  asm volatile("s_nop 1" : "+v"(y[0]), "+v"(y[1]), "+v"(y[2]), "+v"(y[3]), "+v"(y[4]));
#define G2_U(uu, slot, need)                                                                                \
  { const u32 t_ = peek_issue(ccf);                                                                         \
    g2_half<((slot) & 3) * 10240, 5>(y, P1, P0, hf_[2 * (uu)], cab[(slot) >> 2][1]);                        \
    if (RARE(peek_get(t_) < (need))) sw_wait(ccf, need);                                                    \
    g2_half<(((slot) + 1) & 3) * 10240, 4>(y, P0, P1, hf_[2 * (uu) + 1], cab[((slot) + 1) >> 2][0]); }
#define G2_UN(uu, slot)                                                                                     \
  g2_half<((slot) & 3) * 10240, 4>(y, P1, P0, hf_[2 * (uu)], cab[(slot) >> 2][1]);                          \
  g2_half<(((slot) + 1) & 3) * 10240, 4>(y, P0, P1, hf_[2 * (uu) + 1], cab[((slot) + 1) >> 2][0]);
#define G2_ULAST(uu, slot)                                                                                  \
  g2_half<((slot) & 3) * 10240, 4>(y, P1, P0, hf_[2 * (uu)], cab[(slot) >> 2][1]);                          \
  g2_half0<true>(y, P1, hf_[2 * (uu) + 1]);
#define G2_B_IMAGE()                                                                                        \
  { STAMP(11);                                                                                              \
    sw_wait4(L0 + C_BAR, 4);                                                                                \
    const u32 ia = opaque(ima);                                                                             \
    _Pragma("unroll") for (int nt = 0; nt < 5; ++nt) lds_st<f32x4>(ia + nt * 64, y[nt]); }
  auto get_tag = [&]() INL { sw_wait(L0 + C_FULL, 1); tag = __builtin_amdgcn_readfirstlane(lds_ld<u32>(opaque(L0 + L_TAG))) + 1u; };

  const int pl = lane < 40 ? lane : 39;
  u32 pLre, pLim, pLdt;
  if (w < 4) {
    pLre = ((const GAS u16*)P.Lre)[40 * j + pl]; pLim = ((const GAS u16*)P.Lim)[40 * j + pl]; pLdt = ((const GAS u16*)P.ldtA)[40 * j + pl];
    asm volatile("" ::: "memory");
#pragma unroll
    for (int k = 0; k < 3; ++k) {
      const int qq = 64 * k + lane, t = (qq * 6554) >> 16, cc = qq - 10 * t;
      if (k < 2 || lane < 32) dma16(rx, (u32)(64 * ti + 16 * w + t) * 1280 + dj * 160 + cc * 16, 0, L0 + L_RAW + w * 2560 + k * 1024);
    }
    dma_mark(rka, SEQ, L0 + C_STRIP + 4 * w);
  }
  if (__builtin_expect(w == 7, 1)) {
      __builtin_amdgcn_s_setprio(3);
      const rsrc_t rBre = mksrd(P.Bre, 640 * 1280), rBim = mksrd((const char*)P.Bim - 1024, 640 * 1280 + 1024);
      const int q8 = lane >> 3;
      u32 vx[4], vb[10];
      {
        const u32 x0 = (u32)(128 * c + 8 * (q8 >> 2) + (q8 & 3)) * 1280 + (((lane & 7) ^ (q8 >> 1)) << 4);
        vx[0] = x0; vx[1] = (x0 ^ 64) + 16 * 1280 - 1024; vx[2] = x0 + 4 * 1280 - 2048; vx[3] = (x0 ^ 64) + 20 * 1280 - 3072;
        const u32 b0 = (u32)(40 * j + q8) * 1280 + (((lane & 7) ^ (q8 & 6)) << 4), b1 = (b0 ^ 16) + 1024;
#pragma unroll
        for (int k = 0; k < 10; ++k) vb[k] = (k < 5 ? b0 : b1) + 10240 * (k % 5) - 1024 * (k & 3);
      }
      u32 so = rot10 * 128;
      {
        u64 keep;
        asm volatile("s_mov_b32 m0, %1\n\ts_mov_b64 %0, exec\n\ts_mov_b64 exec, 1\n\tbuffer_load_dword off, %2, 0 sc1 lds\n\ts_mov_b64 exec, %0"
                     : "=&s"(keep) : "s"(L0 + L_TAG), "s"(rws) : "m0", "memory");
      }
      auto head = [&](u32 sl) INL {
        dmaq<0>(rx, rx, vx[0], vx[1], vx[2], vx[3], so, sl);
        dmaq<4096>(rx, rx, vx[0], vx[1], vx[2], vx[3], so + 40960, sl);
        dmaq<8192>(rx, rx, vx[0], vx[1], vx[2], vx[3], so + 81920, sl);
        dmaq<12288>(rx, rx, vx[0], vx[1], vx[2], vx[3], so + 122880, sl);
        dmaq<16384>(rBre, rBre, vb[0], vb[1], vb[2], vb[3], so, sl);
      };
      auto tail = [&](u32 sl) INL {
        dmaq<20480>(rBre, rBim, vb[4], vb[5], vb[6], vb[7], so, sl);
        dmad<24576>(rBim, vb[8], vb[9], so, sl);
        so = so == 1152 ? 0 : so + 128;
      };
#pragma unroll
      for (int s = 0; s < 10; ++s) {
        const u32 sb = L0 + ring(s);
        if (s == 5) sw_wait4(L0 + C_PRO, 1);
        if (s == 6 || s == 8) sw_wait4(L0 + C_FREE, s - 4);
        TR(4 + 4 * s);
        if (s >= 2) waitv<34>();
        TR(5 + 4 * s);
        head(sb);
        tail(sb);
        TR(6 + 4 * s);
        if (s == 0 && lane < 10) {
          dma16(mksrd(P.DA, 1280), lane * 16, dj * 160, L0 + L_DSK);
          dma16(mksrd(P.DB, 1280), lane * 16, dj * 160, L0 + L_DSK + 160);
          dma16(mksrd(P.gw, 1280), lane * 16, rot10 * 128, L0 + L_GW);
        }
        dma_mark(rka, SEQ + 4 * s, L0 + C_FULL);
        if (s == 0) __builtin_amdgcn_s_setprio(1);
      }
      STAMP(2);
      u32 vc[4];
#pragma unroll
      for (int k = 0; k < 4; ++k) vc[k] = (u32)(80 * dj + q8) * 1280 + (((lane & 7) ^ (lane >> 4) ^ (4 * (k & 1))) << 4) + 9216 * k;
      auto stageC = [&](int u, u32 dst) INL {
        const rsrc_t rs = (u & 1) ? rCim : rCre;
        const u32 so = (u32)(u >> 1) * 128;
        dma64v(rs, vc[0], vc[1], vc[2], vc[3], so, dst);
        dma64v(rs, vc[0], vc[1], vc[2], vc[3], so + 40960, dst + 4096);
        dma32v(rs, vc[0], vc[1], so + 81920, dst + 8192);
      };
      {
        u32 t0 = 0;
#pragma unroll
        for (int k = 0; k < 7; ++k) {
          const u32 ln = min(r * 400 + k * 64 + lane, r * 400 + 399);
          asm volatile("buffer_load_dword %0, %1, %2, 0 offen" : "+v"(t0) : "v"((ln >= 6400 ? ln - 6400 : ln) * 128), "s"(r >= 16 ? rCim : rCre) : "memory");
        }
        get_tag();
        asm volatile("s_waitcnt vmcnt(0)" : "+v"(t0) :: "memory");
        post(L0 + C_BPREP + 12, 1u);
      }
      sw_wait4(L0 + C_BPREP, 1);
      sw_wait4(L0 + C_BAR, 2);
      stageC(10, L0 + L_A0 + 5 * 10240); stageC(11, L0 + L_A0 + 6 * 10240); stageC(12, L0 + L_A0 + 7 * 10240);
      stageC(0, L0 + L_A0 + 3 * 10240); stageC(1, L0 + L_A0 + 4 * 10240);
      dma_mark(rka, SEQ, ccf);
      stageC(4, L0 + L_A0 + 2 * 10240);
      sw_wait(ccf, 1);
      G2_REGS(cr, 3, L_A0 + 5 * 10240);
      post(L0 + C_BDONE + 12, 1u);
      sw_wait4(L0 + C_BAR, 3);
      stageC(2, L0 + L_A0); stageC(3, L0 + L_A0 + 10240);
      dma_mark(rka, SEQ + 4, ccf);
      STAMP(3);
      sw_wait4(L0 + C_C0, 1);
      waitv<30>();
      stageC(5, L0 + L_A0 + 3 * 10240); stageC(6, L0 + L_A0 + 4 * 10240);
      dma_mark(rka, SEQ + 8, ccf);
      poll_flags(rws, fho, tag);
      STAMP(8);
      u32x4 hf_[20];
#pragma unroll
      for (int ks = 0; ks < 20; ++ks)
        asm volatile("buffer_load_dwordx4 %0, %1, %2, %3 offen offset:%4 sc1" : "=v"(hf_[ks]) : "v"(ho), "s"(rws), "s"((ks >> 2) * 4096), "n"((ks & 3) * 1024) : "memory");
      G2_INIT(L_B0);
      sw_wait4(L0 + C_BDONE, 1);
      STAMP(4);
#define FILL(q, unit, at) stageC(unit, L0 + (at)); dma_mark(rka, SEQ + 4 * (3 + (q)), ccf);
      FILL(0, 13, L_B0); FILL(1, 14, L_B0 + 10240); waitv<50>(); FILL(2, 7, L_A0 + 5 * 10240);
      asm volatile("s_waitcnt vmcnt(33)" : "+v"(hf_[0]), "+v"(hf_[1]), "+v"(hf_[2]), "+v"(hf_[3]), "+v"(hf_[4]), "+v"(hf_[5]), "+v"(hf_[6]), "+v"(hf_[7]), "+v"(hf_[8]), "+v"(hf_[9]) :: "memory");
      asm volatile("" : "+v"(hf_[10]), "+v"(hf_[11]), "+v"(hf_[12]), "+v"(hf_[13]), "+v"(hf_[14]), "+v"(hf_[15]), "+v"(hf_[16]), "+v"(hf_[17]), "+v"(hf_[18]), "+v"(hf_[19]));
      FILL(3, 15, L_B0 + 2 * 10240);

      FILL(4, 8, L_A0 + 6 * 10240);
      g2_half0<false>(y, cr[0], hf_[0]); g2_half0<false>(y, cr[1], hf_[1]);
      waitv<40>(); FILL(5, 16, L_B0 + 3 * 10240);
      g2_half0<false>(y, cr[2], hf_[2]); g2_half0<false>(y, cr[3], hf_[3]);
      waitv<40>(); FILL(6, 9, L_A0 + 7 * 10240);
      g2_half0<false>(y, cr[4], hf_[4]); g2_half<0, 4>(y, P0, cr[5], hf_[5], cab[0][0]);
      waitv<40>(); FILL(7, 17, L_B0 + 4 * 10240);
      G2_UN(3, 0);
      waitv<40>(); FILL(8, 18, L_B0 + 5 * 10240);
      G2_UN(4, 1);
      waitv<40>(); FILL(9, 19, L_B0 + 6 * 10240);
      STAMP(5);
      __builtin_amdgcn_s_setprio(0);
      G2_U(5, 2, 9); G2_U(6, 3, 11); G2_U(7, 4, 12); G2_U(8, 5, 13); G2_ULAST(9, 6);
      G2_B_IMAGE();
  } else if (__builtin_expect(w < 4, 1)) {
    __builtin_amdgcn_s_setprio(2);
    f32x4 acc[2][5], accg[2] = {f32x4{0.f, 0.f, 0.f, 0.f}, f32x4{0.f, 0.f, 0.f, 0.f}};
#pragma unroll
    for (int m = 0; m < 2; ++m)
#pragma unroll
      for (int nt = 0; nt < 5; ++nt) acc[m][nt] = f32x4{0.f, 0.f, 0.f, 0.f};
    asm volatile("" : G1_ACC(acc));
    const u32 ax0 = opaque(L0 + (32 * w + i16) * 128 + ((g ^ ((i16 >> 1) & 7)) << 4));
    const u32 ab0 = opaque(L0 + 16384 + (40 * (i16 & 1) + (i16 >> 1)) * 128 + ((g ^ (2 * (i16 >> 2) + (i16 & 1))) << 4));
    const u32 ax1 = opaque(ax0 ^ 64), ab1 = opaque(ab0 ^ 64);
    const u32 cfull = opaque(L0 + C_FULL), cfree = opaque(L0 + C_FREE), myfree = opaque(L0 + C_FREE + 4 * w);
    const u32 xra = opaque(L0 + L_RAW + (32 * (g >> 1) + 8 * (lane & 3) + ((lane >> 2) & 3)) * 160 + 40 * w + 8 * (g & 1));
    const u32 xrq = opaque(L0 + L_RAW + (16 * w + i16) * 160 + 8 * g);
    const u32 xo = opaque(WS_XT + (u32)(20 * dj + 5 * w + (g & 1)) * 16384 + (8 * ti + 4 * (g >> 1)) * 64 + i16 * 16);
    const u32 xo2 = opaque((g & 1) ? OOB : xo + 65536);
    const u32 fxo_ = opaque(lane == 0 ? WS_FX + (u32)((ti * 8 + dj) * 4 + w) * 128 : OOB);
    const u32 tb = opaque(L0 + L_TAB + w * 3200 + pl * 16), tp = opaque(L0 + L_TAB + w * 3200 + (i16 >> 1) * 16), tg = opaque(tp + g * 640);
    const u32 pa0 = opaque(ax0 + L_A0), pb0 = opaque(ab0 + L_A0), pa1 = opaque(ax1 + L_A0);
    asm volatile("" : "+v"(pLre), "+v"(pLim), "+v"(pLdt));
    STAMP(14);
    const float lre = bf2f(pLre), lim = bf2f(pLim);
    const float dtA = rbf(expf(bf2f(pLdt)));
    const float aA = dtA * lre, tA = dtA * lim;
    const f32x2 lam = cpowk(aA, tA, 1.f), l8 = cpowk(aA, tA, 8.f);
    f32x2 c2;
    {
      const float nr = lam.x - 1.f, ni = lam.y, den = 2.f * __builtin_amdgcn_rcpf(lre * lre + lim * lim);
      c2 = f32x2{(nr * lre + ni * lim) * den, (ni * lre - nr * lim) * den};
    }
    f32x2 zs[4], zh[4];
    zs[3] = cpowk(aA, tA, (float)(96 - 32 * w));
    zh[0] = cmul(cpowk(aA, tA, (float)(32 * w)), c2);
#pragma unroll
    for (int k = 1; k < 4; ++k) { zs[3 - k] = cmul(zs[4 - k], l8); zh[k] = cmul(zh[k - 1], l8); }
    const int sig = 4 * w + g;
    float lr[5], ls[5], sa[5], sb[5], ha[5], hb[5], ca[5], cb[5];
    {
#pragma unroll
      for (int k = 0; k < 4; ++k) lds_st<f32x4>(tb + k * 640, f32x4{zs[k].x, zs[k].y, zh[k].x, -zh[k].y});
      lds_st<f32x4>(tb + 4 * 640, f32x4{lam.x, lam.y, c2.x, -c2.y});
      const float sg = (lane & 1) ? 1.f : -1.f;
#pragma unroll
      for (int nt = 0; nt < 5; ++nt) {
        const f32x4 q1 = lds_ld<f32x4>(tp + 4 * 640 + nt * 128), q0 = lds_ld<f32x4>(tg + nt * 128);
        sa[nt] = q0.x; sb[nt] = sg * q0.y; ha[nt] = -sg * q0.z; hb[nt] = q0.w;
        lr[nt] = q1.x; ls[nt] = sg * q1.y; ca[nt] = -sg * q1.z; cb[nt] = q1.w;
        asm volatile("" : "+v"(sb[nt]), "+v"(ha[nt]), "+v"(ls[nt]), "+v"(ca[nt]));
      }
    }
    STAMP(15);
    u32x2 xq[5];
    {
        sw_wait4(L0 + C_STRIP, 1);
        u32x2 lo[3], hi[3];
#pragma unroll
        for (int k = 0; k < 3; ++k) { lo[k] = __builtin_bit_cast(u32x2, lds_tr(xra + k * 16)); hi[k] = __builtin_bit_cast(u32x2, lds_tr(xra + k * 16 + 640)); }
#pragma unroll
        for (int nt = 0; nt < 5; ++nt) xq[nt] = lds_ld<u32x2>(xrq + 32 * nt);
        post(L0 + C_PRO + 4 * w, 1u);
#pragma unroll
        for (int k = 0; k < 3; ++k) st16(rws, k < 2 ? xo + k * 32768 : xo2, u32x4{lo[k].x, lo[k].y, hi[k].x, hi[k].y});
        asm volatile("s_waitcnt lgkmcnt(0)" : "+v"(xq[0]), "+v"(xq[1]), "+v"(xq[2]), "+v"(xq[3]), "+v"(xq[4]) :: "memory");
      }
    STAMP(1);
    Frag X, Y;
    u32x2 eda[5], edb[5];
    sw_wait(cfull, 1);
    X.a0 = lds_ld<u32x4>(pa0); X.b0 = lds_ld<u32x4>(pb0); X.a1 = lds_ld<u32x4>(pa0 + 2048); X.b1 = lds_ld<u32x4>(pb0 + 1024);
    X.b2 = lds_ld<u32x4>(pb0 + 2048); X.b3 = lds_ld<u32x4>(pb0 + 3072); X.b4 = lds_ld<u32x4>(pb0 + 4096);
    const u32 seed = lds_ld<u32>(opaque(L0 + L_TAG));
    const u32 dk = opaque(L0 + L_DSK + 8 * g);
#pragma unroll
    for (int nt = 0; nt < 5; ++nt) { eda[nt] = lds_ld<u32x2>(dk + 32 * nt); edb[nt] = lds_ld<u32x2>(dk + 160 + 32 * nt); asm volatile("" : "+v"(eda[nt]), "+v"(edb[nt])); }
    const u32 gwa = opaque(L0 + L_GW + g * 16);
    const u32x4 gw0 = lds_ld<u32x4>(gwa), gw1 = lds_ld<u32x4>(gwa + 64);
    accg[0] = mfma(lds_ld<u32x4>(pa1), gw1, mfma(X.a0, gw0, accg[0]));
    accg[1] = mfma(lds_ld<u32x4>(pa1 + 2048), gw1, mfma(X.a1, gw0, accg[1]));
    asm volatile("s_waitcnt lgkmcnt(0)" : "+v"(X.a0), "+v"(X.a1), "+v"(X.b0), "+v"(X.b1), "+v"(X.b2), "+v"(X.b3), "+v"(X.b4));
    tag = __builtin_amdgcn_readfirstlane(seed) + 1u;
    const u32x2 fxv = {tag, tag ^ FMIX};
    u32 so = L_A0;
#pragma unroll
    for (int s = 0; s < 4; s += 2) {
      u32 have = g1_half_peek(acc, Y, X, ax1 + so, ab1 + so, cfull);
      so = s == 2 ? 0 : so + STG;
      TR(4 + 4 * s);
      if (RARE(have < (u32)(s + 2))) sw_wait(cfull, s + 2);
      g1_half<true>(acc, X, Y, ax0 + so, ab0 + so);
      have = g1_half_peek(acc, Y, X, ax1 + so, ab1 + so, cfull);
      so += STG;
      post(myfree, s + 2);
      TR(5 + 4 * s);
      if (RARE(have < (u32)(s + 3))) sw_wait(cfull, s + 3);
      g1_half<true>(acc, X, Y, ax0 + so, ab0 + so);
      TR(6 + 4 * s);
    }
    waitv<0>();
    st8(rws, fxo_, fxv);
    {
      u32 have = g1_half_peek(acc, Y, X, ax1 + ring(4), ab1 + ring(4), cfull);
      if (RARE(have < 6u)) sw_wait(cfull, 6);
      g1_half<true>(acc, X, Y, ax0 + ring(5), ab0 + ring(5));
      have = g1_half_peek(acc, Y, X, ax1 + ring(5), ab1 + ring(5), cfull);
      if (RARE(have < 7u)) sw_wait(cfull, 7);
      g1_half<true>(acc, X, Y, ax0 + ring(6), ab0 + ring(6));
      TR(28);
      have = g1_half_peek(acc, Y, X, ax1 + ring(6), ab1 + ring(6), cfull);
      if (RARE(have < 8u)) sw_wait(cfull, 8);
      g1_half<true>(acc, X, Y, ax0 + ring(7), ab0 + ring(7));
      have = g1_half_peek(acc, Y, X, ax1 + ring(7), ab1 + ring(7), cfull);
      if (RARE(have < 9u)) sw_wait(cfull, 9);
      g1_half<true>(acc, X, Y, ax0 + ring(8), ab0 + ring(8));
      TR(30);
      have = g1_half_peek(acc, Y, X, ax1 + ring(8), ab1 + ring(8), cfull);
      if (RARE(have < 10u)) sw_wait(cfull, 10);
      TR(32);
      g1_half<true>(acc, X, Y, ax0 + ring(9), ab0 + ring(9));
      g1_half<true>(acc, Y, X, ax1 + ring(9), ab1 + ring(9));
      g1_last(acc, Y);
      post(L0 + C_G1 + 4 * w, 1u);
    }
    const u32x4 fr10 = peek4(L0 + C_G1);
    STAMP(2);
    TR(44);
    float v[8][5];
#pragma unroll
    for (int jj = 0; jj < 8; ++jj)
#pragma unroll
      for (int nt = 0; nt < 5; ++nt) v[jj][nt] = acc[jj >> 2][nt][jj & 3];
    float e[5];
#pragma unroll
    for (int nt = 0; nt < 5; ++nt) e[nt] = v[0][nt];
#pragma unroll
    for (int jj = 1; jj < 8; ++jj) scan_step(e, v[jj], e, lr, ls);
    {
      float z[5] = {0.f, 0.f, 0.f, 0.f, 0.f}, we[5];
      scan_step(we, z, e, sa, sb);
      auto s01 = __builtin_amdgcn_permlane32_swap(f2u(we[0]), f2u(we[1]), false, false);
      auto s23 = __builtin_amdgcn_permlane32_swap(f2u(we[2]), f2u(we[3]), false, false);
      auto s44 = __builtin_amdgcn_permlane32_swap(f2u(we[4]), f2u(we[4]), false, false);
      const float y01 = u2f(s01[0]) + u2f(s01[1]), y23 = u2f(s23[0]) + u2f(s23[1]), y4 = u2f(s44[0]) + u2f(s44[1]);
      auto q03 = __builtin_amdgcn_permlane16_swap(f2u(y01), f2u(y23), false, false);
      auto q44 = __builtin_amdgcn_permlane16_swap(f2u(y4), f2u(y4), false, false);
      if (RARE(min4(fr10) < 1u)) sw_wait4(L0 + C_G1, 1);
      const u32 wa = opaque(L0 + L_WS + w * 320 + i16 * 4);
      lds_st<float>(wa + (2 * (g & 1) + (g >> 1)) * 64, u2f(q03[0]) + u2f(q03[1]));
      lds_st<float>(wa + 256, u2f(q44[0]) + u2f(q44[1]));
      const u32 ra = opaque(L0 + L_ERAW + sig * 320 + i16 * 4);
#pragma unroll
      for (int nt = 0; nt < 5; ++nt) lds_st<float>(ra + nt * 64, e[nt]);
    }
    post(L0 + C_BAR + 4 * w, 1u);
    sw_wait4(L0 + C_BAR, 1);
    if (w == 2) {
      const u32 ap = opaque(L0 + L_WS + pl * 8);
      const f32x2 se = (lds_ld<f32x2>(ap) + lds_ld<f32x2>(ap + 320)) + (lds_ld<f32x2>(ap + 640) + lds_ld<f32x2>(ap + 960));
      st16(rws, lane < 40 ? WS_SG + (u32)(c * 640 + 40 * j + lane) * 16 : OOB, u32x4{f2u(se.x), tag, f2u(se.y), tag});
    }
    STAMP(3);
    const int nvalid = (c - w + 3) >> 2;
    const f32x2 lam4 = cpowk(aA, tA, 512.f);
    f32x2 lame = cpowk(aA, tA, (float)(128 * ((c - 1 - w) & 3)));
    asm volatile("" : "+v"(lame));
    if ((xcd & 1) ? ((r & 7) >= 2 && (r & 7) < 7) : (r & 7) < 5) {
      float gl = accg[0][0];
#pragma unroll
      for (int k = 1; k < 8; ++k) gl = (i16 == k) ? accg[k >> 2][k & 3] : gl;
      st8(rws, i16 < 8 ? WS_GG + (u32)(rot10 * 2048 + 128 * c + 32 * w + 8 * g + i16) * 8 : OOB, u32x2{f2u(gl), tag});
    }
    float pf[5];
    {
      u32 era = opaque(L0 + L_ERAW + pl * 8);
      const f32x2 l8r = rot(l8);
      f32x2 pp = {0.f, 0.f};
#pragma unroll 1
      for (int s2 = 0; s2 < w; ++s2, era += 1280) {
        const f32x2 e0 = lds_ld<f32x2>(era), e1 = lds_ld<f32x2>(era + 320), e2 = lds_ld<f32x2>(era + 640), e3 = lds_ld<f32x2>(era + 960);
        pp = cmul(pp, l8, l8r) + e0; pp = cmul(pp, l8, l8r) + e1; pp = cmul(pp, l8, l8r) + e2; pp = cmul(pp, l8, l8r) + e3;
      }
      const f32x2 e0 = lds_ld<f32x2>(era), e1 = lds_ld<f32x2>(era + 320), e2 = lds_ld<f32x2>(era + 640);
      const u32 ptb = L0 + L_PT + w * 1280;
      const u32 pw = opaque(ptb + pl * 8);
#pragma unroll
      for (int k = 0; k < 4; ++k) {
        const f32x2 q = cmul(pp, c2);
        lds_st<f32x2>(pw + k * 320, f32x2{q.x, -q.y});
        if (k < 3) pp = cmul(pp, l8, l8r) + (k == 0 ? e0 : k == 1 ? e1 : e2);
      }
      const u32 pr_ = opaque(ptb + g * 320 + i16 * 4);
#pragma unroll
      for (int nt = 0; nt < 5; ++nt) pf[nt] = lds_ld<float>(pr_ + nt * 64);
      const float z[5] = {0.f, 0.f, 0.f, 0.f, 0.f};
#pragma unroll
      for (int jj = 0; jj < 8; ++jj) scan_step(v[jj], z, v[jj], ca, cb);
#pragma unroll
      for (int nt = 0; nt < 5; ++nt) ls[nt] = -ls[nt];
    }
    u32 hst[5];
    const u32 hpr = opaque(L0 + L_HP + i16 * 16), htw = opaque(L0 + L_HT + w * 1280 + i16 * 80 + g * 16),
              htr = opaque(L0 + L_HT + w * 1280 + (2 * ((lane >> 2) & 3) + (g & 1)) * 80 + (g >> 1) * 32 + (lane & 3) * 8);
    {
      const u32 hb0 = WS_H + (u32)(8 * c + 2 * w + (g >> 1)) * 40960 + (g & 1) * 2048 + i16 * 16;
#pragma unroll
      for (int nt = 0; nt < 5; ++nt) {
        const int o = 5 * j + nt;
        hst[nt] = opaque(hb0 + (u32)((o >> 3) * 4096 + ((o >> 2) & 1) * 1024 + (o & 3) * 256));
      }
      u32 off[4];
      f32x2 mk[3];
#pragma unroll
      for (int k = 0; k < 4; ++k) off[k] = opaque((k < nvalid && lane < 40) ? WS_SG + (u32)((w + 4 * k) * 640 + 40 * j + lane) * 16 : OOB);
#pragma unroll
      for (int k = 0; k < 3; ++k) { mk[k] = k + 1 < nvalid ? lam4 : f32x2{1.f, 0.f}; asm volatile("" : "+v"(mk[k])); }
      const u32 hp = opaque(L0 + L_HP + min(lane, 40) * 32 + w * 4);
      u32x4 sv[4];
#pragma unroll 1
      for (int it = 0; it < SPIN_CAP; ++it) {
        asm volatile("" ::: "memory");
        bool ok = true;
#pragma unroll
        for (int k = 0; k < 4; ++k) sv[k] = ld16(rws, off[k]);
#pragma unroll
        for (int k = 0; k < 4; ++k) ok &= (off[k] == OOB) | ((sv[k].y == tag) & (sv[k].w == tag));
        if (wave_all(ok)) break;
        __builtin_amdgcn_s_sleep(1);
      }
      asm volatile("" ::: "memory");
      STAMP(4);
      f32x2 hsum = {u2f(sv[0].x), u2f(sv[0].z)};
#pragma unroll
      for (int k = 1; k < 4; ++k) hsum = cmul(hsum, mk[k - 1]) + f32x2{u2f(sv[k].x), u2f(sv[k].z)};
      hsum = cmul(hsum, lame);
      lds_st<float>(hp, hsum.x);
      lds_st<float>(hp + 16, hsum.y);
    }
    post(L0 + C_BAR + 4 * w, 2u);
    sw_wait4(L0 + C_BAR, 2);
    {
      float hin[5], h0[5];
#pragma unroll
      for (int nt = 0; nt < 5; ++nt) {
        const f32x4 q = lds_ld<f32x4>(hpr + nt * 256);
        hin[nt] = (q.x + q.y) + (q.z + q.w);
      }
      scan_step(h0, pf, hin, ha, hb);
      scan_step(v[0], v[0], h0, lr, ls);
#pragma unroll
      for (int jj = 1; jj < 8; ++jj) scan_step(v[jj], v[jj], v[jj - 1], lr, ls);
    }
    {
      const u32 wa = htw, ra = htr;
      u32x2 lo[5], hi[5];
#pragma unroll
      for (int nt = 0; nt < 5; ++nt) {
        lds_st<u32x4>(wa, u32x4{pk(v[0][nt], v[1][nt]), pk(v[2][nt], v[3][nt]), pk(v[4][nt], v[5][nt]), pk(v[6][nt], v[7][nt])});
        lo[nt] = __builtin_bit_cast(u32x2, lds_tr(ra));
        hi[nt] = __builtin_bit_cast(u32x2, lds_tr(ra + 640));
      }
      SB();
#pragma unroll
      for (int nt = 0; nt < 5; ++nt) st16(rws, hst[nt], u32x4{lo[nt].x, lo[nt].y, hi[nt].x, hi[nt].y});
    }
    post(L0 + C_BAR + 4 * w, 3u);
    STAMP(5);
    const u32 go = WS_GG + (u32)(g * 2048 + 64 * ti + 16 * kq + i16) * 8, go2 = g < 2 ? go + 8 * 2048 * 8 : OOB;
    u32x2 g0 = ld8(rws, go), g1 = ld8(rws, go + 4 * 2048 * 8), g2 = ld8(rws, go2);
    waitv<0>();
    st8(rws, lane == 0 ? WS_FH + (u32)((c * 4 + w) * 16 + j) * 128 : OOB, u32x2{tag, tag ^ FMIX});
    sw_wait(ccf, 1);
    G2_REGS(cr, 2, L_A0 + 3 * 10240);
    dma_mark(rka, SEQ, L0 + C_C0 + 4 * w);
#pragma unroll 1
    for (int it = 0; it < SPIN_CAP && RARE(!wave_all((g0.y == tag) & (g1.y == tag) & ((g2.y == tag) | (g >= 2)))); ++it) {
      __builtin_amdgcn_s_sleep(1);
      asm volatile("" ::: "memory");
      g0 = ld8(rws, go); g1 = ld8(rws, go + 4 * 2048 * 8); g2 = ld8(rws, go2);
    }
    float gate, ogate;
    {
      const float s3 = (u2f(g0.x) + u2f(g1.x)) + u2f(g2.x);
      auto sa_ = __builtin_amdgcn_permlane32_swap(f2u(s3), f2u(s3), false, false);
      const float s2 = u2f(sa_[0]) + u2f(sa_[1]);
      auto sb_ = __builtin_amdgcn_permlane16_swap(f2u(s2), f2u(s2), false, false);
      gate = rbf(1.f / (1.f + expf(-rbf(u2f(sb_[0]) + u2f(sb_[1])))));
      ogate = rbf(1.f - gate);
    }
    u32x2 ska[5], skb[5];
#pragma unroll
    for (int nt = 0; nt < 5; ++nt) {
#pragma unroll
      for (int e = 0; e < 2; ++e) {
        const float x0 = bflo(xq[nt][e]), x1 = bfhi(xq[nt][e]);
        ska[nt][e] = pk(x0 * bflo(eda[nt][e]), x1 * bfhi(eda[nt][e]));
        skb[nt][e] = pk(x0 * bflo(edb[nt][e]), x1 * bfhi(edb[nt][e]));
      }
      asm volatile("" : "+v"(ska[nt]), "+v"(skb[nt]));
    }
    poll_flags(rws, fho, tag);
    asm volatile("" ::: "memory");
    __builtin_amdgcn_s_setprio(0);
    STAMP(6);
    G2_LOADH();
    STAMP(7);
    G2_INIT(L_A0);

    g2_half0<false>(y, cr[0], hf_[0]); g2_half0<false>(y, cr[1], hf_[1]); g2_half0<false>(y, cr[2], hf_[2]);
    sw_wait(ccf, 2);
    g2_half<0, 4>(y, P0, cr[3], hf_[3], cab[0][0]);
    STAMP(8);

    G2_UN(2, 0); G2_UN(3, 1); G2_U(4, 2, 3);

    G2_UN(5, 3);

    STAMP(9);
    G2_U(6, 4, 6);
    lds_st<float>(opaque(L0 + L_GATE + (16 * w + i16) * 4), gate);
    post(L0 + C_BAR + 4 * w, 4u);
    waitl();
    const u32 bo = WS_BT + (u32)(80 * dj + (lane >> 2)) * 8192 + (64 * ti + 16 * w) * 4 + (lane & 3) * 16;
    u32x4 bv[5];
#pragma unroll
    for (int k = 0; k < 5; ++k) bv[k] = ld16(rws, bo + k * 16 * 8192);
    SB();
    G2_U(7, 5, 8);
    waitl();
    STAMP(10);
#pragma unroll 1
    for (int it = 0; it < SPIN_CAP; ++it) {
      bool ok = true;
#pragma unroll
      for (int k = 0; k < 5; ++k) ok &= (bv[k].y == tag) & (bv[k].w == tag);
      if (!RARE(!wave_all(ok))) break;
      __builtin_amdgcn_s_sleep(2);
      asm volatile("" ::: "memory");
#pragma unroll
      for (int k = 0; k < 5; ++k) bv[k] = ld16(rws, bo + k * 16 * 8192);
    }
    STAMP(11);
    sw_wait4(L0 + C_BAR, 4);
    f32x4 base[5];
    {
      const u32 ba = opaque(L0 + L_BST + w * 2560 + lane * 8);
#pragma unroll
      for (int k = 0; k < 5; ++k) lds_st<u32x2>(ba + k * 512, u32x2{bv[k].x, bv[k].z});
#pragma unroll
      for (int nt = 0; nt < 5; ++nt) {
        const u32x2 yb = __builtin_bit_cast(u32x2, lds_tr(ba + nt * 512));
#pragma unroll
        for (int e = 0; e < 2; ++e) {
          const u32 bb = pk(bflo(yb[e]) + bflo(skb[nt][e]), bfhi(yb[e]) + bfhi(skb[nt][e]));
          const u32 ob = pk(ogate * bflo(bb), ogate * bfhi(bb));
          base[nt][2 * e] = gate * bflo(ska[nt][e]) + bflo(ob);
          base[nt][2 * e + 1] = gate * bfhi(ska[nt][e]) + bfhi(ob);
        }
        asm volatile("" : "+v"(base[nt]));
      }
    }
    G2_U(8, 6, 10); G2_ULAST(9, 7);
    {
      const u32 ia = opaque(ima);
#pragma unroll
      for (int nt = 0; nt < 5; ++nt) lds_st<f32x4>(ia + nt * 64, gate * y[nt] + base[nt]);
    }
  } else {
    const int pr = xcd * 16 + (r >> 1);
    const int nch = (r & 1) ? 2 : 3, ch = 5 * pr + ((r & 1) ? 3 : 0) + kq;
    const u32 slot = opaque(L0 + L_B0 + kq * BSLOT) ;
    u32x2 fhv;
    f32x4 ya[8];
    if (__builtin_expect(kq < nch, 1)) {
      const GAS u32* pa = (const GAS u32*)P.Are + ch * 64 + lane;
      const u32 wAre = *pa, wAim = ((const GAS u32*)P.Aim)[ch * 64 + lane];
      const u32 wBre = ((const GAS u32*)P.sBre)[ch * 64 + lane], wBim = ((const GAS u32*)P.sBim)[ch * 64 + lane];
      const u32 wCre = ((const GAS u32*)P.sCre)[ch * 64 + lane], wCim = ((const GAS u32*)P.sCim)[ch * 64 + lane];
      const u32 wdt = ((const GAS u16*)P.ldtB)[ch];
      const float dt = rbf(expf(bf2f(wdt)));
      f32x4 ka[3] = {f32x4{0.f, 0.f, 0.f, 0.f}, f32x4{0.f, 0.f, 0.f, 0.f}, f32x4{0.f, 0.f, 0.f, 0.f}};
      const u32 tr = opaque(slot + i16 * TROW + g * 16);
      const u32 fxo = lane < 32 ? WS_FX + (u32)((lane * 8 + ch / 80) * 4 + (ch % 80) / 20) * 128 : OOB;
      u32 tv[56], tu[56];
#pragma unroll 1
      for (int rd = 0; rd < 2; ++rd) {
#pragma unroll
        for (int k = 0; k < 56; ++k) tu[k] = tv[k];
        const u32 sh = rd ? 0u : 16u;
        const float a = dt * bfhi(wAre << sh), t = dt * bfhi(wAim << sh);
        const f32x2 db = {dt * bfhi(wBre << sh), dt * bfhi(wBim << sh)}, cc = {bfhi(wCre << sh), bfhi(wCim << sh)};
        const f32x2 W = cmul(cc, db);
        const f32x2 z1 = cpowk(a, t, 1.f), z8 = cpowk(a, t, 8.f), z128 = cpowk(a, t, 128.f);
        {
          f32x2 cv = z8;
          tv[41] = pk(cv.x, cv.y);
#pragma unroll
          for (int jj = 2; jj < 16; ++jj) { cv = cmul(cv, z8); tv[40 + jj] = pk(cv.x, cv.y); }
        }
        {
          const f32x2 y1 = {z1.x, -z1.y}, yh = {z128.x, -z128.y};
          f32x2 R[8];
          R[0] = f32x2{W.x, -W.y};
#pragma unroll
          for (int k = 1; k < 8; ++k) R[k] = cmul(R[k - 1], y1);
#pragma unroll
          for (int k = 0; k < 8; ++k) tv[k] = pk(R[k].x, R[k].y);
#pragma unroll
          for (int k = 0; k < 8; ++k) { R[k] = cmul(R[k], yh); tv[8 + k] = pk(R[k].x, R[k].y); }
#pragma unroll
          for (int hh = 0; hh < 2; ++hh)
#pragma unroll
            for (int k = 0; k < 4; ++k) { R[2 * k] = cmul(R[2 * k], yh); tv[16 + 4 * hh + k] = pk(R[2 * k].x, R[2 * k].y); }
#pragma unroll
          for (int hh = 0; hh < 4; ++hh)
#pragma unroll
            for (int k = 0; k < 2; ++k) { R[4 * k] = cmul(R[4 * k], yh); tv[24 + 2 * hh + k] = pk(R[4 * k].x, R[4 * k].y); }
#pragma unroll
          for (int hh = 0; hh < 9; ++hh) { R[0] = cmul(R[0], yh); tv[32 + hh] = pk(R[0].x, R[0].y); }
        }
      }
      STAMP(1);
      get_tag();
      const u32 kc = slot + KC_OFF;
      const bool r1 = lane < 32;
      const u32 xa = opaque(kc - 4 + (r1 ? 16 * lane : 256 + 8 * lane)), xb = opaque(xa + (r1 ? 12 : 8));
      const u32 sA = opaque(r1 ? 0x05040706u : 0x03020504u), sB = opaque(r1 ? 0x05040706u : 0x05040504u), sC = opaque(r1 ? 0x03020504u : 0x01000302u),
                sD = opaque(r1 ? 0x03020504u : 0x05040504u), sE = opaque(lane <= 32 ? 0x03020504u : 0x05040504u);
      const u32 ka0 = opaque(kc + (128 * (g >> 1) + 8 * i16 + 4 * (g & 1)) * 2);
      const u32 ka1 = opaque(kc + (g < 2 ? 256 + 64 * g + 4 * i16 : 256 + 64 * g + 2 * i16) * 2), ka1b = opaque(ka1 + (g < 2 ? 4 : 64));
      const u32 ka2 = opaque(kc + (512 + 64 * min(g, 2) + i16) * 2);
      const u32 sbase = __builtin_amdgcn_readfirstlane(slot);
      const u32 uo = opaque(WS_XT + (u32)(ch >> 2) * 16384 + (ch & 3) * 16 + lane * 64), uw = opaque(slot + UPAD + lane * 16);
      sw_wait(L0 + C_FULL, 10);
      const u32x2 fx = ld8(rws, fxo);
      sw_wait4(L0 + C_G1, 1);
      STAMP(6);
#define TW(v, row) asm volatile("ds_write_addtid_b32 %0 offset:%1" :: "v"(v), "n"((row) * TROW) : "memory")
      asm volatile("s_mov_b32 m0, %0\n\ts_nop 0" :: "s"(sbase) : "m0", "memory");
      TW(0x00003f80u, 41);
      u32x4 uv[4];
#pragma unroll
      for (int rd = 0; rd < 2; ++rd) {
#pragma unroll
        for (int k = 0; k < 56; ++k) TW(rd ? tv[k] : tu[k], k < 41 ? k : k + 1);
        u32x4 fr[4][4];
#pragma unroll
        for (int ks = 0; ks < 4; ++ks)
#pragma unroll
          for (int m = 0; m < 4; ++m) fr[ks][m] = lds_ld<u32x4>(tr + (m == 0 ? 41 : 16 * (m - 1)) * TROW + ks * 64);
        SB();
#pragma unroll
        for (int ks = 0; ks < 4; ++ks)
#pragma unroll
          for (int m = 0; m < 3; ++m) ka[m] = mfma(fr[ks][m + 1], fr[ks][0], ka[m]);
        SB();
      }
      if (RARE(!flag_ok(fx, fxo, tag))) poll_flags(rws, fxo, tag);
      asm volatile("" ::: "memory");
#pragma unroll
      for (int k = 0; k < 4; ++k) uv[k] = ld16(rws, uo + k * 4096);
      SB();
#define UW(v, off) asm volatile("ds_write_addtid_b32 %0 offset:%1" :: "v"(v), "n"(off) : "memory")
#pragma unroll
      for (int cc = 0; cc < 4; ++cc) { UW(0u, cc * UCOPY); UW(0u, cc * UCOPY + 144); }
      STAMP(5);
      {
        const u32 q0 = pk(ka[2].x, ka[2].y), q1 = pk(ka[2].z, ka[2].w);
        if (g < 3) { lds_st<u16>(ka2, (u16)q0); lds_st<u16>(ka2 + 32, (u16)(q0 >> 16)); lds_st<u16>(ka2 + 64, (u16)q1); lds_st<u16>(ka2 + 96, (u16)(q1 >> 16)); }
        lds_st<u32x2>(ka0, u32x2{pk(ka[0].x, ka[0].y), pk(ka[0].z, ka[0].w)});
        lds_st<u32>(ka1, pk(ka[1].x, ka[1].y));
        lds_st<u32>(ka1b, pk(ka[1].z, ka[1].w));
        lds_st<u32>(kc - 4, 0u);
      }
#define PERM(hi, lo, sel) __builtin_amdgcn_perm(hi, lo, sel)
      {
        const u32 R0 = lds_ld<u32>(xa), R1 = lds_ld<u32>(xa + 4), R2 = lds_ld<u32>(xa + 8), R3 = lds_ld<u32>(xb), R4 = lds_ld<u32>(xb + 4);
        const u32 ea_ = opaque(kc + 768 + 4 * lane), eb_ = opaque(kc + 1024 + 2 * lane);
        const u32 E0 = lds_ld<u32>(ea_), E1 = lds_ld<u32>(ea_ + 4);
        const u32 f0 = lds_ld<u16>(eb_), f1 = lds_ld<u16>(eb_ + 2), f2 = lds_ld<u16>(eb_ + 128), f3 = lds_ld<u16>(eb_ + 130);
        UW(0u, KOFF); UW(0u, KOFF + KCOPY);
        const u32 k0 = opaque(slot + KOFF + KPAD + lane * 16);
        lds_st<u32x4>(k0 + KCOPY, u32x4{PERM(R4, R3, sA), PERM(R3, R2, sB), PERM(R2, R1, sA), PERM(R1, R0, sB)});
        lds_st<u32x4>(k0, u32x4{PERM(R4, R3, sC), PERM(R3, R2, sD), PERM(R2, R1, sC), PERM(R1, R0, sE)});
        const u32 e00 = PERM(E0, E0, 0x01000100u), e11 = PERM(E0, E0, 0x03020302u);
        lds_st<u32x4>(k0 + KCOPY + 1024, u32x4{PERM(E1, E1, 0x01000100u), e11, PERM(E0, E0, 0x01000302u), e00});
        lds_st<u32x4>(k0 + 1024, u32x4{PERM(E0, E1, 0x07060100u), e11, e00, e00});
#pragma unroll
        for (int rr = 0; rr < 2; ++rr) {
          const u32 e0 = rr ? f2 : f0, e1 = rr ? f3 : f1, t4 = (lane & 1) ? e1 : e0;
          const u32 c00 = e0 | (e0 << 16), c11 = e1 | (e1 << 16);
          lds_st<u32x4>(k0 + KCOPY + 2048 + 1024 * rr, u32x4{c11, e1 | (t4 << 16), c00, c00});
          lds_st<u32x4>(k0 + 2048 + 1024 * rr, u32x4{c11, t4 | (e0 << 16), c00, c00});
        }
      }
#pragma unroll
      for (int k = 0; k < 4; ++k) {
        lds_st<u32x4>(uw + k * 1024, uv[k]);
#pragma unroll
        for (int e = 0; e < 4; ++e) { lds_st<u32>(uw + k * 1024 + UCOPY + 4 + 4 * e, uv[k][e]); lds_st<u32>(uw + k * 1024 + 3 * UCOPY + 12 + 4 * e, uv[k][e]); }
        lds_st<u32x2>(uw + k * 1024 + 2 * UCOPY + 8, u32x2{uv[k].x, uv[k].y});
        lds_st<u32x2>(uw + k * 1024 + 2 * UCOPY + 16, u32x2{uv[k].z, uv[k].w});
      }
      post(L0 + C_BPREP + 4 * kq, 1u);
      STAMP(2);
#pragma unroll
      for (int A = 0; A < 8; ++A) ya[A] = f32x4{0.f, 0.f, 0.f, 0.f};
      const u32 ta = opaque(slot + KOFF + KPAD + (i16 & 1) * KCOPY + ((i16 >> 1) - g) * 16 - 64);
      const u32 ua = opaque(slot + (i16 & 3) * UCOPY + (i16 >> 2) * 128 + g * 16);
      u32x4 T[2][8], U[2][8];
#define CV_READ(S, st)                                                                                        \
      _Pragma("unroll") for (int D = 0; D < 8; ++D) {                                                         \
        T[S][D] = lds_ld<u32x4>(ta + ((st) >> 1) * 128 + D * 512 + (1 - ((st) & 1)) * 64);                   \
        U[S][D] = lds_ld<u32x4>(ua + UPAD - ((st) >> 1) * 128 + D * 512 + ((st) & 1) * 64);                  \
      }
      CV_READ(0, 0);
      __builtin_amdgcn_s_setprio(2);
#pragma unroll
      for (int st = 0; st < 8; ++st) {
        SB();
        if (st == 6) fhv = ld8(rws, fho);
        if (st < 7) { CV_READ((st + 1) & 1, st + 1); }
#pragma unroll
        for (int S = 0; S < 8; ++S)
#pragma unroll
          for (int D = 0; D <= S; ++D) ya[S] = mfma(T[st & 1][D], U[st & 1][S - D], ya[S]);
        if (st < 7) {
#pragma unroll
          for (int k = 0; k < 16; ++k) {
            __builtin_amdgcn_sched_group_barrier(0x8, 2, 0);
            __builtin_amdgcn_sched_group_barrier(0x100, 1, 0);
          }
          __builtin_amdgcn_sched_group_barrier(0x8, 4, 0);
        }
      }
      SB();
      STAMP(3);
    } else {
      get_tag();
      sw_wait4(L0 + C_G1, 1);
      post(L0 + C_BPREP + 4 * kq, 1u);
      fhv = ld8(rws, fho);
#pragma unroll
      for (int A = 0; A < 8; ++A) ya[A] = f32x4{0.f, 0.f, 0.f, 0.f};
    }
    sw_wait(ccf, 1);
    G2_REGS(cr, 3, L_A0 + 5 * 10240);
    post(L0 + C_BDONE + 4 * kq, 1u);
    {
      const bool odd = lane & 1;
      const u32 bo = kq < nch ? WS_BT + (u32)ch * 8192 + (i16 >> 2) * 256 + g * 64 + (odd ? 0 : 32) + ((lane & 2) ? 0 : 16) : OOB;
#pragma unroll
      for (int A = 0; A < 8; ++A) {
        const u32 x0 = pk(ya[A].x, ya[A].y), x1 = pk(ya[A].z, ya[A].w);
        const u32 rv = __builtin_amdgcn_update_dpp(0u, odd ? x1 : x0, 0xB1, 0xf, 0xf, true);
        st16(rws, bo + A * 1024, u32x4{odd ? x0 : rv, tag, odd ? rv : x1, tag});
      }
    }
    if (RARE(!flag_ok(fhv, fho, tag))) poll_flags(rws, fho, tag);
    asm volatile("" ::: "memory");
    __builtin_amdgcn_s_setprio(0);
    STAMP(4);
    G2_LOADH();
    G2_INIT(L_B0);

    STAMP(8);
#pragma unroll
    for (int k = 0; k < 5; ++k) g2_half0<false>(y, cr[k], hf_[k]);
    sw_wait(ccf, 4);
    g2_half<0, 4>(y, P0, cr[5], hf_[5], cab[0][0]);
    STAMP(9);

    G2_U(3, 0, 5); G2_U(4, 1, 7); G2_U(5, 2, 9);

    G2_U(6, 3, 11); G2_U(7, 4, 12); G2_U(8, 5, 13); G2_ULAST(9, 6);
    G2_B_IMAGE();
  }

  hwbar();
  STAMP(12);
  if (b == 255 && tid == 0) __hip_atomic_store((GAS u32*)P.ws, tag, __ATOMIC_RELAXED, __HIP_MEMORY_SCOPE_AGENT);
  {
    const int et = tid >> 3, es = tid & 7;
    const u32 ia = opaque(L0 + L_IMG0 + et * 320 + ((es & 4) | ((es ^ (et >> 1)) & 3)) * 16);
    const float gt = lds_ld<float>(opaque(L0 + L_GATE + et * 4));
    const rsrc_t ro = mksrd(P.out, 2048 * 2560);
    const u32 oo = (64 * ti + et) * 2560 + dj * 320 + es * 16;
    const f32x4 o0 = lds_ld<f32x4>(ia) + gt * lds_ld<f32x4>(ia + (L_IMG1 - L_IMG0));
    const f32x4 o1 = lds_ld<f32x4>(ia + 128) + gt * lds_ld<f32x4>(ia + 128 + (L_IMG1 - L_IMG0));
    const f32x4 o2 = lds_ld<f32x4>(ia + 256) + gt * lds_ld<f32x4>(ia + 256 + (L_IMG1 - L_IMG0));
    st16(ro, oo, __builtin_bit_cast(u32x4, o0));
    st16(ro, oo + 128, __builtin_bit_cast(u32x4, o1));
    st16(ro, es < 4 ? oo + 256 : OOB, __builtin_bit_cast(u32x4, o2));
  }
  DUMP();
  STAMP(13);
}

extern "C" void kernel_launch(void* const* d_in, const int* in_sizes, int n_in, void* d_out, int out_size, void* d_ws,
                              size_t ws_size, hipStream_t stream) {
  Params p;
  p.x = (const u16*)d_in[0];
  p.Lre = (const u16*)d_in[1];
  p.Lim = (const u16*)d_in[2];
  p.Bre = (const u16*)d_in[3];
  p.Bim = (const u16*)d_in[4];
  p.Cre = (const u16*)d_in[5];
  p.Cim = (const u16*)d_in[6];
  p.ldtA = (const u16*)d_in[7];
  p.DA = (const u16*)d_in[8];
  p.Are = (const u16*)d_in[10];
  p.Aim = (const u16*)d_in[11];
  p.sBre = (const u16*)d_in[12];
  p.sBim = (const u16*)d_in[13];
  p.sCre = (const u16*)d_in[14];
  p.sCim = (const u16*)d_in[15];
  p.ldtB = (const u16*)d_in[16];
  p.DB = (const u16*)d_in[17];
  p.gw = (const u16*)d_in[18];
  p.out = (float*)d_out;
  p.ws = (char*)d_ws;
  fused_kernel<<<256, 512, 0, stream>>>(p);
}
```

```cpp
#include <hip/hip_runtime.h>
#include <stdint.h>

typedef unsigned short u16;
typedef unsigned int u32;
typedef unsigned long long u64;
typedef __bf16 bf16x8 __attribute__((ext_vector_type(8)));
typedef short s16x4 __attribute__((ext_vector_type(4)));
typedef float f32x4 __attribute__((ext_vector_type(4)));
typedef float f32x2 __attribute__((ext_vector_type(2)));
typedef u32 u32x4 __attribute__((ext_vector_type(4)));
typedef u32 u32x2 __attribute__((ext_vector_type(2)));
typedef u64 u64x8 __attribute__((ext_vector_type(8)));
typedef u64 u64x4 __attribute__((ext_vector_type(4)));
typedef __bf16 bf16x2_t __attribute__((ext_vector_type(2)));
typedef __amdgpu_buffer_rsrc_t rsrc_t;
#define LAS __attribute__((address_space(3)))
#define GAS __attribute__((address_space(1)))
#define DEV __device__ __forceinline__
#define INL __attribute__((always_inline))
#if defined(__HIP_DEVICE_COMPILE__)
#define NOPK __attribute__((target("no-packed-fp32-ops")))
#else
#define NOPK
#endif

constexpr u32 WS_E = 0;
constexpr u32 WS_FH = 4096;
constexpr u32 WS_FX = WS_FH + 1024 * 128;
constexpr u32 WS_SG = WS_FX + 1024 * 128;
constexpr u32 WS_GG = WS_SG + 16 * 640 * 16;
constexpr u32 WS_XT = WS_GG + 10 * 2048 * 8;
constexpr u32 WS_BT = WS_XT + 640 * 2048 * 2;
constexpr u32 WS_H = WS_BT + 640 * 1024 * 8;
constexpr u32 WS_END = WS_H + 128 * 40 * 1024;
constexpr u32 WS_DIAG = (WS_END + 4095) & ~4095u;

constexpr u32 STG = 26624;
constexpr u32 L_A0 = 81920;
constexpr u32 L_B0 = 0;
__attribute__((always_inline)) constexpr u32 ring(int s) { return s % 6 < 3 ? L_A0 + (s % 6) * STG : (s % 6 - 3) * STG; }
constexpr u32 BSLOT = 27008;
constexpr u32 L_CNT = 81024;
constexpr u32 C_FREE = L_CNT, C_PRO = L_CNT + 16, C_BAR = L_CNT + 32, C_BDONE = L_CNT + 48, C_C0 = L_CNT + 64, C_BPREP = L_CNT + 80,
              C_STRIP = L_CNT + 96, C_FULL = L_CNT + 112, C_CFULL = L_CNT + 116,
              C_G1 = L_CNT + 128, L_TAG = L_CNT + 160;
constexpr u32 L_GATE = L_CNT + 256;
constexpr u32 L_DSK = L_A0 + 3 * STG;
constexpr u32 L_GW = L_DSK + 320;
constexpr u32 L_RAW = ring(5);
constexpr u32 L_TAB = L_RAW + 10240;
constexpr u32 L_ERAW = L_A0;
constexpr u32 L_WS = L_A0 + 5120;
constexpr u32 L_HP = L_A0 + 6400;
constexpr u32 L_PT = L_A0 + 7936;
constexpr u32 L_HT = L_A0 + 13056;
constexpr u32 L_BST = L_A0;
constexpr u32 L_IMG0 = L_A0 + 10240, L_IMG1 = L_A0 + 30720;
constexpr u32 UCOPY = 4640, UPAD = 384, KOFF = 4 * UCOPY, KCOPY = 4224, KPAD = 112;
constexpr u32 KC_OFF = 16640;
constexpr u32 TROW = 288;

constexpr int SPIN_CAP = 1 << 16;

static __device__ const __attribute__((aligned(64))) u32 seq_tab[16] = {1, 2, 3, 4, 5, 6, 7, 8, 9, 10, 11, 12, 13, 14, 15, 16};

template <bool B> struct BoolC { static constexpr bool value = B; };

struct Params {
  const u16 *x, *Lre, *Lim, *Bre, *Bim, *Cre, *Cim, *ldtA, *DA;
  const u16 *Are, *Aim, *sBre, *sBim, *sCre, *sCim, *ldtB, *DB, *gw;
  float* out;
  char* ws;
};

DEV bool wave_all(bool p) { return __builtin_amdgcn_ballot_w64(!p) == 0; }
DEV float bf2f(u32 v) { return __builtin_bit_cast(float, v << 16); }
DEV float bflo(u32 v) { return __builtin_bit_cast(float, v << 16); }
DEV float bfhi(u32 v) { return __builtin_bit_cast(float, v & 0xffff0000u); }
DEV u32 pk(float a, float b) { bf16x2_t v = {(__bf16)a, (__bf16)b}; return __builtin_bit_cast(u32, v); }
DEV float rbf(float a) { return bflo(pk(a, a)); }
DEV u32 f2u(float a) { return __builtin_bit_cast(u32, a); }
DEV float u2f(u32 a) { return __builtin_bit_cast(float, a); }
DEV float swp(float x) { return u2f(__builtin_amdgcn_update_dpp(0, f2u(x), 0xB1, 0xf, 0xf, true)); }
DEV f32x2 rot(f32x2 z) { return f32x2{-z.y, z.x}; }
DEV f32x2 cmul(f32x2 c, f32x2 z, f32x2 zr) { return __builtin_elementwise_fma(f32x2{c.y, c.y}, zr, f32x2{c.x, c.x} * z); }
DEV f32x2 cmul(f32x2 c, f32x2 z) { return cmul(c, z, rot(z)); }
DEV f32x2 cpowk(float a, float th, float k) {
  const float L2E = 1.44269504088896f, IH = 0.15915494f, IL = 6.4206382e-9f;
  float m = __builtin_amdgcn_exp2f(k * a * L2E);
  float x = k * th;
  float hi = x * IH;
  float lo = __builtin_fmaf(x, IL, __builtin_fmaf(x, IH, -hi));
  float r = __builtin_amdgcn_fractf(hi) + lo;
  return f32x2{m * __builtin_amdgcn_cosf(r), m * __builtin_amdgcn_sinf(r)};
}
template <typename T> DEV T lds_ld(u32 a) { typedef T __attribute__((may_alias)) TA; return *(const LAS TA*)(size_t)a; }
template <typename T> DEV void lds_st(u32 a, T v) { typedef T __attribute__((may_alias)) TA; *(LAS TA*)(size_t)a = v; }
DEV s16x4 lds_tr(u32 a) { return __builtin_amdgcn_ds_read_tr16_b64_v4i16((LAS s16x4*)(size_t)a); }
DEV f32x4 mfma(u32x4 a, u32x4 b, f32x4 c) {
  return __builtin_amdgcn_mfma_f32_16x16x32_bf16(__builtin_bit_cast(bf16x8, a), __builtin_bit_cast(bf16x8, b), c, 0, 0, 0);
}
DEV rsrc_t mksrd(const void* p, u32 n) { return __builtin_amdgcn_make_buffer_rsrc((void*)p, (short)0, (int)n, 0x00020000); }
DEV void dma16(rsrc_t rs, u32 voff, u32 soff, u32 lds) {
  asm volatile("s_mov_b32 m0, %0\n\ts_nop 0\n\tbuffer_load_dwordx4 %1, %2, %3 offen lds"
               :: "s"(lds), "v"(voff), "s"(rs), "s"(soff) : "m0", "memory");
}
DEV void dma64v(rsrc_t rs, u32 v0, u32 v1, u32 v2, u32 v3, u32 so, u32 lds) {
  asm volatile("s_mov_b32 m0, %0\n\ts_nop 0\n\tbuffer_load_dwordx4 %1, %5, %6 offen lds\n\t"
               "buffer_load_dwordx4 %2, %5, %6 offen offset:1024 lds\n\tbuffer_load_dwordx4 %3, %5, %6 offen offset:2048 lds\n\t"
               "buffer_load_dwordx4 %4, %5, %6 offen offset:3072 lds"
               :: "s"(lds), "v"(v0), "v"(v1), "v"(v2), "v"(v3), "s"(rs), "s"(so) : "m0", "memory");
}
DEV void dma32v(rsrc_t rs, u32 v0, u32 v1, u32 so, u32 lds) {
  asm volatile("s_mov_b32 m0, %0\n\ts_nop 0\n\tbuffer_load_dwordx4 %1, %3, %4 offen lds\n\t"
               "buffer_load_dwordx4 %2, %3, %4 offen offset:1024 lds"
               :: "s"(lds), "v"(v0), "v"(v1), "s"(rs), "s"(so) : "m0", "memory");
}
template <int OFF> DEV void dmaq(rsrc_t r0, rsrc_t r1, u32 v0, u32 v1, u32 v2, u32 v3, u32 so, u32 slot) {
  asm volatile("s_add_u32 m0, %0, %8\n\ts_nop 0\n\tbuffer_load_dwordx4 %1, %5, %7 offen lds\n\t"
               "buffer_load_dwordx4 %2, %6, %7 offen offset:1024 lds\n\tbuffer_load_dwordx4 %3, %6, %7 offen offset:2048 lds\n\t"
               "buffer_load_dwordx4 %4, %6, %7 offen offset:3072 lds"
               :: "s"(slot), "v"(v0), "v"(v1), "v"(v2), "v"(v3), "s"(r0), "s"(r1), "s"(so), "n"(OFF) : "m0", "scc", "memory");
}
template <int OFF> DEV void dmad(rsrc_t r0, u32 v0, u32 v1, u32 so, u32 slot) {
  asm volatile("s_add_u32 m0, %0, %5\n\ts_nop 0\n\tbuffer_load_dwordx4 %1, %3, %4 offen lds\n\t"
               "buffer_load_dwordx4 %2, %3, %4 offen offset:1024 lds"
               :: "s"(slot), "v"(v0), "v"(v1), "s"(r0), "s"(so), "n"(OFF) : "m0", "scc", "memory");
}
DEV void dma_mark(rsrc_t rs, u32 so, u32 lds) {
  u64 keep;
  asm volatile("s_mov_b32 m0, %1\n\ts_mov_b64 %0, exec\n\ts_mov_b64 exec, 1\n\tbuffer_load_dword off, %2, %3 lds\n\ts_mov_b64 exec, %0"
               : "=&s"(keep) : "s"(lds), "s"(rs), "s"(so) : "m0", "memory");
}
DEV u32 opaque(u32 v) { asm volatile("" : "+v"(v)); return v; }
#define SB() __builtin_amdgcn_sched_barrier(0)
#define RARE(x) __builtin_expect(!!(x), 0)
template <int N> DEV void waitv() { asm volatile("s_waitcnt vmcnt(%0)" :: "n"(N) : "memory"); }
DEV void waitl() { asm volatile("s_waitcnt lgkmcnt(0)" ::: "memory"); }
DEV void hwbar() { waitl(); __builtin_amdgcn_s_barrier(); asm volatile("" ::: "memory"); }
DEV void post(u32 a, u32 v) {
  asm volatile("" ::: "memory");
  lds_st<u32>(a, v);
  asm volatile("" ::: "memory");
}
DEV u32 min4(u32x4 v) { return (u32)__builtin_amdgcn_readfirstlane(min(min(v.x, v.y), min(v.z, v.w))); }
DEV u32x4 peek4(u32 a) { asm volatile("" ::: "memory"); return lds_ld<u32x4>(a); }
DEV void sw_wait4(u32 a, u32 target) {
#pragma unroll 1
  for (u32 it = 0; it < (1u << 22) && min4(peek4(a)) < target; ++it) {}
  asm volatile("" ::: "memory");
}
DEV void sw_wait(u32 a, u32 target) {
  u32 v, sv, cnt = 1u << 22;
  asm volatile("1:\n\tds_read_b32 %0, %4\n\ts_sub_u32 %2, %2, 1\n\ts_cselect_b32 %3, 0, %3\n\ts_waitcnt lgkmcnt(0)\n\t"
               "v_readfirstlane_b32 %1, %0\n\ts_cmp_lt_u32 %1, %3\n\ts_cbranch_scc1 1b"
               : "=&v"(v), "=&s"(sv), "+s"(cnt), "+s"(target) : "v"(a) : "scc", "memory");
}
DEV u32 peek(u32 a) { return __hip_atomic_load((LAS u32*)(size_t)a, __ATOMIC_RELAXED, __HIP_MEMORY_SCOPE_WORKGROUP); }
DEV u32x4 ld16(rsrc_t rs, u32 off) { return __builtin_amdgcn_raw_buffer_load_b128(rs, off, 0, 16); }
DEV u32x2 ld8(rsrc_t rs, u32 off) { return __builtin_amdgcn_raw_buffer_load_b64(rs, off, 0, 16); }
DEV void st16(rsrc_t rs, u32 off, u32x4 v) { __builtin_amdgcn_raw_buffer_store_b128(v, rs, off, 0, 16); }
DEV void st8(rsrc_t rs, u32 off, u32x2 v) { __builtin_amdgcn_raw_buffer_store_b64(v, rs, off, 0, 16); }
constexpr u32 OOB = 0x7ff00000u;
constexpr u32 FMIX = 0x9E3779B9u;
DEV bool flag_ok(u32x2 v, u32 off, u32 tag) { return wave_all((off == OOB) | ((v.x == tag) & (v.y == (tag ^ FMIX)))); }
DEV void poll_flags(rsrc_t rs, u32 off, u32 tag) {
#pragma unroll 1
  for (int it = 0; it < SPIN_CAP; ++it) {
    asm volatile("" ::: "memory");
    if (flag_ok(ld8(rs, off), off, tag)) break;
    __builtin_amdgcn_s_sleep(1);
  }
  asm volatile("" ::: "memory");
}

#define TR(k) do {} while (0)
#define DUMP() do {} while (0)
#define STAMP(k) do {} while (0)
#define CSTAMP(k) do {} while (0)

#define SCAN_STEP(o, v, h)                                                                                             \
  asm("v_fma_f32 %0, %15, %10, %5\n\tv_fma_f32 %1, %16, %11, %6\n\tv_fma_f32 %2, %17, %12, %7\n\t"                   \
      "v_fma_f32 %3, %18, %13, %8\n\tv_fma_f32 %4, %19, %14, %9\n\t"                                                   \
      "v_fmac_f32_dpp %0, %10, %20 quad_perm:[1,0,3,2] row_mask:0xf bank_mask:0xf\n\t"                                 \
      "v_fmac_f32_dpp %1, %11, %21 quad_perm:[1,0,3,2] row_mask:0xf bank_mask:0xf\n\t"                                 \
      "v_fmac_f32_dpp %2, %12, %22 quad_perm:[1,0,3,2] row_mask:0xf bank_mask:0xf\n\t"                                 \
      "v_fmac_f32_dpp %3, %13, %23 quad_perm:[1,0,3,2] row_mask:0xf bank_mask:0xf\n\t"                                 \
      "v_fmac_f32_dpp %4, %14, %24 quad_perm:[1,0,3,2] row_mask:0xf bank_mask:0xf"                                     \
      : "=&v"(o[0]), "=&v"(o[1]), "=&v"(o[2]), "=&v"(o[3]), "=&v"(o[4])                                                \
      : "v"(v[0]), "v"(v[1]), "v"(v[2]), "v"(v[3]), "v"(v[4]), "v"(h[0]), "v"(h[1]), "v"(h[2]), "v"(h[3]), "v"(h[4]), \
        "v"(ma[0]), "v"(ma[1]), "v"(ma[2]), "v"(ma[3]), "v"(ma[4]), "v"(mb[0]), "v"(mb[1]), "v"(mb[2]), "v"(mb[3]),   \
        "v"(mb[4]))
DEV void scan_step(float (&o)[5], const float (&v)[5], const float (&h)[5], const float (&ma)[5], const float (&mb)[5]) {
  float t[5];
  SCAN_STEP(t, v, h);
#pragma unroll
  for (int i = 0; i < 5; ++i) o[i] = t[i];
}

struct Frag { u32x4 a0, a1, b0, b1, b2, b3, b4; };
#define MF(d, a, b) "v_mfma_f32_16x16x32_bf16 %" #d ", %" #a ", %" #b ", %" #d "\n\t"
#define RA(d, o) "ds_read_b128 %" #d ", %24 offset:" #o "\n\t"
#define RB(d, o) "ds_read_b128 %" #d ", %25 offset:" #o "\n\t"
#define WL(n) "s_waitcnt lgkmcnt(" #n ")\n\t"
#define G1_BODY(w1, w2, w3, w5, w7, w9)                                                                    \
  w1 MF(0, 17, 19) RA(10, 0) w2 MF(5, 18, 19) RB(12, 0) w3 MF(1, 17, 20) RA(11, 2048) MF(6, 18, 20) RB(13, 1024) \
  w5 MF(2, 17, 21) RB(14, 2048) MF(7, 18, 21) RB(15, 3072) w7 MF(3, 17, 22) RB(16, 4096) MF(8, 18, 22)    \
  w9 MF(4, 17, 23) MF(9, 18, 23)
#define G1_ACC(A) "+v"(A[0][0]), "+v"(A[0][1]), "+v"(A[0][2]), "+v"(A[0][3]), "+v"(A[0][4]), "+v"(A[1][0]), "+v"(A[1][1]), "+v"(A[1][2]), "+v"(A[1][3]), "+v"(A[1][4])
#define G1_OUT(N) "=&v"(N.a0), "=&v"(N.a1), "=&v"(N.b0), "=&v"(N.b1), "=&v"(N.b2), "=&v"(N.b3), "=&v"(N.b4)
#define G1_IN(C) "v"(C.a0), "v"(C.a1), "v"(C.b0), "v"(C.b1), "v"(C.b2), "v"(C.b3), "v"(C.b4)
template <bool WAITS> DEV void g1_half(f32x4 (&A)[2][5], Frag& N, const Frag& C, u32 pa, u32 pb) {
  if constexpr (WAITS)
    asm volatile(G1_BODY(WL(5), WL(5), WL(5), WL(6), WL(7), WL(7)) : G1_ACC(A), G1_OUT(N) : G1_IN(C), "v"(pa), "v"(pb) : "memory");
  else
    asm volatile(G1_BODY("", "", "", "", "", "") : G1_ACC(A), G1_OUT(N) : G1_IN(C), "v"(pa), "v"(pb) : "memory");
}
DEV u32 g1_half_peek(f32x4 (&A)[2][5], Frag& N, const Frag& C, u32 pa, u32 pb, u32 pf) {
  u32 t = pf;
  asm volatile("ds_read_b32 %0, %0" : "+v"(t) :: "memory");
  asm volatile(G1_BODY(WL(6), WL(6), WL(6), WL(7), WL(8), WL(8)) WL(7) : G1_ACC(A), G1_OUT(N) : G1_IN(C), "v"(pa), "v"(pb) : "memory");
  u32 r;
  asm volatile("v_readfirstlane_b32 %0, %1" : "=s"(r) : "v"(t) : "memory");
  return r;
}
DEV void g1_last(f32x4 (&A)[2][5], const Frag& C) {
  asm volatile(WL(5) MF(0, 10, 12) WL(4) MF(5, 11, 12) WL(3) MF(1, 10, 13) MF(6, 11, 13) WL(2) MF(2, 10, 14) MF(7, 11, 14)
               WL(1) MF(3, 10, 15) MF(8, 11, 15) WL(0) MF(4, 10, 16) MF(9, 11, 16) "s_nop 7"
               : G1_ACC(A) : G1_IN(C) : "memory");
}

struct CF { u32x4 f[5]; };
DEV u32 peek_issue(u32 a) { asm volatile("ds_read_b32 %0, %0" : "+v"(a) :: "memory"); return a; }
DEV u32 peek_get(u32 t) { u32 r; asm volatile("s_waitcnt lgkmcnt(5)\n\tv_readfirstlane_b32 %0, %1" : "=s"(r) : "v"(t) : "memory"); return r; }
#define M2(d, a) "v_mfma_f32_16x16x32_bf16 %" #d ", %" #a ", %15, %" #d "\n\t"
#define R2(d, o) "ds_read_b128 %" #d ", %16 offset:%17+" #o "\n\t"
template <int OFF, int WT> DEV void g2_half(f32x4 (&y)[5], CF& N, const CF& Cc, u32x4 h, u32 pa) {
  asm volatile("s_waitcnt lgkmcnt(%18)\n\t" M2(0, 10) R2(5, 0) "s_waitcnt lgkmcnt(%18)\n\t" M2(1, 11) R2(6, 2048) "s_waitcnt lgkmcnt(%18)\n\t" M2(2, 12) R2(7, 4096)
               "s_waitcnt lgkmcnt(%18)\n\t" M2(3, 13) R2(8, 6144) "s_waitcnt lgkmcnt(%18)\n\t" M2(4, 14) R2(9, 8192)
               : "+v"(y[0]), "+v"(y[1]), "+v"(y[2]), "+v"(y[3]), "+v"(y[4]), "=&v"(N.f[0]), "=&v"(N.f[1]), "=&v"(N.f[2]), "=&v"(N.f[3]), "=&v"(N.f[4])
               : "v"(Cc.f[0]), "v"(Cc.f[1]), "v"(Cc.f[2]), "v"(Cc.f[3]), "v"(Cc.f[4]), "v"(h), "v"(pa), "n"(OFF), "n"(WT) : "memory");
}
#define M3(d, a) "v_mfma_f32_16x16x32_bf16 %" #d ", %" #a ", %10, %" #d "\n\t"
template <bool LAST> DEV void g2_half0(f32x4 (&y)[5], const CF& Cc, u32x4 h) {
  if constexpr (LAST)
    asm volatile(WL(4) M3(0, 5) WL(3) M3(1, 6) WL(2) M3(2, 7) WL(1) M3(3, 8) WL(0) M3(4, 9) "s_nop 7"
                 : "+v"(y[0]), "+v"(y[1]), "+v"(y[2]), "+v"(y[3]), "+v"(y[4])
                 : "v"(Cc.f[0]), "v"(Cc.f[1]), "v"(Cc.f[2]), "v"(Cc.f[3]), "v"(Cc.f[4]), "v"(h) : "memory");
  else
    asm volatile(M3(0, 5) M3(1, 6) M3(2, 7) M3(3, 8) M3(4, 9)
                 : "+v"(y[0]), "+v"(y[1]), "+v"(y[2]), "+v"(y[3]), "+v"(y[4])
                 : "v"(Cc.f[0]), "v"(Cc.f[1]), "v"(Cc.f[2]), "v"(Cc.f[3]), "v"(Cc.f[4]), "v"(h) : "memory");
}

NOPK __global__ void __launch_bounds__(512, 2) fused_kernel(Params) {
  __shared__ __attribute__((aligned(16))) char smem[163840];
  asm volatile("" :: "v"((u32)(uintptr_t)(LAS char*)smem));
  constexpr u32 L0 = 0;
  const u64 kap = (u64)__builtin_amdgcn_kernarg_segment_ptr();
  u64x8 k0, k1; u64x4 k2;
  asm volatile("s_load_dwordx16 %0, %3, 0x0\n\ts_load_dwordx16 %1, %3, 0x40\n\ts_load_dwordx8 %2, %3, 0x80"
               : "=&s"(k0), "=&s"(k1), "=&s"(k2) : "s"(kap));
  lds_st<u32>(L0 + L_CNT + min(threadIdx.x & 63, 39u) * 4, 0u);
  asm volatile("s_waitcnt lgkmcnt(0)\n\ts_barrier" : "+s"(k0), "+s"(k1), "+s"(k2) :: "memory");
  struct { const u16 *x, *Lre, *Lim, *Bre, *Bim, *Cre, *Cim, *ldtA, *DA, *Are, *Aim, *sBre, *sBim, *sCre, *sCim, *ldtB, *DB, *gw; float* out; char* ws; }
  const P = {(const u16*)k0[0], (const u16*)k0[1], (const u16*)k0[2], (const u16*)k0[3], (const u16*)k0[4], (const u16*)k0[5], (const u16*)k0[6],
             (const u16*)k0[7], (const u16*)k1[0], (const u16*)k1[1], (const u16*)k1[2], (const u16*)k1[3], (const u16*)k1[4], (const u16*)k1[5],
             (const u16*)k1[6], (const u16*)k1[7], (const u16*)k2[0], (const u16*)k2[1], (float*)k2[2], (char*)k2[3]};
  const int tid = threadIdx.x, lane = tid & 63;
  const int w = __builtin_amdgcn_readfirstlane(tid >> 6);
  const int b = blockIdx.x, xcd = b & 7, r = b >> 3;
  const int c = 4 * (xcd >> 1) + (r >> 3), j = 8 * (xcd & 1) + (r & 7);
  const int ti = 4 * xcd + (r >> 3), dj = r & 7;
  const int i16 = lane & 15, g = lane >> 4;
  const rsrc_t rws = mksrd(P.ws, WS_END);
  const rsrc_t rx = mksrd(P.x, 2048 * 1280);
  const rsrc_t rCre = mksrd(P.Cre, 640 * 1280), rCim = mksrd(P.Cim, 640 * 1280);
  const rsrc_t rka = mksrd(seq_tab, 64);
  constexpr u32 SEQ = 0;
  const int kq = w & 3, kh = w >> 2;
  const int rot10 = ((r & 7) + 2 * (r >> 3) + 3 * xcd) % 10;
  u32 tag;
  STAMP(0);
  TR(0);
  const u32 cf0 = i16 * 128 + ((g ^ ((i16 >> 1) & 7)) << 4);
  const u32 ccf = L0 + C_CFULL;
  const u32 fho = lane < 8 ? WS_FH + (u32)((2 * ti + (kq >> 1)) * 16 + 8 * kh + lane) * 128 : OOB;
  const u32 ho = WS_H + (u32)((4 * ti + kq) * 40 + 20 * kh) * 1024 + lane * 16;
  const u32 ima = L0 + (kh ? L_IMG1 : L_IMG0) + (16 * kq + i16) * 320 + (g ^ ((i16 >> 1) & 3)) * 16;
#define G2_REGS(name, n, at)                                                                                \
  CF name[2 * n];                                                                                           \
  { const u32 a0 = opaque(L0 + (at) + cf0), a1 = opaque(L0 + (at) + (cf0 ^ 64));                            \
    _Pragma("unroll") for (int k = 0; k < 2 * n; ++k)                                                       \
    _Pragma("unroll") for (int nt = 0; nt < 5; ++nt) name[k].f[nt] = lds_ld<u32x4>(((k & 1) ? a1 : a0) + (k >> 1) * 10240 + nt * 2048); \
    _Pragma("unroll") for (int k = 0; k < 2 * n; ++k)                                                       \
      asm volatile("s_waitcnt lgkmcnt(0)" : "+v"(name[k].f[0]), "+v"(name[k].f[1]), "+v"(name[k].f[2]), "+v"(name[k].f[3]), "+v"(name[k].f[4]) :: "memory"); }
#define G2_LOADH()                                                                                          \
  u32x4 hf_[20];                                                                                            \
  _Pragma("unroll") for (int ks = 0; ks < 20; ++ks) hf_[ks] = ld16(rws, ho + ks * 1024);                    \
  SB();
#define G2_INIT(at)                                                                                         \
  f32x4 y[5];                                                                                               \
  _Pragma("unroll") for (int nt = 0; nt < 5; ++nt) y[nt] = f32x4{0.f, 0.f, 0.f, 0.f};                       \
  CF P0, P1;                                                                                                \
  const u32 cab[2][2] = {{opaque(L0 + (at) + cf0), opaque(L0 + (at) + (cf0 ^ 64))},                         \
                         {opaque(L0 + (at) + 40960 + cf0), opaque(L0 + (at) + 40960 + (cf0 ^ 64))}};        \
  asm volatile("s_nop 1" : "+v"(y[0]), "+v"(y[1]), "+v"(y[2]), "+v"(y[3]), "+v"(y[4]));
#define G2_U(uu, slot, need)                                                                                \
  { const u32 t_ = peek_issue(ccf);                                                                         \
    g2_half<((slot) & 3) * 10240, 5>(y, P1, P0, hf_[2 * (uu)], cab[(slot) >> 2][1]);                        \
    if (RARE(peek_get(t_) < (need))) sw_wait(ccf, need);                                                    \
    g2_half<(((slot) + 1) & 3) * 10240, 4>(y, P0, P1, hf_[2 * (uu) + 1], cab[((slot) + 1) >> 2][0]); }
#define G2_UN(uu, slot)                                                                                     \
  g2_half<((slot) & 3) * 10240, 4>(y, P1, P0, hf_[2 * (uu)], cab[(slot) >> 2][1]);                          \
  g2_half<(((slot) + 1) & 3) * 10240, 4>(y, P0, P1, hf_[2 * (uu) + 1], cab[((slot) + 1) >> 2][0]);
#define G2_ULAST(uu, slot)                                                                                  \
  g2_half<((slot) & 3) * 10240, 4>(y, P1, P0, hf_[2 * (uu)], cab[(slot) >> 2][1]);                          \
  g2_half0<true>(y, P1, hf_[2 * (uu) + 1]);
#define G2_B_IMAGE()                                                                                        \
  { STAMP(11);                                                                                              \
    sw_wait4(L0 + C_BAR, 4);                                                                                \
    const u32 ia = opaque(ima);                                                                             \
    _Pragma("unroll") for (int nt = 0; nt < 5; ++nt) lds_st<f32x4>(ia + nt * 64, y[nt]); }
  auto get_tag = [&]() INL { sw_wait(L0 + C_FULL, 1); tag = __builtin_amdgcn_readfirstlane(lds_ld<u32>(opaque(L0 + L_TAG))) + 1u; };

  const int pl = lane < 40 ? lane : 39;
  u32 pLre, pLim, pLdt;
  if (w < 4) {
    pLre = ((const GAS u16*)P.Lre)[40 * j + pl]; pLim = ((const GAS u16*)P.Lim)[40 * j + pl]; pLdt = ((const GAS u16*)P.ldtA)[40 * j + pl];
    asm volatile("" ::: "memory");
#pragma unroll
    for (int k = 0; k < 3; ++k) {
      const int qq = 64 * k + lane, t = (qq * 6554) >> 16, cc = qq - 10 * t;
      if (k < 2 || lane < 32) dma16(rx, (u32)(64 * ti + 16 * w + t) * 1280 + dj * 160 + cc * 16, 0, L0 + L_RAW + w * 2560 + k * 1024);
    }
    dma_mark(rka, SEQ, L0 + C_STRIP + 4 * w);
  }
  if (__builtin_expect(w == 7, 1)) {
      __builtin_amdgcn_s_setprio(3);
      const rsrc_t rBre = mksrd(P.Bre, 640 * 1280), rBim = mksrd((const char*)P.Bim - 1024, 640 * 1280 + 1024);
      const int q8 = lane >> 3;
      u32 vx[4], vb[10];
      {
        const u32 x0 = (u32)(128 * c + 8 * (q8 >> 2) + (q8 & 3)) * 1280 + (((lane & 7) ^ (q8 >> 1)) << 4);
        vx[0] = x0; vx[1] = (x0 ^ 64) + 16 * 1280 - 1024; vx[2] = x0 + 4 * 1280 - 2048; vx[3] = (x0 ^ 64) + 20 * 1280 - 3072;
        const u32 b0 = (u32)(40 * j + q8) * 1280 + (((lane & 7) ^ (q8 & 6)) << 4), b1 = (b0 ^ 16) + 1024;
#pragma unroll
        for (int k = 0; k < 10; ++k) vb[k] = (k < 5 ? b0 : b1) + 10240 * (k % 5) - 1024 * (k & 3);
      }
      u32 so = rot10 * 128;
      {
        u64 keep;
        asm volatile("s_mov_b32 m0, %1\n\ts_mov_b64 %0, exec\n\ts_mov_b64 exec, 1\n\tbuffer_load_dword off, %2, 0 sc1 lds\n\ts_mov_b64 exec, %0"
                     : "=&s"(keep) : "s"(L0 + L_TAG), "s"(rws) : "m0", "memory");
      }
      auto head = [&](u32 sl) INL {
        dmaq<0>(rx, rx, vx[0], vx[1], vx[2], vx[3], so, sl);
        dmaq<4096>(rx, rx, vx[0], vx[1], vx[2], vx[3], so + 40960, sl);
        dmaq<8192>(rx, rx, vx[0], vx[1], vx[2], vx[3], so + 81920, sl);
        dmaq<12288>(rx, rx, vx[0], vx[1], vx[2], vx[3], so + 122880, sl);
        dmaq<16384>(rBre, rBre, vb[0], vb[1], vb[2], vb[3], so, sl);
      };
      auto tail = [&](u32 sl) INL {
        dmaq<20480>(rBre, rBim, vb[4], vb[5], vb[6], vb[7], so, sl);
        dmad<24576>(rBim, vb[8], vb[9], so, sl);
        so = so == 1152 ? 0 : so + 128;
      };
#pragma unroll
      for (int s = 0; s < 10; ++s) {
        const u32 sb = L0 + ring(s);
        if (s == 5) sw_wait4(L0 + C_PRO, 1);
        if (s == 6 || s == 8) sw_wait4(L0 + C_FREE, s - 4);
        TR(4 + 4 * s);
        if (s >= 2) waitv<34>();
        TR(5 + 4 * s);
        head(sb);
        tail(sb);
        TR(6 + 4 * s);
        if (s == 0 && lane < 10) {
          dma16(mksrd(P.DA, 1280), lane * 16, dj * 160, L0 + L_DSK);
          dma16(mksrd(P.DB, 1280), lane * 16, dj * 160, L0 + L_DSK + 160);
          dma16(mksrd(P.gw, 1280), lane * 16, rot10 * 128, L0 + L_GW);
        }
        dma_mark(rka, SEQ + 4 * s, L0 + C_FULL);
        if (s == 0) __builtin_amdgcn_s_setprio(1);
      }
      STAMP(2);
      u32 vc[4];
#pragma unroll
      for (int k = 0; k < 4; ++k) vc[k] = (u32)(80 * dj + q8) * 1280 + (((lane & 7) ^ (lane >> 4) ^ (4 * (k & 1))) << 4) + 9216 * k;
      auto stageC = [&](int u, u32 dst) INL {
        const rsrc_t rs = (u & 1) ? rCim : rCre;
        const u32 so = (u32)(u >> 1) * 128;
        dma64v(rs, vc[0], vc[1], vc[2], vc[3], so, dst);
        dma64v(rs, vc[0], vc[1], vc[2], vc[3], so + 40960, dst + 4096);
        dma32v(rs, vc[0], vc[1], so + 81920, dst + 8192);
      };
      {
        u32 t0 = 0;
#pragma unroll
        for (int k = 0; k < 7; ++k) {
          const u32 ln = min(r * 400 + k * 64 + lane, r * 400 + 399);
          asm volatile("buffer_load_dword %0, %1, %2, 0 offen" : "+v"(t0) : "v"((ln >= 6400 ? ln - 6400 : ln) * 128), "s"(r >= 16 ? rCim : rCre) : "memory");
        }
        get_tag();
        asm volatile("s_waitcnt vmcnt(0)" : "+v"(t0) :: "memory");
        post(L0 + C_BPREP + 12, 1u);
      }
      sw_wait4(L0 + C_BPREP, 1);
      sw_wait4(L0 + C_BAR, 2);
      stageC(10, L0 + L_A0 + 5 * 10240); stageC(11, L0 + L_A0 + 6 * 10240); stageC(12, L0 + L_A0 + 7 * 10240);
      stageC(0, L0 + L_A0 + 3 * 10240); stageC(1, L0 + L_A0 + 4 * 10240);
      dma_mark(rka, SEQ, ccf);
      stageC(4, L0 + L_A0 + 2 * 10240);
      sw_wait(ccf, 1);
      G2_REGS(cr, 3, L_A0 + 5 * 10240);
      post(L0 + C_BDONE + 12, 1u);
      sw_wait4(L0 + C_BAR, 3);
      stageC(2, L0 + L_A0); stageC(3, L0 + L_A0 + 10240);
      dma_mark(rka, SEQ + 4, ccf);
      STAMP(3);
      sw_wait4(L0 + C_C0, 1);
      waitv<30>();
      stageC(5, L0 + L_A0 + 3 * 10240); stageC(6, L0 + L_A0 + 4 * 10240);
      dma_mark(rka, SEQ + 8, ccf);
      poll_flags(rws, fho, tag);
      STAMP(8);
      u32x4 hf_[20];
#pragma unroll
      for (int ks = 0; ks < 20; ++ks)
        asm volatile("buffer_load_dwordx4 %0, %1, %2, %3 offen offset:%4 sc1" : "=v"(hf_[ks]) : "v"(ho), "s"(rws), "s"((ks >> 2) * 4096), "n"((ks & 3) * 1024) : "memory");
      G2_INIT(L_B0);
      sw_wait4(L0 + C_BDONE, 1);
      STAMP(4);
#define FILL(q, unit, at) stageC(unit, L0 + (at)); dma_mark(rka, SEQ + 4 * (3 + (q)), ccf);
      FILL(0, 13, L_B0); FILL(1, 14, L_B0 + 10240); waitv<50>(); FILL(2, 7, L_A0 + 5 * 10240);
      asm volatile("s_waitcnt vmcnt(33)" : "+v"(hf_[0]), "+v"(hf_[1]), "+v"(hf_[2]), "+v"(hf_[3]), "+v"(hf_[4]), "+v"(hf_[5]), "+v"(hf_[6]), "+v"(hf_[7]), "+v"(hf_[8]), "+v"(hf_[9]) :: "memory");
      asm volatile("" : "+v"(hf_[10]), "+v"(hf_[11]), "+v"(hf_[12]), "+v"(hf_[13]), "+v"(hf_[14]), "+v"(hf_[15]), "+v"(hf_[16]), "+v"(hf_[17]), "+v"(hf_[18]), "+v"(hf_[19]));
      FILL(3, 15, L_B0 + 2 * 10240);

      FILL(4, 8, L_A0 + 6 * 10240);
      g2_half0<false>(y, cr[0], hf_[0]); g2_half0<false>(y, cr[1], hf_[1]);
      waitv<40>(); FILL(5, 16, L_B0 + 3 * 10240);
      g2_half0<false>(y, cr[2], hf_[2]); g2_half0<false>(y, cr[3], hf_[3]);
      waitv<40>(); FILL(6, 9, L_A0 + 7 * 10240);
      g2_half0<false>(y, cr[4], hf_[4]); g2_half<0, 4>(y, P0, cr[5], hf_[5], cab[0][0]);
      waitv<40>(); FILL(7, 17, L_B0 + 4 * 10240);
      G2_UN(3, 0);
      waitv<40>(); FILL(8, 18, L_B0 + 5 * 10240);
      G2_UN(4, 1);
      waitv<40>(); FILL(9, 19, L_B0 + 6 * 10240);
      STAMP(5);
      __builtin_amdgcn_s_setprio(0);
      G2_U(5, 2, 9); G2_U(6, 3, 11); G2_U(7, 4, 12); G2_U(8, 5, 13); G2_ULAST(9, 6);
      G2_B_IMAGE();
  } else if (__builtin_expect(w < 4, 1)) {
    __builtin_amdgcn_s_setprio(2);
    f32x4 acc[2][5], accg[2] = {f32x4{0.f, 0.f, 0.f, 0.f}, f32x4{0.f, 0.f, 0.f, 0.f}};
#pragma unroll
    for (int m = 0; m < 2; ++m)
#pragma unroll
      for (int nt = 0; nt < 5; ++nt) acc[m][nt] = f32x4{0.f, 0.f, 0.f, 0.f};
    asm volatile("" : G1_ACC(acc));
    const u32 ax0 = opaque(L0 + (32 * w + i16) * 128 + ((g ^ ((i16 >> 1) & 7)) << 4));
    const u32 ab0 = opaque(L0 + 16384 + (40 * (i16 & 1) + (i16 >> 1)) * 128 + ((g ^ (2 * (i16 >> 2) + (i16 & 1))) << 4));
    const u32 ax1 = opaque(ax0 ^ 64), ab1 = opaque(ab0 ^ 64);
    const u32 cfull = opaque(L0 + C_FULL), cfree = opaque(L0 + C_FREE), myfree = opaque(L0 + C_FREE + 4 * w);
    const u32 xra = opaque(L0 + L_RAW + (32 * (g >> 1) + 8 * (lane & 3) + ((lane >> 2) & 3)) * 160 + 40 * w + 8 * (g & 1));
    const u32 xrq = opaque(L0 + L_RAW + (16 * w + i16) * 160 + 8 * g);
    const u32 xo = opaque(WS_XT + (u32)(20 * dj + 5 * w + (g & 1)) * 16384 + (8 * ti + 4 * (g >> 1)) * 64 + i16 * 16);
    const u32 xo2 = opaque((g & 1) ? OOB : xo + 65536);
    const u32 fxo_ = opaque(lane == 0 ? WS_FX + (u32)((ti * 8 + dj) * 4 + w) * 128 : OOB);
    const u32 tb = opaque(L0 + L_TAB + w * 3200 + pl * 16), tp = opaque(L0 + L_TAB + w * 3200 + (i16 >> 1) * 16), tg = opaque(tp + g * 640);
    const u32 pa0 = opaque(ax0 + L_A0), pb0 = opaque(ab0 + L_A0), pa1 = opaque(ax1 + L_A0);
    asm volatile("" : "+v"(pLre), "+v"(pLim), "+v"(pLdt));
    STAMP(14);
    const float lre = bf2f(pLre), lim = bf2f(pLim);
    const float dtA = rbf(expf(bf2f(pLdt)));
    const float aA = dtA * lre, tA = dtA * lim;
    const f32x2 lam = cpowk(aA, tA, 1.f), l8 = cpowk(aA, tA, 8.f);
    f32x2 c2;
    {
      const float nr = lam.x - 1.f, ni = lam.y, den = 2.f * __builtin_amdgcn_rcpf(lre * lre + lim * lim);
      c2 = f32x2{(nr * lre + ni * lim) * den, (ni * lre - nr * lim) * den};
    }
    f32x2 zs[4], zh[4];
    zs[3] = cpowk(aA, tA, (float)(96 - 32 * w));
    zh[0] = cmul(cpowk(aA, tA, (float)(32 * w)), c2);
#pragma unroll
    for (int k = 1; k < 4; ++k) { zs[3 - k] = cmul(zs[4 - k], l8); zh[k] = cmul(zh[k - 1], l8); }
    const int sig = 4 * w + g;
    float lr[5], ls[5], sa[5], sb[5], ha[5], hb[5], ca[5], cb[5];
    {
#pragma unroll
      for (int k = 0; k < 4; ++k) lds_st<f32x4>(tb + k * 640, f32x4{zs[k].x, zs[k].y, zh[k].x, -zh[k].y});
      lds_st<f32x4>(tb + 4 * 640, f32x4{lam.x, lam.y, c2.x, -c2.y});
      const float sg = (lane & 1) ? 1.f : -1.f;
#pragma unroll
      for (int nt = 0; nt < 5; ++nt) {
        const f32x4 q1 = lds_ld<f32x4>(tp + 4 * 640 + nt * 128), q0 = lds_ld<f32x4>(tg + nt * 128);
        sa[nt] = q0.x; sb[nt] = sg * q0.y; ha[nt] = -sg * q0.z; hb[nt] = q0.w;
        lr[nt] = q1.x; ls[nt] = sg * q1.y; ca[nt] = -sg * q1.z; cb[nt] = q1.w;
        asm volatile("" : "+v"(sb[nt]), "+v"(ha[nt]), "+v"(ls[nt]), "+v"(ca[nt]));
      }
    }
    STAMP(15);
    u32x2 xq[5];
    {
        sw_wait4(L0 + C_STRIP, 1);
        u32x2 lo[3], hi[3];
#pragma unroll
        for (int k = 0; k < 3; ++k) { lo[k] = __builtin_bit_cast(u32x2, lds_tr(xra + k * 16)); hi[k] = __builtin_bit_cast(u32x2, lds_tr(xra + k * 16 + 640)); }
#pragma unroll
        for (int nt = 0; nt < 5; ++nt) xq[nt] = lds_ld<u32x2>(xrq + 32 * nt);
        post(L0 + C_PRO + 4 * w, 1u);
#pragma unroll
        for (int k = 0; k < 3; ++k) st16(rws, k < 2 ? xo + k * 32768 : xo2, u32x4{lo[k].x, lo[k].y, hi[k].x, hi[k].y});
        asm volatile("s_waitcnt lgkmcnt(0)" : "+v"(xq[0]), "+v"(xq[1]), "+v"(xq[2]), "+v"(xq[3]), "+v"(xq[4]) :: "memory");
      }
    STAMP(1);
    Frag X, Y;
    u32x2 eda[5], edb[5];
    sw_wait(cfull, 1);
    X.a0 = lds_ld<u32x4>(pa0); X.b0 = lds_ld<u32x4>(pb0); X.a1 = lds_ld<u32x4>(pa0 + 2048); X.b1 = lds_ld<u32x4>(pb0 + 1024);
    X.b2 = lds_ld<u32x4>(pb0 + 2048); X.b3 = lds_ld<u32x4>(pb0 + 3072); X.b4 = lds_ld<u32x4>(pb0 + 4096);
    const u32 seed = lds_ld<u32>(opaque(L0 + L_TAG));
    const u32 dk = opaque(L0 + L_DSK + 8 * g);
#pragma unroll
    for (int nt = 0; nt < 5; ++nt) { eda[nt] = lds_ld<u32x2>(dk + 32 * nt); edb[nt] = lds_ld<u32x2>(dk + 160 + 32 * nt); asm volatile("" : "+v"(eda[nt]), "+v"(edb[nt])); }
    const u32 gwa = opaque(L0 + L_GW + g * 16);
    const u32x4 gw0 = lds_ld<u32x4>(gwa), gw1 = lds_ld<u32x4>(gwa + 64);
    accg[0] = mfma(lds_ld<u32x4>(pa1), gw1, mfma(X.a0, gw0, accg[0]));
    accg[1] = mfma(lds_ld<u32x4>(pa1 + 2048), gw1, mfma(X.a1, gw0, accg[1]));
    asm volatile("s_waitcnt lgkmcnt(0)" : "+v"(X.a0), "+v"(X.a1), "+v"(X.b0), "+v"(X.b1), "+v"(X.b2), "+v"(X.b3), "+v"(X.b4));
    tag = __builtin_amdgcn_readfirstlane(seed) + 1u;
    const u32x2 fxv = {tag, tag ^ FMIX};
    u32 so = L_A0;
#pragma unroll
    for (int s = 0; s < 4; s += 2) {
      u32 have = g1_half_peek(acc, Y, X, ax1 + so, ab1 + so, cfull);
      so = s == 2 ? 0 : so + STG;
      TR(4 + 4 * s);
      if (RARE(have < (u32)(s + 2))) sw_wait(cfull, s + 2);
      g1_half<true>(acc, X, Y, ax0 + so, ab0 + so);
      have = g1_half_peek(acc, Y, X, ax1 + so, ab1 + so, cfull);
      so += STG;
      post(myfree, s + 2);
      TR(5 + 4 * s);
      if (RARE(have < (u32)(s + 3))) sw_wait(cfull, s + 3);
      g1_half<true>(acc, X, Y, ax0 + so, ab0 + so);
      TR(6 + 4 * s);
    }
    waitv<0>();
    st8(rws, fxo_, fxv);
    {
      u32 have = g1_half_peek(acc, Y, X, ax1 + ring(4), ab1 + ring(4), cfull);
      if (RARE(have < 6u)) sw_wait(cfull, 6);
      g1_half<true>(acc, X, Y, ax0 + ring(5), ab0 + ring(5));
      have = g1_half_peek(acc, Y, X, ax1 + ring(5), ab1 + ring(5), cfull);
      if (RARE(have < 7u)) sw_wait(cfull, 7);
      g1_half<true>(acc, X, Y, ax0 + ring(6), ab0 + ring(6));
      TR(28);
      have = g1_half_peek(acc, Y, X, ax1 + ring(6), ab1 + ring(6), cfull);
      if (RARE(have < 8u)) sw_wait(cfull, 8);
      g1_half<true>(acc, X, Y, ax0 + ring(7), ab0 + ring(7));
      have = g1_half_peek(acc, Y, X, ax1 + ring(7), ab1 + ring(7), cfull);
      if (RARE(have < 9u)) sw_wait(cfull, 9);
      g1_half<true>(acc, X, Y, ax0 + ring(8), ab0 + ring(8));
      TR(30);
      have = g1_half_peek(acc, Y, X, ax1 + ring(8), ab1 + ring(8), cfull);
      if (RARE(have < 10u)) sw_wait(cfull, 10);
      TR(32);
      g1_half<true>(acc, X, Y, ax0 + ring(9), ab0 + ring(9));
      g1_half<true>(acc, Y, X, ax1 + ring(9), ab1 + ring(9));
      g1_last(acc, Y);
      post(L0 + C_G1 + 4 * w, 1u);
    }
    const u32x4 fr10 = peek4(L0 + C_G1);
    STAMP(2);
    TR(44);
    float v[8][5];
#pragma unroll
    for (int jj = 0; jj < 8; ++jj)
#pragma unroll
      for (int nt = 0; nt < 5; ++nt) v[jj][nt] = acc[jj >> 2][nt][jj & 3];
    float e[5];
#pragma unroll
    for (int nt = 0; nt < 5; ++nt) e[nt] = v[0][nt];
#pragma unroll
    for (int jj = 1; jj < 8; ++jj) scan_step(e, v[jj], e, lr, ls);
    {
      float z[5] = {0.f, 0.f, 0.f, 0.f, 0.f}, we[5];
      scan_step(we, z, e, sa, sb);
      auto s01 = __builtin_amdgcn_permlane32_swap(f2u(we[0]), f2u(we[1]), false, false);
      auto s23 = __builtin_amdgcn_permlane32_swap(f2u(we[2]), f2u(we[3]), false, false);
      auto s44 = __builtin_amdgcn_permlane32_swap(f2u(we[4]), f2u(we[4]), false, false);
      const float y01 = u2f(s01[0]) + u2f(s01[1]), y23 = u2f(s23[0]) + u2f(s23[1]), y4 = u2f(s44[0]) + u2f(s44[1]);
      auto q03 = __builtin_amdgcn_permlane16_swap(f2u(y01), f2u(y23), false, false);
      auto q44 = __builtin_amdgcn_permlane16_swap(f2u(y4), f2u(y4), false, false);
      if (RARE(min4(fr10) < 1u)) sw_wait4(L0 + C_G1, 1);
      const u32 wa = opaque(L0 + L_WS + w * 320 + i16 * 4);
      lds_st<float>(wa + (2 * (g & 1) + (g >> 1)) * 64, u2f(q03[0]) + u2f(q03[1]));
      lds_st<float>(wa + 256, u2f(q44[0]) + u2f(q44[1]));
      const u32 ra = opaque(L0 + L_ERAW + sig * 320 + i16 * 4);
#pragma unroll
      for (int nt = 0; nt < 5; ++nt) lds_st<float>(ra + nt * 64, e[nt]);
    }
    post(L0 + C_BAR + 4 * w, 1u);
    sw_wait4(L0 + C_BAR, 1);
    if (w == 2) {
      const u32 ap = opaque(L0 + L_WS + pl * 8);
      const f32x2 se = (lds_ld<f32x2>(ap) + lds_ld<f32x2>(ap + 320)) + (lds_ld<f32x2>(ap + 640) + lds_ld<f32x2>(ap + 960));
      st16(rws, lane < 40 ? WS_SG + (u32)(c * 640 + 40 * j + lane) * 16 : OOB, u32x4{f2u(se.x), tag, f2u(se.y), tag});
    }
    STAMP(3);
    const int nvalid = (c - w + 3) >> 2;
    const f32x2 lam4 = cpowk(aA, tA, 512.f);
    f32x2 lame = cpowk(aA, tA, (float)(128 * ((c - 1 - w) & 3)));
    asm volatile("" : "+v"(lame));
    if ((xcd & 1) ? ((r & 7) >= 2 && (r & 7) < 7) : (r & 7) < 5) {
      float gl = accg[0][0];
#pragma unroll
      for (int k = 1; k < 8; ++k) gl = (i16 == k) ? accg[k >> 2][k & 3] : gl;
      st8(rws, i16 < 8 ? WS_GG + (u32)(rot10 * 2048 + 128 * c + 32 * w + 8 * g + i16) * 8 : OOB, u32x2{f2u(gl), tag});
    }
    float pf[5];
    {
      u32 era = opaque(L0 + L_ERAW + pl * 8);
      const f32x2 l8r = rot(l8);
      f32x2 pp = {0.f, 0.f};
#pragma unroll 1
      for (int s2 = 0; s2 < w; ++s2, era += 1280) {
        const f32x2 e0 = lds_ld<f32x2>(era), e1 = lds_ld<f32x2>(era + 320), e2 = lds_ld<f32x2>(era + 640), e3 = lds_ld<f32x2>(era + 960);
        pp = cmul(pp, l8, l8r) + e0; pp = cmul(pp, l8, l8r) + e1; pp = cmul(pp, l8, l8r) + e2; pp = cmul(pp, l8, l8r) + e3;
      }
      const f32x2 e0 = lds_ld<f32x2>(era), e1 = lds_ld<f32x2>(era + 320), e2 = lds_ld<f32x2>(era + 640);
      const u32 ptb = L0 + L_PT + w * 1280;
      const u32 pw = opaque(ptb + pl * 8);
#pragma unroll
      for (int k = 0; k < 4; ++k) {
        const f32x2 q = cmul(pp, c2);
        lds_st<f32x2>(pw + k * 320, f32x2{q.x, -q.y});
        if (k < 3) pp = cmul(pp, l8, l8r) + (k == 0 ? e0 : k == 1 ? e1 : e2);
      }
      const u32 pr_ = opaque(ptb + g * 320 + i16 * 4);
#pragma unroll
      for (int nt = 0; nt < 5; ++nt) pf[nt] = lds_ld<float>(pr_ + nt * 64);
      const float z[5] = {0.f, 0.f, 0.f, 0.f, 0.f};
#pragma unroll
      for (int jj = 0; jj < 8; ++jj) scan_step(v[jj], z, v[jj], ca, cb);
#pragma unroll
      for (int nt = 0; nt < 5; ++nt) ls[nt] = -ls[nt];
    }
    u32 hst[5];
    const u32 hpr = opaque(L0 + L_HP + i16 * 16), htw = opaque(L0 + L_HT + w * 1280 + i16 * 80 + g * 16),
              htr = opaque(L0 + L_HT + w * 1280 + (2 * ((lane >> 2) & 3) + (g & 1)) * 80 + (g >> 1) * 32 + (lane & 3) * 8);
    {
      const u32 hb0 = WS_H + (u32)(8 * c + 2 * w + (g >> 1)) * 40960 + (g & 1) * 2048 + i16 * 16;
#pragma unroll
      for (int nt = 0; nt < 5; ++nt) {
        const int o = 5 * j + nt;
        hst[nt] = opaque(hb0 + (u32)((o >> 3) * 4096 + ((o >> 2) & 1) * 1024 + (o & 3) * 256));
      }
      u32 off[4];
      f32x2 mk[3];
#pragma unroll
      for (int k = 0; k < 4; ++k) off[k] = opaque((k < nvalid && lane < 40) ? WS_SG + (u32)((w + 4 * k) * 640 + 40 * j + lane) * 16 : OOB);
#pragma unroll
      for (int k = 0; k < 3; ++k) { mk[k] = k + 1 < nvalid ? lam4 : f32x2{1.f, 0.f}; asm volatile("" : "+v"(mk[k])); }
      const u32 hp = opaque(L0 + L_HP + min(lane, 40) * 32 + w * 4);
      u32x4 sv[4];
#pragma unroll 1
      for (int it = 0; it < SPIN_CAP; ++it) {
        asm volatile("" ::: "memory");
        bool ok = true;
#pragma unroll
        for (int k = 0; k < 4; ++k) sv[k] = ld16(rws, off[k]);
#pragma unroll
        for (int k = 0; k < 4; ++k) ok &= (off[k] == OOB) | ((sv[k].y == tag) & (sv[k].w == tag));
        if (wave_all(ok)) break;
        __builtin_amdgcn_s_sleep(1);
      }
      asm volatile("" ::: "memory");
      STAMP(4);
      f32x2 hsum = {u2f(sv[0].x), u2f(sv[0].z)};
#pragma unroll
      for (int k = 1; k < 4; ++k) hsum = cmul(hsum, mk[k - 1]) + f32x2{u2f(sv[k].x), u2f(sv[k].z)};
      hsum = cmul(hsum, lame);
      lds_st<float>(hp, hsum.x);
      lds_st<float>(hp + 16, hsum.y);
    }
    post(L0 + C_BAR + 4 * w, 2u);
    sw_wait4(L0 + C_BAR, 2);
    {
      float hin[5], h0[5];
#pragma unroll
      for (int nt = 0; nt < 5; ++nt) {
        const f32x4 q = lds_ld<f32x4>(hpr + nt * 256);
        hin[nt] = (q.x + q.y) + (q.z + q.w);
      }
      scan_step(h0, pf, hin, ha, hb);
      scan_step(v[0], v[0], h0, lr, ls);
#pragma unroll
      for (int jj = 1; jj < 8; ++jj) scan_step(v[jj], v[jj], v[jj - 1], lr, ls);
    }
    {
      const u32 wa = htw, ra = htr;
      u32x2 lo[5], hi[5];
#pragma unroll
      for (int nt = 0; nt < 5; ++nt) {
        lds_st<u32x4>(wa, u32x4{pk(v[0][nt], v[1][nt]), pk(v[2][nt], v[3][nt]), pk(v[4][nt], v[5][nt]), pk(v[6][nt], v[7][nt])});
        lo[nt] = __builtin_bit_cast(u32x2, lds_tr(ra));
        hi[nt] = __builtin_bit_cast(u32x2, lds_tr(ra + 640));
      }
      SB();
#pragma unroll
      for (int nt = 0; nt < 5; ++nt) st16(rws, hst[nt], u32x4{lo[nt].x, lo[nt].y, hi[nt].x, hi[nt].y});
    }
    post(L0 + C_BAR + 4 * w, 3u);
    STAMP(5);
    const u32 go = WS_GG + (u32)(g * 2048 + 64 * ti + 16 * kq + i16) * 8, go2 = g < 2 ? go + 8 * 2048 * 8 : OOB;
    u32x2 g0 = ld8(rws, go), g1 = ld8(rws, go + 4 * 2048 * 8), g2 = ld8(rws, go2);
    waitv<0>();
    st8(rws, lane == 0 ? WS_FH + (u32)((c * 4 + w) * 16 + j) * 128 : OOB, u32x2{tag, tag ^ FMIX});
    sw_wait(ccf, 1);
    G2_REGS(cr, 2, L_A0 + 3 * 10240);
    dma_mark(rka, SEQ, L0 + C_C0 + 4 * w);
#pragma unroll 1
    for (int it = 0; it < SPIN_CAP && RARE(!wave_all((g0.y == tag) & (g1.y == tag) & ((g2.y == tag) | (g >= 2)))); ++it) {
      __builtin_amdgcn_s_sleep(1);
      asm volatile("" ::: "memory");
      g0 = ld8(rws, go); g1 = ld8(rws, go + 4 * 2048 * 8); g2 = ld8(rws, go2);
    }
    float gate, ogate;
    {
      const float s3 = (u2f(g0.x) + u2f(g1.x)) + u2f(g2.x);
      auto sa_ = __builtin_amdgcn_permlane32_swap(f2u(s3), f2u(s3), false, false);
      const float s2 = u2f(sa_[0]) + u2f(sa_[1]);
      auto sb_ = __builtin_amdgcn_permlane16_swap(f2u(s2), f2u(s2), false, false);
      gate = rbf(1.f / (1.f + expf(-rbf(u2f(sb_[0]) + u2f(sb_[1])))));
      ogate = rbf(1.f - gate);
    }
    u32x2 ska[5], skb[5];
#pragma unroll
    for (int nt = 0; nt < 5; ++nt) {
#pragma unroll
      for (int e = 0; e < 2; ++e) {
        const float x0 = bflo(xq[nt][e]), x1 = bfhi(xq[nt][e]);
        ska[nt][e] = pk(x0 * bflo(eda[nt][e]), x1 * bfhi(eda[nt][e]));
        skb[nt][e] = pk(x0 * bflo(edb[nt][e]), x1 * bfhi(edb[nt][e]));
      }
      asm volatile("" : "+v"(ska[nt]), "+v"(skb[nt]));
    }
    poll_flags(rws, fho, tag);
    asm volatile("" ::: "memory");
    __builtin_amdgcn_s_setprio(0);
    STAMP(6);
    G2_LOADH();
    STAMP(7);
    G2_INIT(L_A0);

    g2_half0<false>(y, cr[0], hf_[0]); g2_half0<false>(y, cr[1], hf_[1]); g2_half0<false>(y, cr[2], hf_[2]);
    sw_wait(ccf, 2);
    g2_half<0, 4>(y, P0, cr[3], hf_[3], cab[0][0]);
    STAMP(8);

    G2_UN(2, 0); G2_UN(3, 1); G2_U(4, 2, 3);

    G2_UN(5, 3);

    STAMP(9);
    G2_U(6, 4, 6);
    lds_st<float>(opaque(L0 + L_GATE + (16 * w + i16) * 4), gate);
    post(L0 + C_BAR + 4 * w, 4u);
    waitl();
    const u32 bo = WS_BT + (u32)(80 * dj + (lane >> 2)) * 8192 + (64 * ti + 16 * w) * 4 + (lane & 3) * 16;
    u32x4 bv[5];
#pragma unroll
    for (int k = 0; k < 5; ++k) bv[k] = ld16(rws, bo + k * 16 * 8192);
    SB();
    G2_U(7, 5, 8);
    waitl();
    STAMP(10);
#pragma unroll 1
    for (int it = 0; it < SPIN_CAP; ++it) {
      bool ok = true;
#pragma unroll
      for (int k = 0; k < 5; ++k) ok &= (bv[k].y == tag) & (bv[k].w == tag);
      if (!RARE(!wave_all(ok))) break;
      __builtin_amdgcn_s_sleep(2);
      asm volatile("" ::: "memory");
#pragma unroll
      for (int k = 0; k < 5; ++k) bv[k] = ld16(rws, bo + k * 16 * 8192);
    }
    STAMP(11);
    sw_wait4(L0 + C_BAR, 4);
    f32x4 base[5];
    {
      const u32 ba = opaque(L0 + L_BST + w * 2560 + lane * 8);
#pragma unroll
      for (int k = 0; k < 5; ++k) lds_st<u32x2>(ba + k * 512, u32x2{bv[k].x, bv[k].z});
#pragma unroll
      for (int nt = 0; nt < 5; ++nt) {
        const u32x2 yb = __builtin_bit_cast(u32x2, lds_tr(ba + nt * 512));
#pragma unroll
        for (int e = 0; e < 2; ++e) {
          const u32 bb = pk(bflo(yb[e]) + bflo(skb[nt][e]), bfhi(yb[e]) + bfhi(skb[nt][e]));
          const u32 ob = pk(ogate * bflo(bb), ogate * bfhi(bb));
          base[nt][2 * e] = gate * bflo(ska[nt][e]) + bflo(ob);
          base[nt][2 * e + 1] = gate * bfhi(ska[nt][e]) + bfhi(ob);
        }
        asm volatile("" : "+v"(base[nt]));
      }
    }
    G2_U(8, 6, 10); G2_ULAST(9, 7);
    {
      const u32 ia = opaque(ima);
#pragma unroll
      for (int nt = 0; nt < 5; ++nt) lds_st<f32x4>(ia + nt * 64, gate * y[nt] + base[nt]);
    }
  } else {
    const int pr = xcd * 16 + (r >> 1);
    const int nch = (r & 1) ? 2 : 3, ch = 5 * pr + ((r & 1) ? 3 : 0) + kq;
    const u32 slot = opaque(L0 + L_B0 + kq * BSLOT) ;
    u32x2 fhv;
    f32x4 ya[8];
    if (__builtin_expect(kq < nch, 1)) {
      const GAS u32* pa = (const GAS u32*)P.Are + ch * 64 + lane;
      const u32 wAre = *pa, wAim = ((const GAS u32*)P.Aim)[ch * 64 + lane];
      const u32 wBre = ((const GAS u32*)P.sBre)[ch * 64 + lane], wBim = ((const GAS u32*)P.sBim)[ch * 64 + lane];
      const u32 wCre = ((const GAS u32*)P.sCre)[ch * 64 + lane], wCim = ((const GAS u32*)P.sCim)[ch * 64 + lane];
      const u32 wdt = ((const GAS u16*)P.ldtB)[ch];
      const float dt = rbf(expf(bf2f(wdt)));
      f32x4 ka[3] = {f32x4{0.f, 0.f, 0.f, 0.f}, f32x4{0.f, 0.f, 0.f, 0.f}, f32x4{0.f, 0.f, 0.f, 0.f}};
      const u32 tr = opaque(slot + i16 * TROW + g * 16);
      const u32 fxo = lane < 32 ? WS_FX + (u32)((lane * 8 + ch / 80) * 4 + (ch % 80) / 20) * 128 : OOB;
      u32 tv[56], tu[56];
#pragma unroll 1
      for (int rd = 0; rd < 2; ++rd) {
#pragma unroll
        for (int k = 0; k < 56; ++k) tu[k] = tv[k];
        const u32 sh = rd ? 0u : 16u;
        const float a = dt * bfhi(wAre << sh), t = dt * bfhi(wAim << sh);
        const f32x2 db = {dt * bfhi(wBre << sh), dt * bfhi(wBim << sh)}, cc = {bfhi(wCre << sh), bfhi(wCim << sh)};
        const f32x2 W = cmul(cc, db);
        const f32x2 z1 = cpowk(a, t, 1.f), z8 = cpowk(a, t, 8.f), z128 = cpowk(a, t, 128.f);
        {
          f32x2 cv = z8;
          tv[41] = pk(cv.x, cv.y);
#pragma unroll
          for (int jj = 2; jj < 16; ++jj) { cv = cmul(cv, z8); tv[40 + jj] = pk(cv.x, cv.y); }
        }
        {
          const f32x2 y1 = {z1.x, -z1.y}, yh = {z128.x, -z128.y};
          f32x2 R[8];
          R[0] = f32x2{W.x, -W.y};
#pragma unroll
          for (int k = 1; k < 8; ++k) R[k] = cmul(R[k - 1], y1);
#pragma unroll
          for (int k = 0; k < 8; ++k) tv[k] = pk(R[k].x, R[k].y);
#pragma unroll
          for (int k = 0; k < 8; ++k) { R[k] = cmul(R[k], yh); tv[8 + k] = pk(R[k].x, R[k].y); }
#pragma unroll
          for (int hh = 0; hh < 2; ++hh)
#pragma unroll
            for (int k = 0; k < 4; ++k) { R[2 * k] = cmul(R[2 * k], yh); tv[16 + 4 * hh + k] = pk(R[2 * k].x, R[2 * k].y); }
#pragma unroll
          for (int hh = 0; hh < 4; ++hh)
#pragma unroll
            for (int k = 0; k < 2; ++k) { R[4 * k] = cmul(R[4 * k], yh); tv[24 + 2 * hh + k] = pk(R[4 * k].x, R[4 * k].y); }
#pragma unroll
          for (int hh = 0; hh < 9; ++hh) { R[0] = cmul(R[0], yh); tv[32 + hh] = pk(R[0].x, R[0].y); }
        }
      }
      STAMP(1);
      get_tag();
      const u32 kc = slot + KC_OFF;
      const bool r1 = lane < 32;
      const u32 xa = opaque(kc - 4 + (r1 ? 16 * lane : 256 + 8 * lane)), xb = opaque(xa + (r1 ? 12 : 8));
      const u32 sA = opaque(r1 ? 0x05040706u : 0x03020504u), sB = opaque(r1 ? 0x05040706u : 0x05040504u), sC = opaque(r1 ? 0x03020504u : 0x01000302u),
                sD = opaque(r1 ? 0x03020504u : 0x05040504u), sE = opaque(lane <= 32 ? 0x03020504u : 0x05040504u);
      const u32 ka0 = opaque(kc + (128 * (g >> 1) + 8 * i16 + 4 * (g & 1)) * 2);
      const u32 ka1 = opaque(kc + (g < 2 ? 256 + 64 * g + 4 * i16 : 256 + 64 * g + 2 * i16) * 2), ka1b = opaque(ka1 + (g < 2 ? 4 : 64));
      const u32 ka2 = opaque(kc + (512 + 64 * min(g, 2) + i16) * 2);
      const u32 sbase = __builtin_amdgcn_readfirstlane(slot);
      const u32 uo = opaque(WS_XT + (u32)(ch >> 2) * 16384 + (ch & 3) * 16 + lane * 64), uw = opaque(slot + UPAD + lane * 16);
      sw_wait(L0 + C_FULL, 10);
      const u32x2 fx = ld8(rws, fxo);
      sw_wait4(L0 + C_G1, 1);
      STAMP(6);
#define TW(v, row) asm volatile("ds_write_addtid_b32 %0 offset:%1" :: "v"(v), "n"((row) * TROW) : "memory")
      asm volatile("s_mov_b32 m0, %0\n\ts_nop 0" :: "s"(sbase) : "m0", "memory");
      TW(0x00003f80u, 41);
      u32x4 uv[4];
#pragma unroll
      for (int rd = 0; rd < 2; ++rd) {
#pragma unroll
        for (int k = 0; k < 56; ++k) TW(rd ? tv[k] : tu[k], k < 41 ? k : k + 1);
        if (rd == 1) {
          if (RARE(!flag_ok(fx, fxo, tag))) poll_flags(rws, fxo, tag);
          asm volatile("" ::: "memory");
#pragma unroll
          for (int k = 0; k < 4; ++k) uv[k] = ld16(rws, uo + k * 4096);
        }
#pragma unroll
        for (int ks = 0; ks < 4; ++ks) {
          const u32x4 bb = lds_ld<u32x4>(tr + 41 * TROW + ks * 64);
#pragma unroll
          for (int m = 0; m < 3; ++m) ka[m] = mfma(lds_ld<u32x4>(tr + 16 * m * TROW + ks * 64), bb, ka[m]);
        }
      }
#define UW(v, off) asm volatile("ds_write_addtid_b32 %0 offset:%1" :: "v"(v), "n"(off) : "memory")
#pragma unroll
      for (int cc = 0; cc < 4; ++cc) { UW(0u, cc * UCOPY); UW(0u, cc * UCOPY + 144); }
      STAMP(5);
      {
        const u32 q0 = pk(ka[2].x, ka[2].y), q1 = pk(ka[2].z, ka[2].w);
        if (g < 3) { lds_st<u16>(ka2, (u16)q0); lds_st<u16>(ka2 + 32, (u16)(q0 >> 16)); lds_st<u16>(ka2 + 64, (u16)q1); lds_st<u16>(ka2 + 96, (u16)(q1 >> 16)); }
        lds_st<u32x2>(ka0, u32x2{pk(ka[0].x, ka[0].y), pk(ka[0].z, ka[0].w)});
        lds_st<u32>(ka1, pk(ka[1].x, ka[1].y));
        lds_st<u32>(ka1b, pk(ka[1].z, ka[1].w));
        lds_st<u32>(kc - 4, 0u);
      }
#define PERM(hi, lo, sel) __builtin_amdgcn_perm(hi, lo, sel)
      {
        const u32 R0 = lds_ld<u32>(xa), R1 = lds_ld<u32>(xa + 4), R2 = lds_ld<u32>(xa + 8), R3 = lds_ld<u32>(xb), R4 = lds_ld<u32>(xb + 4);
        const u32 ea_ = opaque(kc + 768 + 4 * lane), eb_ = opaque(kc + 1024 + 2 * lane);
        const u32 E0 = lds_ld<u32>(ea_), E1 = lds_ld<u32>(ea_ + 4);
        const u32 f0 = lds_ld<u16>(eb_), f1 = lds_ld<u16>(eb_ + 2), f2 = lds_ld<u16>(eb_ + 128), f3 = lds_ld<u16>(eb_ + 130);
        UW(0u, KOFF); UW(0u, KOFF + KCOPY);
        const u32 k0 = opaque(slot + KOFF + KPAD + lane * 16);
        lds_st<u32x4>(k0 + KCOPY, u32x4{PERM(R4, R3, sA), PERM(R3, R2, sB), PERM(R2, R1, sA), PERM(R1, R0, sB)});
        lds_st<u32x4>(k0, u32x4{PERM(R4, R3, sC), PERM(R3, R2, sD), PERM(R2, R1, sC), PERM(R1, R0, sE)});
        const u32 e00 = PERM(E0, E0, 0x01000100u), e11 = PERM(E0, E0, 0x03020302u);
        lds_st<u32x4>(k0 + KCOPY + 1024, u32x4{PERM(E1, E1, 0x01000100u), e11, PERM(E0, E0, 0x01000302u), e00});
        lds_st<u32x4>(k0 + 1024, u32x4{PERM(E0, E1, 0x07060100u), e11, e00, e00});
#pragma unroll
        for (int rr = 0; rr < 2; ++rr) {
          const u32 e0 = rr ? f2 : f0, e1 = rr ? f3 : f1, t4 = (lane & 1) ? e1 : e0;
          const u32 c00 = e0 | (e0 << 16), c11 = e1 | (e1 << 16);
          lds_st<u32x4>(k0 + KCOPY + 2048 + 1024 * rr, u32x4{c11, e1 | (t4 << 16), c00, c00});
          lds_st<u32x4>(k0 + 2048 + 1024 * rr, u32x4{c11, t4 | (e0 << 16), c00, c00});
        }
      }
#pragma unroll
      for (int k = 0; k < 4; ++k) {
        lds_st<u32x4>(uw + k * 1024, uv[k]);
#pragma unroll
        for (int e = 0; e < 4; ++e) { lds_st<u32>(uw + k * 1024 + UCOPY + 4 + 4 * e, uv[k][e]); lds_st<u32>(uw + k * 1024 + 3 * UCOPY + 12 + 4 * e, uv[k][e]); }
        lds_st<u32x2>(uw + k * 1024 + 2 * UCOPY + 8, u32x2{uv[k].x, uv[k].y});
        lds_st<u32x2>(uw + k * 1024 + 2 * UCOPY + 16, u32x2{uv[k].z, uv[k].w});
      }
      post(L0 + C_BPREP + 4 * kq, 1u);
      STAMP(2);
#pragma unroll
      for (int A = 0; A < 8; ++A) ya[A] = f32x4{0.f, 0.f, 0.f, 0.f};
      const u32 ta = opaque(slot + KOFF + KPAD + (i16 & 1) * KCOPY + ((i16 >> 1) - g) * 16 - 64);
      const u32 ua = opaque(slot + (i16 & 3) * UCOPY + (i16 >> 2) * 128 + g * 16);
      u32x4 T[2][8], U[2][8];
#define CV_READ(S, st)                                                                                        \
      _Pragma("unroll") for (int D = 0; D < 8; ++D) {                                                         \
        T[S][D] = lds_ld<u32x4>(ta + ((st) >> 1) * 128 + D * 512 + (1 - ((st) & 1)) * 64);                   \
        U[S][D] = lds_ld<u32x4>(ua + UPAD - ((st) >> 1) * 128 + D * 512 + ((st) & 1) * 64);                  \
      }
      CV_READ(0, 0);
      __builtin_amdgcn_s_setprio(2);
#pragma unroll
      for (int st = 0; st < 8; ++st) {
        SB();
        if (st == 6) fhv = ld8(rws, fho);
        if (st < 7) { CV_READ((st + 1) & 1, st + 1); }
#pragma unroll
        for (int S = 0; S < 8; ++S)
#pragma unroll
          for (int D = 0; D <= S; ++D) ya[S] = mfma(T[st & 1][D], U[st & 1][S - D], ya[S]);
        if (st < 7) {
#pragma unroll
          for (int k = 0; k < 16; ++k) {
            __builtin_amdgcn_sched_group_barrier(0x8, 2, 0);
            __builtin_amdgcn_sched_group_barrier(0x100, 1, 0);
          }
          __builtin_amdgcn_sched_group_barrier(0x8, 4, 0);
        }
      }
      SB();
      STAMP(3);
    } else {
      get_tag();
      sw_wait4(L0 + C_G1, 1);
      post(L0 + C_BPREP + 4 * kq, 1u);
      fhv = ld8(rws, fho);
#pragma unroll
      for (int A = 0; A < 8; ++A) ya[A] = f32x4{0.f, 0.f, 0.f, 0.f};
    }
    sw_wait(ccf, 1);
    G2_REGS(cr, 3, L_A0 + 5 * 10240);
    post(L0 + C_BDONE + 4 * kq, 1u);
    {
      const bool odd = lane & 1;
      const u32 bo = kq < nch ? WS_BT + (u32)ch * 8192 + (i16 >> 2) * 256 + g * 64 + (odd ? 0 : 32) + ((lane & 2) ? 0 : 16) : OOB;
#pragma unroll
      for (int A = 0; A < 8; ++A) {
        const u32 x0 = pk(ya[A].x, ya[A].y), x1 = pk(ya[A].z, ya[A].w);
        const u32 rv = __builtin_amdgcn_update_dpp(0u, odd ? x1 : x0, 0xB1, 0xf, 0xf, true);
        st16(rws, bo + A * 1024, u32x4{odd ? x0 : rv, tag, odd ? rv : x1, tag});
      }
    }
    if (RARE(!flag_ok(fhv, fho, tag))) poll_flags(rws, fho, tag);
    asm volatile("" ::: "memory");
    __builtin_amdgcn_s_setprio(0);
    STAMP(4);
    G2_LOADH();
    G2_INIT(L_B0);

    STAMP(8);
#pragma unroll
    for (int k = 0; k < 5; ++k) g2_half0<false>(y, cr[k], hf_[k]);
    sw_wait(ccf, 4);
    g2_half<0, 4>(y, P0, cr[5], hf_[5], cab[0][0]);
    STAMP(9);

    G2_U(3, 0, 5); G2_U(4, 1, 7); G2_U(5, 2, 9);

    G2_U(6, 3, 11); G2_U(7, 4, 12); G2_U(8, 5, 13); G2_ULAST(9, 6);
    G2_B_IMAGE();
  }

  hwbar();
  STAMP(12);
  if (b == 255 && tid == 0) __hip_atomic_store((GAS u32*)P.ws, tag, __ATOMIC_RELAXED, __HIP_MEMORY_SCOPE_AGENT);
  {
    const int et = tid >> 3, es = tid & 7;
    const u32 ia = opaque(L0 + L_IMG0 + et * 320 + ((es & 4) | ((es ^ (et >> 1)) & 3)) * 16);
    const float gt = lds_ld<float>(opaque(L0 + L_GATE + et * 4));
    const rsrc_t ro = mksrd(P.out, 2048 * 2560);
    const u32 oo = (64 * ti + et) * 2560 + dj * 320 + es * 16;
    const f32x4 o0 = lds_ld<f32x4>(ia) + gt * lds_ld<f32x4>(ia + (L_IMG1 - L_IMG0));
    const f32x4 o1 = lds_ld<f32x4>(ia + 128) + gt * lds_ld<f32x4>(ia + 128 + (L_IMG1 - L_IMG0));
    const f32x4 o2 = lds_ld<f32x4>(ia + 256) + gt * lds_ld<f32x4>(ia + 256 + (L_IMG1 - L_IMG0));
    st16(ro, oo, __builtin_bit_cast(u32x4, o0));
    st16(ro, oo + 128, __builtin_bit_cast(u32x4, o1));
    st16(ro, es < 4 ? oo + 256 : OOB, __builtin_bit_cast(u32x4, o2));
  }
  DUMP();
  STAMP(13);
}

extern "C" void kernel_launch(void* const* d_in, const int* in_sizes, int n_in, void* d_out, int out_size, void* d_ws,
                              size_t ws_size, hipStream_t stream) {
  Params p;
  p.x = (const u16*)d_in[0];
  p.Lre = (const u16*)d_in[1];
  p.Lim = (const u16*)d_in[2];
  p.Bre = (const u16*)d_in[3];
  p.Bim = (const u16*)d_in[4];
  p.Cre = (const u16*)d_in[5];
  p.Cim = (const u16*)d_in[6];
  p.ldtA = (const u16*)d_in[7];
  p.DA = (const u16*)d_in[8];
  p.Are = (const u16*)d_in[10];
  p.Aim = (const u16*)d_in[11];
  p.sBre = (const u16*)d_in[12];
  p.sBim = (const u16*)d_in[13];
  p.sCre = (const u16*)d_in[14];
  p.sCim = (const u16*)d_in[15];
  p.ldtB = (const u16*)d_in[16];
  p.DB = (const u16*)d_in[17];
  p.gw = (const u16*)d_in[18];
  p.out = (float*)d_out;
  p.ws = (char*)d_ws;
  fused_kernel<<<256, 512, 0, stream>>>(p);
}
```

```cpp
#include <hip/hip_runtime.h>
#include <stdint.h>

typedef unsigned short u16;
typedef unsigned int u32;
typedef unsigned long long u64;
typedef __bf16 bf16x8 __attribute__((ext_vector_type(8)));
typedef short s16x4 __attribute__((ext_vector_type(4)));
typedef float f32x4 __attribute__((ext_vector_type(4)));
typedef float f32x2 __attribute__((ext_vector_type(2)));
typedef u32 u32x4 __attribute__((ext_vector_type(4)));
typedef u32 u32x2 __attribute__((ext_vector_type(2)));
typedef u64 u64x8 __attribute__((ext_vector_type(8)));
typedef u64 u64x4 __attribute__((ext_vector_type(4)));
typedef __bf16 bf16x2_t __attribute__((ext_vector_type(2)));
typedef __amdgpu_buffer_rsrc_t rsrc_t;
#define LAS __attribute__((address_space(3)))
#define GAS __attribute__((address_space(1)))
#define DEV __device__ __forceinline__
#define INL __attribute__((always_inline))
#if defined(__HIP_DEVICE_COMPILE__)
#define NOPK __attribute__((target("no-packed-fp32-ops")))
#else
#define NOPK
#endif

constexpr u32 WS_E = 0;
constexpr u32 WS_FH = 4096;
constexpr u32 WS_FX = WS_FH + 1024 * 128;
constexpr u32 WS_SG = WS_FX + 1024 * 128;
constexpr u32 WS_GG = WS_SG + 16 * 640 * 16;
constexpr u32 WS_XT = WS_GG + 10 * 2048 * 8;
constexpr u32 WS_BT = WS_XT + 640 * 2048 * 2;
constexpr u32 WS_H = WS_BT + 640 * 1024 * 8;
constexpr u32 WS_END = WS_H + 128 * 40 * 1024;
constexpr u32 WS_DIAG = (WS_END + 4095) & ~4095u;

constexpr u32 STG = 26624;
constexpr u32 L_A0 = 81920;
constexpr u32 L_B0 = 0;
__attribute__((always_inline)) constexpr u32 ring(int s) { return s % 6 < 3 ? L_A0 + (s % 6) * STG : (s % 6 - 3) * STG; }
constexpr u32 BSLOT = 27008;
constexpr u32 L_CNT = 81024;
constexpr u32 C_FREE = L_CNT, C_PRO = L_CNT + 16, C_BAR = L_CNT + 32, C_BDONE = L_CNT + 48, C_C0 = L_CNT + 64, C_BPREP = L_CNT + 80,
              C_STRIP = L_CNT + 96, C_FULL = L_CNT + 112, C_CFULL = L_CNT + 116, C_HOK = L_CNT + 120,
              C_G1 = L_CNT + 128, C_FLUSH = L_CNT + 144, L_TAG = L_CNT + 160;
constexpr u32 L_GATE = L_CNT + 256;
constexpr u32 L_DSK = L_A0 + 3 * STG;
constexpr u32 L_GW = L_DSK + 320;
constexpr u32 L_RAW = ring(5);
constexpr u32 L_TAB = L_RAW + 10240;
constexpr u32 L_ERAW = L_A0;
constexpr u32 L_WS = L_A0 + 5120;
constexpr u32 L_HP = L_A0 + 6400;
constexpr u32 L_PT = L_A0 + 7936;
constexpr u32 L_HT = L_A0 + 13056;
constexpr u32 L_BST = L_A0;
constexpr u32 L_IMG0 = L_A0 + 10240, L_IMG1 = L_A0 + 30720;
constexpr u32 UCOPY = 4640, UPAD = 384, KOFF = 4 * UCOPY, KCOPY = 4224, KPAD = 112;
constexpr u32 KC_OFF = 16640;
constexpr u32 TROW = 288;

constexpr int SPIN_CAP = 1 << 16;

static __device__ const __attribute__((aligned(64))) u32 seq_tab[16] = {1, 2, 3, 4, 5, 6, 7, 8, 9, 10, 11, 12, 13, 14, 15, 16};

template <bool B> struct BoolC { static constexpr bool value = B; };

struct Params {
  const u16 *x, *Lre, *Lim, *Bre, *Bim, *Cre, *Cim, *ldtA, *DA;
  const u16 *Are, *Aim, *sBre, *sBim, *sCre, *sCim, *ldtB, *DB, *gw;
  float* out;
  char* ws;
};

DEV bool wave_all(bool p) { return __builtin_amdgcn_ballot_w64(!p) == 0; }
DEV float bf2f(u32 v) { return __builtin_bit_cast(float, v << 16); }
DEV float bflo(u32 v) { return __builtin_bit_cast(float, v << 16); }
DEV float bfhi(u32 v) { return __builtin_bit_cast(float, v & 0xffff0000u); }
DEV u32 pk(float a, float b) { bf16x2_t v = {(__bf16)a, (__bf16)b}; return __builtin_bit_cast(u32, v); }
DEV float rbf(float a) { return bflo(pk(a, a)); }
DEV u32 f2u(float a) { return __builtin_bit_cast(u32, a); }
DEV float u2f(u32 a) { return __builtin_bit_cast(float, a); }
DEV float swp(float x) { return u2f(__builtin_amdgcn_update_dpp(0, f2u(x), 0xB1, 0xf, 0xf, true)); }
DEV f32x2 rot(f32x2 z) { return f32x2{-z.y, z.x}; }
DEV f32x2 cmul(f32x2 c, f32x2 z, f32x2 zr) { return __builtin_elementwise_fma(f32x2{c.y, c.y}, zr, f32x2{c.x, c.x} * z); }
DEV f32x2 cmul(f32x2 c, f32x2 z) { return cmul(c, z, rot(z)); }
DEV f32x2 cfma(f32x2 c, f32x2 z, f32x2 e) {
  return f32x2{__builtin_fmaf(-c.y, z.y, __builtin_fmaf(c.x, z.x, e.x)), __builtin_fmaf(c.y, z.x, __builtin_fmaf(c.x, z.y, e.y))};
}
DEV f32x2 cpowk(float a, float th, float k) {
  const float L2E = 1.44269504088896f, IH = 0.15915494f, IL = 6.4206382e-9f;
  float m = __builtin_amdgcn_exp2f(k * a * L2E);
  float x = k * th;
  float hi = x * IH;
  float lo = __builtin_fmaf(x, IL, __builtin_fmaf(x, IH, -hi));
  float r = __builtin_amdgcn_fractf(hi) + lo;
  return f32x2{m * __builtin_amdgcn_cosf(r), m * __builtin_amdgcn_sinf(r)};
}
template <typename T> DEV T lds_ld(u32 a) { typedef T __attribute__((may_alias)) TA; return *(const LAS TA*)(size_t)a; }
template <typename T> DEV void lds_st(u32 a, T v) { typedef T __attribute__((may_alias)) TA; *(LAS TA*)(size_t)a = v; }
DEV s16x4 lds_tr(u32 a) { return __builtin_amdgcn_ds_read_tr16_b64_v4i16((LAS s16x4*)(size_t)a); }
DEV f32x4 mfma(u32x4 a, u32x4 b, f32x4 c) {
  return __builtin_amdgcn_mfma_f32_16x16x32_bf16(__builtin_bit_cast(bf16x8, a), __builtin_bit_cast(bf16x8, b), c, 0, 0, 0);
}
DEV rsrc_t mksrd(const void* p, u32 n) { return __builtin_amdgcn_make_buffer_rsrc((void*)p, (short)0, (int)n, 0x00020000); }
DEV void dma16(rsrc_t rs, u32 voff, u32 soff, u32 lds) {
  asm volatile("s_mov_b32 m0, %0\n\ts_nop 0\n\tbuffer_load_dwordx4 %1, %2, %3 offen lds"
               :: "s"(lds), "v"(voff), "s"(rs), "s"(soff) : "m0", "memory");
}
DEV void dma64v(rsrc_t rs, u32 v0, u32 v1, u32 v2, u32 v3, u32 so, u32 lds) {
  asm volatile("s_mov_b32 m0, %0\n\ts_nop 0\n\tbuffer_load_dwordx4 %1, %5, %6 offen lds\n\t"
               "buffer_load_dwordx4 %2, %5, %6 offen offset:1024 lds\n\tbuffer_load_dwordx4 %3, %5, %6 offen offset:2048 lds\n\t"
               "buffer_load_dwordx4 %4, %5, %6 offen offset:3072 lds"
               :: "s"(lds), "v"(v0), "v"(v1), "v"(v2), "v"(v3), "s"(rs), "s"(so) : "m0", "memory");
}
DEV void dma32v(rsrc_t rs, u32 v0, u32 v1, u32 so, u32 lds) {
  asm volatile("s_mov_b32 m0, %0\n\ts_nop 0\n\tbuffer_load_dwordx4 %1, %3, %4 offen lds\n\t"
               "buffer_load_dwordx4 %2, %3, %4 offen offset:1024 lds"
               :: "s"(lds), "v"(v0), "v"(v1), "s"(rs), "s"(so) : "m0", "memory");
}
template <int OFF> DEV void dmaq(rsrc_t r0, rsrc_t r1, u32 v0, u32 v1, u32 v2, u32 v3, u32 so, u32 slot) {
  asm volatile("s_add_u32 m0, %0, %8\n\ts_nop 0\n\tbuffer_load_dwordx4 %1, %5, %7 offen lds\n\t"
               "buffer_load_dwordx4 %2, %6, %7 offen offset:1024 lds\n\tbuffer_load_dwordx4 %3, %6, %7 offen offset:2048 lds\n\t"
               "buffer_load_dwordx4 %4, %6, %7 offen offset:3072 lds"
               :: "s"(slot), "v"(v0), "v"(v1), "v"(v2), "v"(v3), "s"(r0), "s"(r1), "s"(so), "n"(OFF) : "m0", "scc", "memory");
}
template <int OFF> DEV void dmad(rsrc_t r0, u32 v0, u32 v1, u32 so, u32 slot) {
  asm volatile("s_add_u32 m0, %0, %5\n\ts_nop 0\n\tbuffer_load_dwordx4 %1, %3, %4 offen lds\n\t"
               "buffer_load_dwordx4 %2, %3, %4 offen offset:1024 lds"
               :: "s"(slot), "v"(v0), "v"(v1), "s"(r0), "s"(so), "n"(OFF) : "m0", "scc", "memory");
}
DEV void dma_mark(rsrc_t rs, u32 so, u32 lds) {
  u64 keep;
  asm volatile("s_mov_b32 m0, %1\n\ts_mov_b64 %0, exec\n\ts_mov_b64 exec, 1\n\tbuffer_load_dword off, %2, %3 lds\n\ts_mov_b64 exec, %0"
               : "=&s"(keep) : "s"(lds), "s"(rs), "s"(so) : "m0", "memory");
}
DEV u32 opaque(u32 v) { asm volatile("" : "+v"(v)); return v; }
#define SB() __builtin_amdgcn_sched_barrier(0)
#define RARE(x) __builtin_expect(!!(x), 0)
template <int N> DEV void waitv() { asm volatile("s_waitcnt vmcnt(%0)" :: "n"(N) : "memory"); }
DEV void waitl() { asm volatile("s_waitcnt lgkmcnt(0)" ::: "memory"); }
DEV void hwbar() { waitl(); __builtin_amdgcn_s_barrier(); asm volatile("" ::: "memory"); }
DEV void post(u32 a, u32 v) {
  asm volatile("" ::: "memory");
  lds_st<u32>(a, v);
  asm volatile("" ::: "memory");
}
DEV u32 min4(u32x4 v) { return (u32)__builtin_amdgcn_readfirstlane(min(min(v.x, v.y), min(v.z, v.w))); }
DEV u32x4 peek4(u32 a) { asm volatile("" ::: "memory"); return lds_ld<u32x4>(a); }
DEV void sw_wait4(u32 a, u32 target) {
#pragma unroll 1
  for (u32 it = 0; it < (1u << 22) && min4(peek4(a)) < target; ++it) {}
  asm volatile("" ::: "memory");
}
DEV void sw_wait(u32 a, u32 target) {
  u32 v, sv, cnt = 1u << 22;
  asm volatile("1:\n\tds_read_b32 %0, %4\n\ts_sub_u32 %2, %2, 1\n\ts_cselect_b32 %3, 0, %3\n\ts_waitcnt lgkmcnt(0)\n\t"
               "v_readfirstlane_b32 %1, %0\n\ts_cmp_lt_u32 %1, %3\n\ts_cbranch_scc1 1b"
               : "=&v"(v), "=&s"(sv), "+s"(cnt), "+s"(target) : "v"(a) : "scc", "memory");
}
DEV u32 peek(u32 a) { return __hip_atomic_load((LAS u32*)(size_t)a, __ATOMIC_RELAXED, __HIP_MEMORY_SCOPE_WORKGROUP); }
DEV u32x4 ld16(rsrc_t rs, u32 off) { return __builtin_amdgcn_raw_buffer_load_b128(rs, off, 0, 16); }
DEV u32x2 ld8(rsrc_t rs, u32 off) { return __builtin_amdgcn_raw_buffer_load_b64(rs, off, 0, 16); }
DEV void st16(rsrc_t rs, u32 off, u32x4 v) { __builtin_amdgcn_raw_buffer_store_b128(v, rs, off, 0, 16); }
DEV void st8(rsrc_t rs, u32 off, u32x2 v) { __builtin_amdgcn_raw_buffer_store_b64(v, rs, off, 0, 16); }
constexpr u32 OOB = 0x7ff00000u;
constexpr u32 FMIX = 0x9E3779B9u;
DEV bool flag_ok(u32x2 v, u32 off, u32 tag) { return wave_all((off == OOB) | ((v.x == tag) & (v.y == (tag ^ FMIX)))); }
DEV void poll_flags(rsrc_t rs, u32 off, u32 tag) {
#pragma unroll 1
  for (int it = 0; it < SPIN_CAP; ++it) {
    asm volatile("" ::: "memory");
    if (flag_ok(ld8(rs, off), off, tag)) break;
    __builtin_amdgcn_s_sleep(1);
  }
  asm volatile("" ::: "memory");
}

#define TR(k) do {} while (0)
#define DUMP() do {} while (0)
#define STAMP(k) do {} while (0)
#define CSTAMP(k) do {} while (0)

#define SCAN_STEP(o, v, h)                                                                                             \
  asm("v_fma_f32 %0, %15, %10, %5\n\tv_fma_f32 %1, %16, %11, %6\n\tv_fma_f32 %2, %17, %12, %7\n\t"                   \
      "v_fma_f32 %3, %18, %13, %8\n\tv_fma_f32 %4, %19, %14, %9\n\t"                                                   \
      "v_fmac_f32_dpp %0, %10, %20 quad_perm:[1,0,3,2] row_mask:0xf bank_mask:0xf\n\t"                                 \
      "v_fmac_f32_dpp %1, %11, %21 quad_perm:[1,0,3,2] row_mask:0xf bank_mask:0xf\n\t"                                 \
      "v_fmac_f32_dpp %2, %12, %22 quad_perm:[1,0,3,2] row_mask:0xf bank_mask:0xf\n\t"                                 \
      "v_fmac_f32_dpp %3, %13, %23 quad_perm:[1,0,3,2] row_mask:0xf bank_mask:0xf\n\t"                                 \
      "v_fmac_f32_dpp %4, %14, %24 quad_perm:[1,0,3,2] row_mask:0xf bank_mask:0xf"                                     \
      : "=&v"(o[0]), "=&v"(o[1]), "=&v"(o[2]), "=&v"(o[3]), "=&v"(o[4])                                                \
      : "v"(v[0]), "v"(v[1]), "v"(v[2]), "v"(v[3]), "v"(v[4]), "v"(h[0]), "v"(h[1]), "v"(h[2]), "v"(h[3]), "v"(h[4]), \
        "v"(ma[0]), "v"(ma[1]), "v"(ma[2]), "v"(ma[3]), "v"(ma[4]), "v"(mb[0]), "v"(mb[1]), "v"(mb[2]), "v"(mb[3]),   \
        "v"(mb[4]))
DEV void scan_step(float (&o)[5], const float (&v)[5], const float (&h)[5], const float (&ma)[5], const float (&mb)[5]) {
  float t[5];
  SCAN_STEP(t, v, h);
#pragma unroll
  for (int i = 0; i < 5; ++i) o[i] = t[i];
}

struct Frag { u32x4 a0, a1, b0, b1, b2, b3, b4; };
#define MF(d, a, b) "v_mfma_f32_16x16x32_bf16 %" #d ", %" #a ", %" #b ", %" #d "\n\t"
#define RA(d, o) "ds_read_b128 %" #d ", %24 offset:" #o "\n\t"
#define RB(d, o) "ds_read_b128 %" #d ", %25 offset:" #o "\n\t"
#define WL(n) "s_waitcnt lgkmcnt(" #n ")\n\t"
#define G1_BODY(w1, w2, w3, w5, w7, w9)                                                                    \
  w1 MF(0, 17, 19) RA(10, 0) w2 MF(5, 18, 19) RB(12, 0) w3 MF(1, 17, 20) RA(11, 2048) MF(6, 18, 20) RB(13, 1024) \
  w5 MF(2, 17, 21) RB(14, 2048) MF(7, 18, 21) RB(15, 3072) w7 MF(3, 17, 22) RB(16, 4096) MF(8, 18, 22)    \
  w9 MF(4, 17, 23) MF(9, 18, 23)
#define G1_ACC(A) "+v"(A[0][0]), "+v"(A[0][1]), "+v"(A[0][2]), "+v"(A[0][3]), "+v"(A[0][4]), "+v"(A[1][0]), "+v"(A[1][1]), "+v"(A[1][2]), "+v"(A[1][3]), "+v"(A[1][4])
#define G1_OUT(N) "=&v"(N.a0), "=&v"(N.a1), "=&v"(N.b0), "=&v"(N.b1), "=&v"(N.b2), "=&v"(N.b3), "=&v"(N.b4)
#define G1_IN(C) "v"(C.a0), "v"(C.a1), "v"(C.b0), "v"(C.b1), "v"(C.b2), "v"(C.b3), "v"(C.b4)
template <bool WAITS> DEV void g1_half(f32x4 (&A)[2][5], Frag& N, const Frag& C, u32 pa, u32 pb) {
  if constexpr (WAITS)
    asm volatile(G1_BODY(WL(5), WL(5), WL(5), WL(6), WL(7), WL(7)) : G1_ACC(A), G1_OUT(N) : G1_IN(C), "v"(pa), "v"(pb) : "memory");
  else
    asm volatile(G1_BODY("", "", "", "", "", "") : G1_ACC(A), G1_OUT(N) : G1_IN(C), "v"(pa), "v"(pb) : "memory");
}
DEV u32 g1_half_peek(f32x4 (&A)[2][5], Frag& N, const Frag& C, u32 pa, u32 pb, u32 pf) {
  u32 t = pf;
  asm volatile("ds_read_b32 %0, %0" : "+v"(t) :: "memory");
  asm volatile(G1_BODY(WL(6), WL(6), WL(6), WL(7), WL(8), WL(8)) WL(7) : G1_ACC(A), G1_OUT(N) : G1_IN(C), "v"(pa), "v"(pb) : "memory");
  u32 r;
  asm volatile("v_readfirstlane_b32 %0, %1" : "=s"(r) : "v"(t) : "memory");
  return r;
}
DEV void g1_last(f32x4 (&A)[2][5], const Frag& C) {
  asm volatile(WL(5) MF(0, 10, 12) WL(4) MF(5, 11, 12) WL(3) MF(1, 10, 13) MF(6, 11, 13) WL(2) MF(2, 10, 14) MF(7, 11, 14)
               WL(1) MF(3, 10, 15) MF(8, 11, 15) WL(0) MF(4, 10, 16) MF(9, 11, 16) "s_nop 7"
               : G1_ACC(A) : G1_IN(C) : "memory");
}

struct CF { u32x4 f[5]; };
DEV u32 peek_issue(u32 a) { asm volatile("ds_read_b32 %0, %0" : "+v"(a) :: "memory"); return a; }
DEV u32 peek_get(u32 t) { u32 r; asm volatile("s_waitcnt lgkmcnt(5)\n\tv_readfirstlane_b32 %0, %1" : "=s"(r) : "v"(t) : "memory"); return r; }
#define M2(d, a) "v_mfma_f32_16x16x32_bf16 %" #d ", %" #a ", %15, %" #d "\n\t"
#define R2(d, o) "ds_read_b128 %" #d ", %16 offset:%17+" #o "\n\t"
template <int OFF, int WT> DEV void g2_half(f32x4 (&y)[5], CF& N, const CF& Cc, u32x4 h, u32 pa) {
  asm volatile("s_waitcnt lgkmcnt(%18)\n\t" M2(0, 10) R2(5, 0) "s_waitcnt lgkmcnt(%18)\n\t" M2(1, 11) R2(6, 2048) "s_waitcnt lgkmcnt(%18)\n\t" M2(2, 12) R2(7, 4096)
               "s_waitcnt lgkmcnt(%18)\n\t" M2(3, 13) R2(8, 6144) "s_waitcnt lgkmcnt(%18)\n\t" M2(4, 14) R2(9, 8192)
               : "+v"(y[0]), "+v"(y[1]), "+v"(y[2]), "+v"(y[3]), "+v"(y[4]), "=&v"(N.f[0]), "=&v"(N.f[1]), "=&v"(N.f[2]), "=&v"(N.f[3]), "=&v"(N.f[4])
               : "v"(Cc.f[0]), "v"(Cc.f[1]), "v"(Cc.f[2]), "v"(Cc.f[3]), "v"(Cc.f[4]), "v"(h), "v"(pa), "n"(OFF), "n"(WT) : "memory");
}
#define M3(d, a) "v_mfma_f32_16x16x32_bf16 %" #d ", %" #a ", %10, %" #d "\n\t"
template <bool LAST> DEV void g2_half0(f32x4 (&y)[5], const CF& Cc, u32x4 h) {
  if constexpr (LAST)
    asm volatile(WL(4) M3(0, 5) WL(3) M3(1, 6) WL(2) M3(2, 7) WL(1) M3(3, 8) WL(0) M3(4, 9) "s_nop 7"
                 : "+v"(y[0]), "+v"(y[1]), "+v"(y[2]), "+v"(y[3]), "+v"(y[4])
                 : "v"(Cc.f[0]), "v"(Cc.f[1]), "v"(Cc.f[2]), "v"(Cc.f[3]), "v"(Cc.f[4]), "v"(h) : "memory");
  else
    asm volatile(M3(0, 5) M3(1, 6) M3(2, 7) M3(3, 8) M3(4, 9)
                 : "+v"(y[0]), "+v"(y[1]), "+v"(y[2]), "+v"(y[3]), "+v"(y[4])
                 : "v"(Cc.f[0]), "v"(Cc.f[1]), "v"(Cc.f[2]), "v"(Cc.f[3]), "v"(Cc.f[4]), "v"(h) : "memory");
}

NOPK __global__ void __launch_bounds__(512, 2) fused_kernel(Params) {
  __shared__ __attribute__((aligned(16))) char smem[163840];
  asm volatile("" :: "v"((u32)(uintptr_t)(LAS char*)smem));
  constexpr u32 L0 = 0;
  const u64 kap = (u64)__builtin_amdgcn_kernarg_segment_ptr();
  u64x8 k0, k1; u64x4 k2;
  asm volatile("s_load_dwordx16 %0, %3, 0x0\n\ts_load_dwordx16 %1, %3, 0x40\n\ts_load_dwordx8 %2, %3, 0x80"
               : "=&s"(k0), "=&s"(k1), "=&s"(k2) : "s"(kap));
  lds_st<u32>(L0 + L_CNT + min(threadIdx.x & 63, 39u) * 4, 0u);
  asm volatile("s_waitcnt lgkmcnt(0)\n\ts_barrier" : "+s"(k0), "+s"(k1), "+s"(k2) :: "memory");
  struct { const u16 *x, *Lre, *Lim, *Bre, *Bim, *Cre, *Cim, *ldtA, *DA, *Are, *Aim, *sBre, *sBim, *sCre, *sCim, *ldtB, *DB, *gw; float* out; char* ws; }
  const P = {(const u16*)k0[0], (const u16*)k0[1], (const u16*)k0[2], (const u16*)k0[3], (const u16*)k0[4], (const u16*)k0[5], (const u16*)k0[6],
             (const u16*)k0[7], (const u16*)k1[0], (const u16*)k1[1], (const u16*)k1[2], (const u16*)k1[3], (const u16*)k1[4], (const u16*)k1[5],
             (const u16*)k1[6], (const u16*)k1[7], (const u16*)k2[0], (const u16*)k2[1], (float*)k2[2], (char*)k2[3]};
  const int tid = threadIdx.x, lane = tid & 63;
  const int w = __builtin_amdgcn_readfirstlane(tid >> 6);
  const int b = blockIdx.x, xcd = b & 7, r = b >> 3;
  const int c = 4 * (xcd >> 1) + (r >> 3), j = 8 * (xcd & 1) + (r & 7);
  const int ti = 4 * xcd + (r >> 3), dj = r & 7;
  const int i16 = lane & 15, g = lane >> 4;
  const rsrc_t rws = mksrd(P.ws, WS_END);
  const rsrc_t rx = mksrd(P.x, 2048 * 1280);
  const rsrc_t rCre = mksrd(P.Cre, 640 * 1280), rCim = mksrd(P.Cim, 640 * 1280);
  const rsrc_t rka = mksrd(seq_tab, 64);
  constexpr u32 SEQ = 0;
  const int kq = w & 3, kh = w >> 2;
  const int rot10 = ((r & 7) + 2 * (r >> 3) + 3 * xcd) % 10;
  u32 tag;
  STAMP(0);
  TR(0);
  const u32 cf0 = i16 * 128 + ((g ^ ((i16 >> 1) & 7)) << 4);
  const u32 ccf = L0 + C_CFULL;
  const u32 ho = WS_H + (u32)((4 * ti + kq) * 40 + 20 * kh) * 1024 + lane * 16;
  const u32 ima = L0 + (kh ? L_IMG1 : L_IMG0) + (16 * kq + i16) * 320 + (g ^ ((i16 >> 1) & 3)) * 16;
#define G2_REGS(name, n, at)                                                                                \
  CF name[2 * n];                                                                                           \
  { const u32 a0 = opaque(L0 + (at) + cf0), a1 = opaque(L0 + (at) + (cf0 ^ 64));                            \
    _Pragma("unroll") for (int k = 0; k < 2 * n; ++k)                                                       \
    _Pragma("unroll") for (int nt = 0; nt < 5; ++nt) name[k].f[nt] = lds_ld<u32x4>(((k & 1) ? a1 : a0) + (k >> 1) * 10240 + nt * 2048); \
    _Pragma("unroll") for (int k = 0; k < 2 * n; ++k)                                                       \
      asm volatile("s_waitcnt lgkmcnt(0)" : "+v"(name[k].f[0]), "+v"(name[k].f[1]), "+v"(name[k].f[2]), "+v"(name[k].f[3]), "+v"(name[k].f[4]) :: "memory"); }
#define G2_LOADH()                                                                                          \
  u32x4 hf_[20];                                                                                            \
  _Pragma("unroll") for (int ks = 0; ks < 20; ++ks) hf_[ks] = ld16(rws, ho + ks * 1024);                    \
  SB();
#define G2_INIT(at)                                                                                         \
  f32x4 y[5];                                                                                               \
  _Pragma("unroll") for (int nt = 0; nt < 5; ++nt) y[nt] = f32x4{0.f, 0.f, 0.f, 0.f};                       \
  CF P0, P1;                                                                                                \
  const u32 cab[2][2] = {{opaque(L0 + (at) + cf0), opaque(L0 + (at) + (cf0 ^ 64))},                         \
                         {opaque(L0 + (at) + 40960 + cf0), opaque(L0 + (at) + 40960 + (cf0 ^ 64))}};        \
  asm volatile("s_nop 1" : "+v"(y[0]), "+v"(y[1]), "+v"(y[2]), "+v"(y[3]), "+v"(y[4]));
#define G2_U(uu, slot, need)                                                                                \
  { const u32 t_ = peek_issue(ccf);                                                                         \
    g2_half<((slot) & 3) * 10240, 5>(y, P1, P0, hf_[2 * (uu)], cab[(slot) >> 2][1]);                        \
    if (RARE(peek_get(t_) < (need))) sw_wait(ccf, need);                                                    \
    g2_half<(((slot) + 1) & 3) * 10240, 4>(y, P0, P1, hf_[2 * (uu) + 1], cab[((slot) + 1) >> 2][0]); }
#define G2_UN(uu, slot)                                                                                     \
  g2_half<((slot) & 3) * 10240, 4>(y, P1, P0, hf_[2 * (uu)], cab[(slot) >> 2][1]);                          \
  g2_half<(((slot) + 1) & 3) * 10240, 4>(y, P0, P1, hf_[2 * (uu) + 1], cab[((slot) + 1) >> 2][0]);
#define G2_ULAST(uu, slot)                                                                                  \
  g2_half<((slot) & 3) * 10240, 4>(y, P1, P0, hf_[2 * (uu)], cab[(slot) >> 2][1]);                          \
  g2_half0<true>(y, P1, hf_[2 * (uu) + 1]);
#define G2_B()                                                                                              \
  G2_LOADH();                                                                                               \
  G2_INIT(L_B0);                                                                                            \
  _Pragma("unroll") for (int k = 0; k < 5; ++k) g2_half0<false>(y, cr[k], hf_[k]);                          \
  sw_wait4(L0 + C_FLUSH, 1);                                                                                \
  g2_half<0, 4>(y, P0, cr[5], hf_[5], cab[0][0]);                                                           \
  STAMP(9);                                                                                                 \
  G2_UN(3, 0); G2_UN(4, 1); G2_UN(5, 2); G2_UN(6, 3); G2_UN(7, 4); G2_UN(8, 5); G2_ULAST(9, 6);             \
  G2_B_IMAGE();
#define G2_B_IMAGE()                                                                                        \
  { STAMP(11);                                                                                              \
    sw_wait4(L0 + C_BAR, 4);                                                                                \
    const u32 ia = opaque(ima);                                                                             \
    _Pragma("unroll") for (int nt = 0; nt < 5; ++nt) lds_st<f32x4>(ia + nt * 64, y[nt]); }
  auto get_tag = [&]() INL { sw_wait(L0 + C_FULL, 1); tag = __builtin_amdgcn_readfirstlane(lds_ld<u32>(opaque(L0 + L_TAG))) + 1u; };

  const int pl = lane < 40 ? lane : 39;
  u32 pLre, pLim, pLdt;
  if (w < 4) {
    pLre = ((const GAS u16*)P.Lre)[40 * j + pl]; pLim = ((const GAS u16*)P.Lim)[40 * j + pl]; pLdt = ((const GAS u16*)P.ldtA)[40 * j + pl];
    asm volatile("" ::: "memory");
#pragma unroll
    for (int k = 0; k < 3; ++k) {
      const int qq = 64 * k + lane, t = (qq * 6554) >> 16, cc = qq - 10 * t;
      if (k < 2 || lane < 32) dma16(rx, (u32)(64 * ti + 16 * w + t) * 1280 + dj * 160 + cc * 16, 0, L0 + L_RAW + w * 2560 + k * 1024);
    }
    dma_mark(rka, SEQ, L0 + C_STRIP + 4 * w);
  }
  if (__builtin_expect(w == 7, 1)) {
      __builtin_amdgcn_s_setprio(3);
      const rsrc_t rBre = mksrd(P.Bre, 640 * 1280), rBim = mksrd((const char*)P.Bim - 1024, 640 * 1280 + 1024);
      const int q8 = lane >> 3;
      u32 vx[4], vb[10];
      {
        const u32 x0 = (u32)(128 * c + 8 * (q8 >> 2) + (q8 & 3)) * 1280 + (((lane & 7) ^ (q8 >> 1)) << 4);
        vx[0] = x0; vx[1] = (x0 ^ 64) + 16 * 1280 - 1024; vx[2] = x0 + 4 * 1280 - 2048; vx[3] = (x0 ^ 64) + 20 * 1280 - 3072;
        const u32 b0 = (u32)(40 * j + q8) * 1280 + (((lane & 7) ^ (q8 & 6)) << 4), b1 = (b0 ^ 16) + 1024;
#pragma unroll
        for (int k = 0; k < 10; ++k) vb[k] = (k < 5 ? b0 : b1) + 10240 * (k % 5) - 1024 * (k & 3);
      }
      u32 so = rot10 * 128;
      {
        u64 keep;
        asm volatile("s_mov_b32 m0, %1\n\ts_mov_b64 %0, exec\n\ts_mov_b64 exec, 1\n\tbuffer_load_dword off, %2, 0 sc1 lds\n\ts_mov_b64 exec, %0"
                     : "=&s"(keep) : "s"(L0 + L_TAG), "s"(rws) : "m0", "memory");
      }
      auto head = [&](u32 sl) INL {
        dmaq<0>(rx, rx, vx[0], vx[1], vx[2], vx[3], so, sl);
        dmaq<4096>(rx, rx, vx[0], vx[1], vx[2], vx[3], so + 40960, sl);
        dmaq<8192>(rx, rx, vx[0], vx[1], vx[2], vx[3], so + 81920, sl);
        dmaq<12288>(rx, rx, vx[0], vx[1], vx[2], vx[3], so + 122880, sl);
        dmaq<16384>(rBre, rBre, vb[0], vb[1], vb[2], vb[3], so, sl);
      };
      auto tail = [&](u32 sl) INL {
        dmaq<20480>(rBre, rBim, vb[4], vb[5], vb[6], vb[7], so, sl);
        dmad<24576>(rBim, vb[8], vb[9], so, sl);
        so = so == 1152 ? 0 : so + 128;
      };
#pragma unroll
      for (int s = 0; s < 10; ++s) {
        const u32 sb = L0 + ring(s);
        if (s == 5) sw_wait4(L0 + C_PRO, 1);
        if (s == 6 || s == 8) sw_wait4(L0 + C_FREE, s - 4);
        TR(4 + 4 * s);
        if (s >= 2) waitv<34>();
        TR(5 + 4 * s);
        head(sb);
        tail(sb);
        TR(6 + 4 * s);
        if (s == 0 && lane < 10) {
          dma16(mksrd(P.DA, 1280), lane * 16, dj * 160, L0 + L_DSK);
          dma16(mksrd(P.DB, 1280), lane * 16, dj * 160, L0 + L_DSK + 160);
          dma16(mksrd(P.gw, 1280), lane * 16, rot10 * 128, L0 + L_GW);
        }
        dma_mark(rka, SEQ + 4 * s, L0 + C_FULL);
        if (s == 0) __builtin_amdgcn_s_setprio(1);
      }
      STAMP(2);
      u32 vc[4];
#pragma unroll
      for (int k = 0; k < 4; ++k) vc[k] = (u32)(80 * dj + q8) * 1280 + (((lane & 7) ^ (lane >> 4) ^ (4 * (k & 1))) << 4) + 9216 * k;
      auto stageC = [&](int u, u32 dst) INL {
        const rsrc_t rs = (u & 1) ? rCim : rCre;
        const u32 so = (u32)(u >> 1) * 128;
        dma64v(rs, vc[0], vc[1], vc[2], vc[3], so, dst);
        dma64v(rs, vc[0], vc[1], vc[2], vc[3], so + 40960, dst + 4096);
        dma32v(rs, vc[0], vc[1], so + 81920, dst + 8192);
      };
      {
        u32 t0 = 0;
#pragma unroll
        for (int k = 0; k < 7; ++k) {
          const u32 ln = min(r * 400 + k * 64 + lane, r * 400 + 399);
          asm volatile("buffer_load_dword %0, %1, %2, 0 offen" : "+v"(t0) : "v"((ln >= 6400 ? ln - 6400 : ln) * 128), "s"(r >= 16 ? rCim : rCre) : "memory");
        }
        get_tag();
        asm volatile("s_waitcnt vmcnt(0)" : "+v"(t0) :: "memory");
        post(L0 + C_BPREP + 12, 1u);
      }
      sw_wait4(L0 + C_BPREP, 1);
      sw_wait4(L0 + C_BAR, 2);
      stageC(10, L0 + L_A0 + 5 * 10240); stageC(11, L0 + L_A0 + 6 * 10240); stageC(12, L0 + L_A0 + 7 * 10240);
      stageC(0, L0 + L_A0 + 3 * 10240); stageC(1, L0 + L_A0 + 4 * 10240);
      dma_mark(rka, SEQ, ccf);
      stageC(4, L0 + L_A0 + 2 * 10240);
      sw_wait(ccf, 1);
      G2_REGS(cr, 3, L_A0 + 5 * 10240);
      post(L0 + C_BDONE + 12, 1u);
      sw_wait4(L0 + C_BAR, 3);
      stageC(2, L0 + L_A0); stageC(3, L0 + L_A0 + 10240);
      dma_mark(rka, SEQ + 4, ccf);
      STAMP(3);
      sw_wait4(L0 + C_C0, 1);
      waitv<30>();
      stageC(5, L0 + L_A0 + 3 * 10240); stageC(6, L0 + L_A0 + 4 * 10240);
      dma_mark(rka, SEQ + 8, ccf);
      __builtin_amdgcn_s_setprio(0);
      poll_flags(rws, lane < 32 ? WS_FH + (u32)(32 * ti + lane) * 128 : OOB, tag);
      post(L0 + C_HOK, 1u);
      STAMP(8);
      G2_B();
  } else if (__builtin_expect(w < 4, 1)) {
    __builtin_amdgcn_s_setprio(2);
    f32x4 acc[2][5], accg[2] = {f32x4{0.f, 0.f, 0.f, 0.f}, f32x4{0.f, 0.f, 0.f, 0.f}};
#pragma unroll
    for (int m = 0; m < 2; ++m)
#pragma unroll
      for (int nt = 0; nt < 5; ++nt) acc[m][nt] = f32x4{0.f, 0.f, 0.f, 0.f};
    asm volatile("" : G1_ACC(acc));
    const u32 ax0 = opaque(L0 + (32 * w + i16) * 128 + ((g ^ ((i16 >> 1) & 7)) << 4));
    const u32 ab0 = opaque(L0 + 16384 + (40 * (i16 & 1) + (i16 >> 1)) * 128 + ((g ^ (2 * (i16 >> 2) + (i16 & 1))) << 4));
    const u32 ax1 = opaque(ax0 ^ 64), ab1 = opaque(ab0 ^ 64);
    const u32 cfull = opaque(L0 + C_FULL), cfree = opaque(L0 + C_FREE), myfree = opaque(L0 + C_FREE + 4 * w);
    const u32 xra = opaque(L0 + L_RAW + (32 * (g >> 1) + 8 * (lane & 3) + ((lane >> 2) & 3)) * 160 + 40 * w + 8 * (g & 1));
    const u32 xrq = opaque(L0 + L_RAW + (16 * w + i16) * 160 + 8 * g);
    const u32 xo = opaque(WS_XT + (u32)(20 * dj + 5 * w + (g & 1)) * 16384 + (8 * ti + 4 * (g >> 1)) * 64 + i16 * 16);
    const u32 xo2 = opaque((g & 1) ? OOB : xo + 65536);
    const u32 fxo_ = opaque(lane == 0 ? WS_FX + (u32)((ti * 8 + dj) * 4 + w) * 128 : OOB);
    const u32 tb = opaque(L0 + L_TAB + w * 3200 + pl * 16), tp = opaque(L0 + L_TAB + w * 3200 + (i16 >> 1) * 16), tg = opaque(tp + g * 640);
    const u32 pa0 = opaque(ax0 + L_A0), pb0 = opaque(ab0 + L_A0), pa1 = opaque(ax1 + L_A0);
    asm volatile("" : "+v"(pLre), "+v"(pLim), "+v"(pLdt));
    const float lre = bf2f(pLre), lim = bf2f(pLim);
    const float dtA = rbf(expf(bf2f(pLdt)));
    const float aA = dtA * lre, tA = dtA * lim;
    const f32x2 lam = cpowk(aA, tA, 1.f), l8 = cpowk(aA, tA, 8.f);
    f32x2 c2;
    {
      const float nr = lam.x - 1.f, ni = lam.y, den = 2.f * __builtin_amdgcn_rcpf(lre * lre + lim * lim);
      c2 = f32x2{(nr * lre + ni * lim) * den, (ni * lre - nr * lim) * den};
    }
    f32x2 zs[4], zh[4];
    zs[3] = cpowk(aA, tA, (float)(96 - 32 * w));
    zh[0] = cmul(cpowk(aA, tA, (float)(32 * w)), c2);
#pragma unroll
    for (int k = 1; k < 4; ++k) { zs[3 - k] = cmul(zs[4 - k], l8); zh[k] = cmul(zh[k - 1], l8); }
    const int sig = 4 * w + g;
    float lr[5], ls[5], sa[5], sb[5], ha[5], hb[5], ca[5], cb[5];
    {
#pragma unroll
      for (int k = 0; k < 4; ++k) lds_st<f32x4>(tb + k * 640, f32x4{zs[k].x, zs[k].y, zh[k].x, -zh[k].y});
      lds_st<f32x4>(tb + 4 * 640, f32x4{lam.x, lam.y, c2.x, -c2.y});
      const float sg = (lane & 1) ? 1.f : -1.f;
#pragma unroll
      for (int nt = 0; nt < 5; ++nt) {
        const f32x4 q1 = lds_ld<f32x4>(tp + 4 * 640 + nt * 128), q0 = lds_ld<f32x4>(tg + nt * 128);
        sa[nt] = q0.x; sb[nt] = sg * q0.y; ha[nt] = -sg * q0.z; hb[nt] = q0.w;
        lr[nt] = q1.x; ls[nt] = sg * q1.y; ca[nt] = -sg * q1.z; cb[nt] = q1.w;
        asm volatile("" : "+v"(sb[nt]), "+v"(ha[nt]), "+v"(ls[nt]), "+v"(ca[nt]));
      }
    }
    u32x2 xq[5];
    {
        sw_wait4(L0 + C_STRIP, 1);
        u32x2 lo[3], hi[3];
#pragma unroll
        for (int k = 0; k < 3; ++k) { lo[k] = __builtin_bit_cast(u32x2, lds_tr(xra + k * 16)); hi[k] = __builtin_bit_cast(u32x2, lds_tr(xra + k * 16 + 640)); }
#pragma unroll
        for (int nt = 0; nt < 5; ++nt) xq[nt] = lds_ld<u32x2>(xrq + 32 * nt);
        post(L0 + C_PRO + 4 * w, 1u);
#pragma unroll
        for (int k = 0; k < 3; ++k) st16(rws, k < 2 ? xo + k * 32768 : xo2, u32x4{lo[k].x, lo[k].y, hi[k].x, hi[k].y});
        asm volatile("s_waitcnt lgkmcnt(0)" : "+v"(xq[0]), "+v"(xq[1]), "+v"(xq[2]), "+v"(xq[3]), "+v"(xq[4]) :: "memory");
      }
    STAMP(1);
    Frag X, Y;
    u32x2 eda[5], edb[5];
    sw_wait(cfull, 1);
    X.a0 = lds_ld<u32x4>(pa0); X.b0 = lds_ld<u32x4>(pb0); X.a1 = lds_ld<u32x4>(pa0 + 2048); X.b1 = lds_ld<u32x4>(pb0 + 1024);
    X.b2 = lds_ld<u32x4>(pb0 + 2048); X.b3 = lds_ld<u32x4>(pb0 + 3072); X.b4 = lds_ld<u32x4>(pb0 + 4096);
    const u32 seed = lds_ld<u32>(opaque(L0 + L_TAG));
    const u32 dk = opaque(L0 + L_DSK + 8 * g);
#pragma unroll
    for (int nt = 0; nt < 5; ++nt) { eda[nt] = lds_ld<u32x2>(dk + 32 * nt); edb[nt] = lds_ld<u32x2>(dk + 160 + 32 * nt); asm volatile("" : "+v"(eda[nt]), "+v"(edb[nt])); }
    const u32 gwa = opaque(L0 + L_GW + g * 16);
    const u32x4 gw0 = lds_ld<u32x4>(gwa), gw1 = lds_ld<u32x4>(gwa + 64);
    accg[0] = mfma(lds_ld<u32x4>(pa1), gw1, mfma(X.a0, gw0, accg[0]));
    accg[1] = mfma(lds_ld<u32x4>(pa1 + 2048), gw1, mfma(X.a1, gw0, accg[1]));
    asm volatile("s_waitcnt lgkmcnt(0)" : "+v"(X.a0), "+v"(X.a1), "+v"(X.b0), "+v"(X.b1), "+v"(X.b2), "+v"(X.b3), "+v"(X.b4));
    tag = __builtin_amdgcn_readfirstlane(seed) + 1u;
    const u32x2 fxv = {tag, tag ^ FMIX};
    u32 so = L_A0;
#pragma unroll
    for (int s = 0; s < 4; s += 2) {
      u32 have = g1_half_peek(acc, Y, X, ax1 + so, ab1 + so, cfull);
      so = s == 2 ? 0 : so + STG;
      TR(4 + 4 * s);
      if (RARE(have < (u32)(s + 2))) sw_wait(cfull, s + 2);
      g1_half<true>(acc, X, Y, ax0 + so, ab0 + so);
      have = g1_half_peek(acc, Y, X, ax1 + so, ab1 + so, cfull);
      so += STG;
      post(myfree, s + 2);
      TR(5 + 4 * s);
      if (RARE(have < (u32)(s + 3))) sw_wait(cfull, s + 3);
      g1_half<true>(acc, X, Y, ax0 + so, ab0 + so);
      TR(6 + 4 * s);
    }
    waitv<0>();
    st8(rws, fxo_, fxv);
    {
      u32 have = g1_half_peek(acc, Y, X, ax1 + ring(4), ab1 + ring(4), cfull);
      if (RARE(have < 6u)) sw_wait(cfull, 6);
      g1_half<true>(acc, X, Y, ax0 + ring(5), ab0 + ring(5));
      have = g1_half_peek(acc, Y, X, ax1 + ring(5), ab1 + ring(5), cfull);
      if (RARE(have < 7u)) sw_wait(cfull, 7);
      g1_half<true>(acc, X, Y, ax0 + ring(6), ab0 + ring(6));
      TR(28);
      have = g1_half_peek(acc, Y, X, ax1 + ring(6), ab1 + ring(6), cfull);
      if (RARE(have < 8u)) sw_wait(cfull, 8);
      g1_half<true>(acc, X, Y, ax0 + ring(7), ab0 + ring(7));
      have = g1_half_peek(acc, Y, X, ax1 + ring(7), ab1 + ring(7), cfull);
      if (RARE(have < 9u)) sw_wait(cfull, 9);
      g1_half<true>(acc, X, Y, ax0 + ring(8), ab0 + ring(8));
      TR(30);
      have = g1_half_peek(acc, Y, X, ax1 + ring(8), ab1 + ring(8), cfull);
      if (RARE(have < 10u)) sw_wait(cfull, 10);
      TR(32);
      g1_half<true>(acc, X, Y, ax0 + ring(9), ab0 + ring(9));
      g1_half<true>(acc, Y, X, ax1 + ring(9), ab1 + ring(9));
      g1_last(acc, Y);
      post(L0 + C_G1 + 4 * w, 1u);
    }
    const u32x4 fr10 = peek4(L0 + C_G1);
    STAMP(2);
    TR(44);
    float v[8][5];
#pragma unroll
    for (int jj = 0; jj < 8; ++jj)
#pragma unroll
      for (int nt = 0; nt < 5; ++nt) v[jj][nt] = acc[jj >> 2][nt][jj & 3];
    float e[5];
#pragma unroll
    for (int nt = 0; nt < 5; ++nt) e[nt] = v[0][nt];
#pragma unroll
    for (int jj = 1; jj < 8; ++jj) scan_step(e, v[jj], e, lr, ls);
    {
      float z[5] = {0.f, 0.f, 0.f, 0.f, 0.f}, we[5];
      scan_step(we, z, e, sa, sb);
      auto s01 = __builtin_amdgcn_permlane32_swap(f2u(we[0]), f2u(we[1]), false, false);
      auto s23 = __builtin_amdgcn_permlane32_swap(f2u(we[2]), f2u(we[3]), false, false);
      auto s44 = __builtin_amdgcn_permlane32_swap(f2u(we[4]), f2u(we[4]), false, false);
      const float y01 = u2f(s01[0]) + u2f(s01[1]), y23 = u2f(s23[0]) + u2f(s23[1]), y4 = u2f(s44[0]) + u2f(s44[1]);
      auto q03 = __builtin_amdgcn_permlane16_swap(f2u(y01), f2u(y23), false, false);
      auto q44 = __builtin_amdgcn_permlane16_swap(f2u(y4), f2u(y4), false, false);
      if (RARE(min4(fr10) < 1u)) sw_wait4(L0 + C_G1, 1);
      const u32 wa = opaque(L0 + L_WS + w * 320 + i16 * 4);
      lds_st<float>(wa + (2 * (g & 1) + (g >> 1)) * 64, u2f(q03[0]) + u2f(q03[1]));
      lds_st<float>(wa + 256, u2f(q44[0]) + u2f(q44[1]));
      const u32 ra = opaque(L0 + L_ERAW + sig * 320 + i16 * 4);
#pragma unroll
      for (int nt = 0; nt < 5; ++nt) lds_st<float>(ra + nt * 64, e[nt]);
    }
    post(L0 + C_BAR + 4 * w, 1u);
    const u32 ap = opaque(L0 + L_WS + pl * 8), sgo = opaque(lane < 40 ? WS_SG + (u32)(c * 640 + 40 * j + lane) * 16 : OOB);
    sw_wait4(L0 + C_BAR, 1);
    if (w == 0) {
      const f32x2 se = (lds_ld<f32x2>(ap) + lds_ld<f32x2>(ap + 320)) + (lds_ld<f32x2>(ap + 640) + lds_ld<f32x2>(ap + 960));
      st16(rws, sgo, u32x4{f2u(se.x), tag, f2u(se.y), tag});
    }
    STAMP(3);
    const int nvalid = (c - w + 3) >> 2;
    const f32x2 lam4 = cpowk(aA, tA, 512.f);
    f32x2 lame = cpowk(aA, tA, (float)(128 * ((c - 1 - w) & 3)));
    asm volatile("" : "+v"(lame));
    if ((xcd & 1) ? ((r & 7) >= 2 && (r & 7) < 7) : (r & 7) < 5) {
      float gl = accg[0][0];
#pragma unroll
      for (int k = 1; k < 8; ++k) gl = (i16 == k) ? accg[k >> 2][k & 3] : gl;
      st8(rws, i16 < 8 ? WS_GG + (u32)(rot10 * 2048 + 128 * c + 32 * w + 8 * g + i16) * 8 : OOB, u32x2{f2u(gl), tag});
    }
    float pf[5];
    {
      u32 era = opaque(L0 + L_ERAW + pl * 8);
      f32x2 pp = {0.f, 0.f};
#pragma unroll 1
      for (int s2 = 0; s2 < w; ++s2, era += 1280) {
        const f32x2 e0 = lds_ld<f32x2>(era), e1 = lds_ld<f32x2>(era + 320), e2 = lds_ld<f32x2>(era + 640), e3 = lds_ld<f32x2>(era + 960);
        pp = cfma(cfma(cfma(cfma(pp, l8, e0), l8, e1), l8, e2), l8, e3);
      }
      const f32x2 e0 = lds_ld<f32x2>(era), e1 = lds_ld<f32x2>(era + 320), e2 = lds_ld<f32x2>(era + 640);
      const u32 ptb = L0 + L_PT + w * 1280;
      const u32 pw = opaque(ptb + pl * 8);
#pragma unroll
      for (int k = 0; k < 4; ++k) {
        const f32x2 q = cmul(pp, c2);
        lds_st<f32x2>(pw + k * 320, f32x2{q.x, -q.y});
        if (k < 3) pp = cfma(pp, l8, k == 0 ? e0 : k == 1 ? e1 : e2);
      }
      const u32 pr_ = opaque(ptb + g * 320 + i16 * 4);
#pragma unroll
      for (int nt = 0; nt < 5; ++nt) pf[nt] = lds_ld<float>(pr_ + nt * 64);
    }
    u32 hst[5];
    const u32 go = opaque(WS_GG + (u32)(g * 2048 + 64 * ti + 16 * kq + i16) * 8), go2 = opaque(g < 2 ? go + 8 * 2048 * 8 : OOB);
    const u32 hpr = opaque(L0 + L_HP + i16 * 16), htw = opaque(L0 + L_HT + w * 1280 + i16 * 80 + g * 16),
              htr = opaque(L0 + L_HT + w * 1280 + (2 * ((lane >> 2) & 3) + (g & 1)) * 80 + (g >> 1) * 32 + (lane & 3) * 8);
    {
      const u32 hb0 = WS_H + (u32)(8 * c + 2 * w + (g >> 1)) * 40960 + (g & 1) * 2048 + i16 * 16;
#pragma unroll
      for (int nt = 0; nt < 5; ++nt) {
        const int o = 5 * j + nt;
        hst[nt] = opaque(hb0 + (u32)((o >> 3) * 4096 + ((o >> 2) & 1) * 1024 + (o & 3) * 256));
      }
      u32 off[4];
      f32x2 mk[3];
#pragma unroll
      for (int k = 0; k < 4; ++k) off[k] = opaque((k < nvalid && lane < 40) ? WS_SG + (u32)((w + 4 * k) * 640 + 40 * j + lane) * 16 : OOB);
#pragma unroll
      for (int k = 0; k < 3; ++k) { mk[k] = k + 1 < nvalid ? lam4 : f32x2{1.f, 0.f}; asm volatile("" : "+v"(mk[k])); }
      const u32 hp = opaque(L0 + L_HP + min(lane, 40) * 32 + w * 4);
      u32x4 sv[4];
#pragma unroll
      for (int k = 0; k < 4; ++k) sv[k] = ld16(rws, off[k]);
      SB();
      const float z[5] = {0.f, 0.f, 0.f, 0.f, 0.f};
#pragma unroll
      for (int jj = 0; jj < 8; ++jj) scan_step(v[jj], z, v[jj], ca, cb);
#pragma unroll
      for (int nt = 0; nt < 5; ++nt) ls[nt] = -ls[nt];
      asm volatile("" : "+v"(v[7][4]), "+v"(ls[4]));
      SB();
#pragma unroll 1
      for (int it = 0; it < SPIN_CAP; ++it) {
        bool ok = true;
#pragma unroll
        for (int k = 0; k < 4; ++k) ok &= (off[k] == OOB) | ((sv[k].y == tag) & (sv[k].w == tag));
        if (wave_all(ok)) break;
        asm volatile("" ::: "memory");
#pragma unroll
        for (int k = 0; k < 4; ++k) sv[k] = ld16(rws, off[k]);
      }
      asm volatile("" ::: "memory");
      STAMP(4);
      f32x2 hsum = {u2f(sv[0].x), u2f(sv[0].z)};
#pragma unroll
      for (int k = 1; k < 4; ++k) hsum = cfma(hsum, mk[k - 1], f32x2{u2f(sv[k].x), u2f(sv[k].z)});
      hsum = cmul(hsum, lame);
      lds_st<float>(hp, hsum.x);
      lds_st<float>(hp + 16, hsum.y);
    }
    post(L0 + C_BAR + 4 * w, 2u);
    u32x2 g0 = ld8(rws, go), g1 = ld8(rws, go + 4 * 2048 * 8), g2 = ld8(rws, go2);
    SB();
    sw_wait4(L0 + C_BAR, 2);
    {
      float hin[5], h0[5];
#pragma unroll
      for (int nt = 0; nt < 5; ++nt) {
        const f32x4 q = lds_ld<f32x4>(hpr + nt * 256);
        hin[nt] = (q.x + q.y) + (q.z + q.w);
      }
      scan_step(h0, pf, hin, ha, hb);
      scan_step(v[0], v[0], h0, lr, ls);
#pragma unroll
      for (int jj = 1; jj < 8; ++jj) scan_step(v[jj], v[jj], v[jj - 1], lr, ls);
    }
    {
      const u32 wa = htw, ra = htr;
      u32x2 lo[5], hi[5];
#pragma unroll
      for (int nt = 0; nt < 5; ++nt) {
        lds_st<u32x4>(wa, u32x4{pk(v[0][nt], v[1][nt]), pk(v[2][nt], v[3][nt]), pk(v[4][nt], v[5][nt]), pk(v[6][nt], v[7][nt])});
        lo[nt] = __builtin_bit_cast(u32x2, lds_tr(ra));
        hi[nt] = __builtin_bit_cast(u32x2, lds_tr(ra + 640));
      }
      SB();
#pragma unroll
      for (int nt = 0; nt < 5; ++nt) st16(rws, hst[nt], u32x4{lo[nt].x, lo[nt].y, hi[nt].x, hi[nt].y});
    }
    post(L0 + C_BAR + 4 * w, 3u);
    STAMP(5);
#pragma unroll 1
    for (int it = 0; it < SPIN_CAP && RARE(!wave_all((g0.y == tag) & (g1.y == tag) & ((g2.y == tag) | (g >= 2)))); ++it) {
      __builtin_amdgcn_s_sleep(1);
      asm volatile("" ::: "memory");
      g0 = ld8(rws, go); g1 = ld8(rws, go + 4 * 2048 * 8); g2 = ld8(rws, go2);
    }
    float gate, ogate;
    {
      const float s3 = (u2f(g0.x) + u2f(g1.x)) + u2f(g2.x);
      auto sa_ = __builtin_amdgcn_permlane32_swap(f2u(s3), f2u(s3), false, false);
      const float s2 = u2f(sa_[0]) + u2f(sa_[1]);
      auto sb_ = __builtin_amdgcn_permlane16_swap(f2u(s2), f2u(s2), false, false);
      gate = rbf(__builtin_amdgcn_rcpf(1.f + __builtin_amdgcn_exp2f(-1.44269504088896f * rbf(u2f(sb_[0]) + u2f(sb_[1])))));
      ogate = rbf(1.f - gate);
      asm volatile("" : "+v"(gate), "+v"(ogate));
    }
    f32x4 base[5];
#pragma unroll
    for (int nt = 0; nt < 5; ++nt) {
#pragma unroll
      for (int e = 0; e < 2; ++e) {
        base[nt][2 * e] = bflo(xq[nt][e]) * __builtin_fmaf(gate, bflo(eda[nt][e]), ogate * bflo(edb[nt][e]));
        base[nt][2 * e + 1] = bfhi(xq[nt][e]) * __builtin_fmaf(gate, bfhi(eda[nt][e]), ogate * bfhi(edb[nt][e]));
      }
      asm volatile("" : "+v"(base[nt]));
    }
    waitv<0>();
    st8(rws, lane == 0 ? WS_FH + (u32)((c * 4 + w) * 16 + j) * 128 : OOB, u32x2{tag, tag ^ FMIX});
    STAMP(14);
    sw_wait(ccf, 1);
    G2_REGS(cr, 2, L_A0 + 3 * 10240);
    post(L0 + C_C0 + 4 * w, 1u);
    u32x4 held[5][5];
    {
      const u32 v0 = (u32)(80 * dj + 40 * (w & 1) + (lane >> 3)) * 1280 + (((lane & 7) ^ (lane >> 4) ^ (4 * (w & 1))) << 4), v1 = v0 ^ 64;
#pragma unroll
      for (int pr = 0; pr < 5; ++pr) {
        const int u = (w >> 1) ? (pr < 3 ? 14 + 2 * pr : pr == 3 ? 7 : 9) : (pr < 4 ? 13 + 2 * pr : 8);
#pragma unroll
        for (int k = 0; k < 5; ++k)
          held[pr][k] = __builtin_amdgcn_raw_buffer_load_b128((u & 1) ? rCim : rCre, (k & 1) ? v1 : v0, (u >> 1) * 128 + k * 10240, 0);
      }
      SB();
    }
    STAMP(15);
    sw_wait4(L0 + C_BDONE, 1);
    {
      const u32 fb = opaque(L0 + L_B0 + (w >> 1) * 10240 + (w & 1) * 5120 + lane * 16);
      const u32 f3 = opaque(L0 + ((w >> 1) ? L_A0 + 5 * 10240 : L_B0 + 6 * 10240) + (w & 1) * 5120 + lane * 16);
      const u32 f4 = opaque(L0 + L_A0 + (6 + (w >> 1)) * 10240 + (w & 1) * 5120 + lane * 16);
#pragma unroll
      for (int pr = 0; pr < 5; ++pr)
#pragma unroll
        for (int k = 0; k < 5; ++k) lds_st<u32x4>((pr < 3 ? fb + pr * 20480 : pr == 3 ? f3 : f4) + k * 1024, held[pr][k]);
      post(L0 + C_FLUSH + 4 * w, 1u);
    }
    sw_wait(L0 + C_HOK, 1);
    __builtin_amdgcn_s_setprio(0);
    STAMP(6);
    G2_LOADH();
    STAMP(7);
    G2_INIT(L_A0);

    g2_half0<false>(y, cr[0], hf_[0]); g2_half0<false>(y, cr[1], hf_[1]); g2_half0<false>(y, cr[2], hf_[2]);
    sw_wait(ccf, 3);
    sw_wait4(L0 + C_FLUSH, 1);
    g2_half<0, 4>(y, P0, cr[3], hf_[3], cab[0][0]);
    STAMP(8);
    G2_UN(2, 0); G2_UN(3, 1); G2_UN(4, 2); G2_UN(5, 3);
    STAMP(9);
    G2_UN(6, 4);
    lds_st<float>(opaque(L0 + L_GATE + (16 * w + i16) * 4), gate);
    post(L0 + C_BAR + 4 * w, 4u);
    waitl();
    const u32 bo = WS_BT + (u32)(80 * dj + (lane >> 2)) * 8192 + (64 * ti + 16 * w) * 4 + (lane & 3) * 16;
    u32x4 bv[5];
#pragma unroll
    for (int k = 0; k < 5; ++k) bv[k] = ld16(rws, bo + k * 16 * 8192);
    SB();
    G2_UN(7, 5);
    waitl();
    STAMP(10);
#pragma unroll 1
    for (int it = 0; it < SPIN_CAP; ++it) {
      bool ok = true;
#pragma unroll
      for (int k = 0; k < 5; ++k) ok &= (bv[k].y == tag) & (bv[k].w == tag);
      if (!RARE(!wave_all(ok))) break;
      __builtin_amdgcn_s_sleep(2);
      asm volatile("" ::: "memory");
#pragma unroll
      for (int k = 0; k < 5; ++k) bv[k] = ld16(rws, bo + k * 16 * 8192);
    }
    STAMP(11);
    sw_wait4(L0 + C_BAR, 4);
    {
      const u32 ba = opaque(L0 + L_BST + w * 2560 + lane * 8);
#pragma unroll
      for (int k = 0; k < 5; ++k) lds_st<u32x2>(ba + k * 512, u32x2{bv[k].x, bv[k].z});
      u32x2 yb[5];
#pragma unroll
      for (int nt = 0; nt < 5; ++nt) yb[nt] = __builtin_bit_cast(u32x2, lds_tr(ba + nt * 512));
      SB();
#pragma unroll
      for (int nt = 0; nt < 5; ++nt) {
        base[nt] += ogate * f32x4{bflo(yb[nt].x), bfhi(yb[nt].x), bflo(yb[nt].y), bfhi(yb[nt].y)};
        asm volatile("" : "+v"(base[nt]));
      }
    }
    G2_UN(8, 6); G2_ULAST(9, 7);
    {
      const u32 ia = opaque(ima);
#pragma unroll
      for (int nt = 0; nt < 5; ++nt) lds_st<f32x4>(ia + nt * 64, gate * y[nt] + base[nt]);
    }
  } else {
    const int pr = xcd * 16 + (r >> 1);
    const int nch = (r & 1) ? 2 : 3, ch = 5 * pr + ((r & 1) ? 3 : 0) + kq;
    const u32 slot = opaque(L0 + L_B0 + kq * BSLOT) ;
    f32x4 ya[8];
    if (__builtin_expect(kq < nch, 1)) {
      const GAS u32* pa = (const GAS u32*)P.Are + ch * 64 + lane;
      const u32 wAre = *pa, wAim = ((const GAS u32*)P.Aim)[ch * 64 + lane];
      const u32 wBre = ((const GAS u32*)P.sBre)[ch * 64 + lane], wBim = ((const GAS u32*)P.sBim)[ch * 64 + lane];
      const u32 wCre = ((const GAS u32*)P.sCre)[ch * 64 + lane], wCim = ((const GAS u32*)P.sCim)[ch * 64 + lane];
      const u32 wdt = ((const GAS u16*)P.ldtB)[ch];
      const float dt = rbf(expf(bf2f(wdt)));
      f32x4 ka[3] = {f32x4{0.f, 0.f, 0.f, 0.f}, f32x4{0.f, 0.f, 0.f, 0.f}, f32x4{0.f, 0.f, 0.f, 0.f}};
      const u32 tr = opaque(slot + i16 * TROW + g * 16);
      const u32 fxo = lane < 32 ? WS_FX + (u32)((lane * 8 + ch / 80) * 4 + (ch % 80) / 20) * 128 : OOB;
      u32 tv[56], tu[56];
#pragma unroll 1
      for (int rd = 0; rd < 2; ++rd) {
#pragma unroll
        for (int k = 0; k < 56; ++k) tu[k] = tv[k];
        const u32 sh = rd ? 0u : 16u;
        const float a = dt * bfhi(wAre << sh), t = dt * bfhi(wAim << sh);
        const f32x2 db = {dt * bfhi(wBre << sh), dt * bfhi(wBim << sh)}, cc = {bfhi(wCre << sh), bfhi(wCim << sh)};
        const f32x2 W = cmul(cc, db);
        const f32x2 z1 = cpowk(a, t, 1.f), z8 = cpowk(a, t, 8.f), z128 = cpowk(a, t, 128.f);
        {
          f32x2 cv = z8;
          tv[41] = pk(cv.x, cv.y);
#pragma unroll
          for (int jj = 2; jj < 16; ++jj) { cv = cmul(cv, z8); tv[40 + jj] = pk(cv.x, cv.y); }
        }
        {
          const f32x2 y1 = {z1.x, -z1.y}, yh = {z128.x, -z128.y};
          f32x2 R[8];
          R[0] = f32x2{W.x, -W.y};
#pragma unroll
          for (int k = 1; k < 8; ++k) R[k] = cmul(R[k - 1], y1);
#pragma unroll
          for (int k = 0; k < 8; ++k) tv[k] = pk(R[k].x, R[k].y);
#pragma unroll
          for (int k = 0; k < 8; ++k) { R[k] = cmul(R[k], yh); tv[8 + k] = pk(R[k].x, R[k].y); }
#pragma unroll
          for (int hh = 0; hh < 2; ++hh)
#pragma unroll
            for (int k = 0; k < 4; ++k) { R[2 * k] = cmul(R[2 * k], yh); tv[16 + 4 * hh + k] = pk(R[2 * k].x, R[2 * k].y); }
#pragma unroll
          for (int hh = 0; hh < 4; ++hh)
#pragma unroll
            for (int k = 0; k < 2; ++k) { R[4 * k] = cmul(R[4 * k], yh); tv[24 + 2 * hh + k] = pk(R[4 * k].x, R[4 * k].y); }
#pragma unroll
          for (int hh = 0; hh < 9; ++hh) { R[0] = cmul(R[0], yh); tv[32 + hh] = pk(R[0].x, R[0].y); }
        }
      }
      STAMP(1);
      get_tag();
      const u32 kc = slot + KC_OFF;
      const bool r1 = lane < 32;
      const u32 xa = opaque(kc - 4 + (r1 ? 16 * lane : 256 + 8 * lane)), xb = opaque(xa + (r1 ? 12 : 8));
      const u32 sA = opaque(r1 ? 0x05040706u : 0x03020504u), sB = opaque(r1 ? 0x05040706u : 0x05040504u), sC = opaque(r1 ? 0x03020504u : 0x01000302u),
                sD = opaque(r1 ? 0x03020504u : 0x05040504u), sE = opaque(lane <= 32 ? 0x03020504u : 0x05040504u);
      const u32 ka0 = opaque(kc + (128 * (g >> 1) + 8 * i16 + 4 * (g & 1)) * 2);
      const u32 ka1 = opaque(kc + (g < 2 ? 256 + 64 * g + 4 * i16 : 256 + 64 * g + 2 * i16) * 2), ka1b = opaque(ka1 + (g < 2 ? 4 : 64));
      const u32 ka2 = opaque(kc + (512 + 64 * min(g, 2) + i16) * 2);
      const u32 sbase = __builtin_amdgcn_readfirstlane(slot);
      const u32 uo = opaque(WS_XT + (u32)(ch >> 2) * 16384 + (ch & 3) * 16 + lane * 64), uw = opaque(slot + UPAD + lane * 16);
      sw_wait(L0 + C_FULL, 10);
      const u32x2 fx = ld8(rws, fxo);
      sw_wait4(L0 + C_G1, 1);
      STAMP(6);
#define TW(v, row) asm volatile("ds_write_addtid_b32 %0 offset:%1" :: "v"(v), "n"((row) * TROW) : "memory")
      asm volatile("s_mov_b32 m0, %0\n\ts_nop 0" :: "s"(sbase) : "m0", "memory");
      TW(0x00003f80u, 41);
      u32x4 uv[4];
#pragma unroll
      for (int rd = 0; rd < 2; ++rd) {
#pragma unroll
        for (int k = 0; k < 56; ++k) TW(rd ? tv[k] : tu[k], k < 41 ? k : k + 1);
        u32x4 fr[4][4];
#pragma unroll
        for (int ks = 0; ks < 4; ++ks)
#pragma unroll
          for (int m = 0; m < 4; ++m) fr[ks][m] = lds_ld<u32x4>(tr + (m == 0 ? 41 : 16 * (m - 1)) * TROW + ks * 64);
        SB();
#pragma unroll
        for (int ks = 0; ks < 4; ++ks)
#pragma unroll
          for (int m = 0; m < 3; ++m) ka[m] = mfma(fr[ks][m + 1], fr[ks][0], ka[m]);
        SB();
      }
      if (RARE(!flag_ok(fx, fxo, tag))) poll_flags(rws, fxo, tag);
      asm volatile("" ::: "memory");
#pragma unroll
      for (int k = 0; k < 4; ++k) uv[k] = ld16(rws, uo + k * 4096);
      SB();
#define UW(v, off) asm volatile("ds_write_addtid_b32 %0 offset:%1" :: "v"(v), "n"(off) : "memory")
#pragma unroll
      for (int cc = 0; cc < 4; ++cc) { UW(0u, cc * UCOPY); UW(0u, cc * UCOPY + 144); }
      STAMP(5);
      {
        const u32 q0 = pk(ka[2].x, ka[2].y), q1 = pk(ka[2].z, ka[2].w);
        if (g < 3) { lds_st<u16>(ka2, (u16)q0); lds_st<u16>(ka2 + 32, (u16)(q0 >> 16)); lds_st<u16>(ka2 + 64, (u16)q1); lds_st<u16>(ka2 + 96, (u16)(q1 >> 16)); }
        lds_st<u32x2>(ka0, u32x2{pk(ka[0].x, ka[0].y), pk(ka[0].z, ka[0].w)});
        lds_st<u32>(ka1, pk(ka[1].x, ka[1].y));
        lds_st<u32>(ka1b, pk(ka[1].z, ka[1].w));
        lds_st<u32>(kc - 4, 0u);
      }
#define PERM(hi, lo, sel) __builtin_amdgcn_perm(hi, lo, sel)
      {
        const u32 R0 = lds_ld<u32>(xa), R1 = lds_ld<u32>(xa + 4), R2 = lds_ld<u32>(xa + 8), R3 = lds_ld<u32>(xb), R4 = lds_ld<u32>(xb + 4);
        const u32 ea_ = opaque(kc + 768 + 4 * lane), eb_ = opaque(kc + 1024 + 2 * lane);
        const u32 E0 = lds_ld<u32>(ea_), E1 = lds_ld<u32>(ea_ + 4);
        const u32 f0 = lds_ld<u16>(eb_), f1 = lds_ld<u16>(eb_ + 2), f2 = lds_ld<u16>(eb_ + 128), f3 = lds_ld<u16>(eb_ + 130);
        UW(0u, KOFF); UW(0u, KOFF + KCOPY);
        const u32 k0 = opaque(slot + KOFF + KPAD + lane * 16);
        lds_st<u32x4>(k0 + KCOPY, u32x4{PERM(R4, R3, sA), PERM(R3, R2, sB), PERM(R2, R1, sA), PERM(R1, R0, sB)});
        lds_st<u32x4>(k0, u32x4{PERM(R4, R3, sC), PERM(R3, R2, sD), PERM(R2, R1, sC), PERM(R1, R0, sE)});
        const u32 e00 = PERM(E0, E0, 0x01000100u), e11 = PERM(E0, E0, 0x03020302u);
        lds_st<u32x4>(k0 + KCOPY + 1024, u32x4{PERM(E1, E1, 0x01000100u), e11, PERM(E0, E0, 0x01000302u), e00});
        lds_st<u32x4>(k0 + 1024, u32x4{PERM(E0, E1, 0x07060100u), e11, e00, e00});
#pragma unroll
        for (int rr = 0; rr < 2; ++rr) {
          const u32 e0 = rr ? f2 : f0, e1 = rr ? f3 : f1, t4 = (lane & 1) ? e1 : e0;
          const u32 c00 = e0 | (e0 << 16), c11 = e1 | (e1 << 16);
          lds_st<u32x4>(k0 + KCOPY + 2048 + 1024 * rr, u32x4{c11, e1 | (t4 << 16), c00, c00});
          lds_st<u32x4>(k0 + 2048 + 1024 * rr, u32x4{c11, t4 | (e0 << 16), c00, c00});
        }
      }
#pragma unroll
      for (int k = 0; k < 4; ++k) {
        lds_st<u32x4>(uw + k * 1024, uv[k]);
#pragma unroll
        for (int e = 0; e < 4; ++e) { lds_st<u32>(uw + k * 1024 + UCOPY + 4 + 4 * e, uv[k][e]); lds_st<u32>(uw + k * 1024 + 3 * UCOPY + 12 + 4 * e, uv[k][e]); }
        lds_st<u32x2>(uw + k * 1024 + 2 * UCOPY + 8, u32x2{uv[k].x, uv[k].y});
        lds_st<u32x2>(uw + k * 1024 + 2 * UCOPY + 16, u32x2{uv[k].z, uv[k].w});
      }
      post(L0 + C_BPREP + 4 * kq, 1u);
      STAMP(2);
#pragma unroll
      for (int A = 0; A < 8; ++A) ya[A] = f32x4{0.f, 0.f, 0.f, 0.f};
      const u32 ta = opaque(slot + KOFF + KPAD + (i16 & 1) * KCOPY + ((i16 >> 1) - g) * 16 - 64);
      const u32 ua = opaque(slot + (i16 & 3) * UCOPY + (i16 >> 2) * 128 + g * 16);
      u32x4 T[2][8], U[2][8];
#define CV_READ(S, st)                                                                                        \
      _Pragma("unroll") for (int D = 0; D < 8; ++D) {                                                         \
        T[S][D] = lds_ld<u32x4>(ta + ((st) >> 1) * 128 + D * 512 + (1 - ((st) & 1)) * 64);                   \
        U[S][D] = lds_ld<u32x4>(ua + UPAD - ((st) >> 1) * 128 + D * 512 + ((st) & 1) * 64);                  \
      }
      CV_READ(0, 0);
      __builtin_amdgcn_s_setprio(2);
#pragma unroll
      for (int st = 0; st < 8; ++st) {
        SB();
        if (st < 7) { CV_READ((st + 1) & 1, st + 1); }
#pragma unroll
        for (int S = 0; S < 8; ++S)
#pragma unroll
          for (int D = 0; D <= S; ++D) ya[S] = mfma(T[st & 1][D], U[st & 1][S - D], ya[S]);
        if (st < 7) {
#pragma unroll
          for (int k = 0; k < 16; ++k) {
            __builtin_amdgcn_sched_group_barrier(0x8, 2, 0);
            __builtin_amdgcn_sched_group_barrier(0x100, 1, 0);
          }
          __builtin_amdgcn_sched_group_barrier(0x8, 4, 0);
        }
      }
      SB();
      STAMP(3);
    } else {
      get_tag();
      sw_wait4(L0 + C_G1, 1);
      post(L0 + C_BPREP + 4 * kq, 1u);
#pragma unroll
      for (int A = 0; A < 8; ++A) ya[A] = f32x4{0.f, 0.f, 0.f, 0.f};
    }
    sw_wait(ccf, 1);
    G2_REGS(cr, 3, L_A0 + 5 * 10240);
    post(L0 + C_BDONE + 4 * kq, 1u);
    {
      const bool odd = lane & 1;
      const u32 bo = kq < nch ? WS_BT + (u32)ch * 8192 + (i16 >> 2) * 256 + g * 64 + (odd ? 0 : 32) + ((lane & 2) ? 0 : 16) : OOB;
#pragma unroll
      for (int A = 0; A < 8; ++A) {
        const u32 x0 = pk(ya[A].x, ya[A].y), x1 = pk(ya[A].z, ya[A].w);
        const u32 rv = __builtin_amdgcn_update_dpp(0u, odd ? x1 : x0, 0xB1, 0xf, 0xf, true);
        st16(rws, bo + A * 1024, u32x4{odd ? x0 : rv, tag, odd ? rv : x1, tag});
      }
    }
    sw_wait(L0 + C_HOK, 1);
    __builtin_amdgcn_s_setprio(0);
    STAMP(4);
    G2_B();
  }

  hwbar();
  STAMP(12);
  if (b == 255 && tid == 0) __hip_atomic_store((GAS u32*)P.ws, tag, __ATOMIC_RELAXED, __HIP_MEMORY_SCOPE_AGENT);
  {
    const int et = tid >> 3, es = tid & 7;
    const u32 ia = opaque(L0 + L_IMG0 + et * 320 + ((es & 4) | ((es ^ (et >> 1)) & 3)) * 16);
    const float gt = lds_ld<float>(opaque(L0 + L_GATE + et * 4));
    const rsrc_t ro = mksrd(P.out, 2048 * 2560);
    const u32 oo = (64 * ti + et) * 2560 + dj * 320 + es * 16;
    const f32x4 o0 = lds_ld<f32x4>(ia) + gt * lds_ld<f32x4>(ia + (L_IMG1 - L_IMG0));
    const f32x4 o1 = lds_ld<f32x4>(ia + 128) + gt * lds_ld<f32x4>(ia + 128 + (L_IMG1 - L_IMG0));
    const f32x4 o2 = lds_ld<f32x4>(ia + 256) + gt * lds_ld<f32x4>(ia + 256 + (L_IMG1 - L_IMG0));
    st16(ro, oo, __builtin_bit_cast(u32x4, o0));
    st16(ro, oo + 128, __builtin_bit_cast(u32x4, o1));
    st16(ro, es < 4 ? oo + 256 : OOB, __builtin_bit_cast(u32x4, o2));
  }
  DUMP();
  STAMP(13);
}

extern "C" void kernel_launch(void* const* d_in, const int* in_sizes, int n_in, void* d_out, int out_size, void* d_ws,
                              size_t ws_size, hipStream_t stream) {
  Params p;
  p.x = (const u16*)d_in[0];
  p.Lre = (const u16*)d_in[1];
  p.Lim = (const u16*)d_in[2];
  p.Bre = (const u16*)d_in[3];
  p.Bim = (const u16*)d_in[4];
  p.Cre = (const u16*)d_in[5];
  p.Cim = (const u16*)d_in[6];
  p.ldtA = (const u16*)d_in[7];
  p.DA = (const u16*)d_in[8];
  p.Are = (const u16*)d_in[10];
  p.Aim = (const u16*)d_in[11];
  p.sBre = (const u16*)d_in[12];
  p.sBim = (const u16*)d_in[13];
  p.sCre = (const u16*)d_in[14];
  p.sCim = (const u16*)d_in[15];
  p.ldtB = (const u16*)d_in[16];
  p.DB = (const u16*)d_in[17];
  p.gw = (const u16*)d_in[18];
  p.out = (float*)d_out;
  p.ws = (char*)d_ws;
  fused_kernel<<<256, 512, 0, stream>>>(p);
}
```

```cpp
#include <hip/hip_runtime.h>
#include <stdint.h>

typedef unsigned short u16;
typedef unsigned int u32;
typedef unsigned long long u64;
typedef __bf16 bf16x8 __attribute__((ext_vector_type(8)));
typedef short s16x4 __attribute__((ext_vector_type(4)));
typedef float f32x4 __attribute__((ext_vector_type(4)));
typedef float f32x2 __attribute__((ext_vector_type(2)));
typedef u32 u32x4 __attribute__((ext_vector_type(4)));
typedef u32 u32x2 __attribute__((ext_vector_type(2)));
typedef u64 u64x8 __attribute__((ext_vector_type(8)));
typedef u64 u64x4 __attribute__((ext_vector_type(4)));
typedef __bf16 bf16x2_t __attribute__((ext_vector_type(2)));
typedef __amdgpu_buffer_rsrc_t rsrc_t;
#define LAS __attribute__((address_space(3)))
#define GAS __attribute__((address_space(1)))
#define DEV __device__ __forceinline__
#define INL __attribute__((always_inline))
#if defined(__HIP_DEVICE_COMPILE__)
#define NOPK __attribute__((target("no-packed-fp32-ops")))
#else
#define NOPK
#endif

constexpr u32 WS_E = 0;
constexpr u32 WS_FH = 4096;
constexpr u32 WS_FX = WS_FH + 1024 * 128;
constexpr u32 WS_SG = WS_FX + 1024 * 128;
constexpr u32 WS_GG = WS_SG + 16 * 640 * 16;
constexpr u32 WS_XT = WS_GG + 10 * 2048 * 8;
constexpr u32 WS_BT = WS_XT + 640 * 2048 * 2;
constexpr u32 WS_H = WS_BT + 640 * 1024 * 8;
constexpr u32 WS_END = WS_H + 128 * 40 * 1024;
constexpr u32 WS_DIAG = (WS_END + 4095) & ~4095u;

constexpr u32 STG = 26624;
constexpr u32 L_A0 = 81920;
constexpr u32 L_B0 = 0;
__attribute__((always_inline)) constexpr u32 ring(int s) { return s % 6 < 3 ? L_A0 + (s % 6) * STG : (s % 6 - 3) * STG; }
constexpr u32 BSLOT = 27008;
constexpr u32 L_CNT = 81024;
constexpr u32 C_FREE = L_CNT, C_PRO = L_CNT + 16, C_BAR = L_CNT + 32, C_BDONE = L_CNT + 48, C_C0 = L_CNT + 64, C_BPREP = L_CNT + 80,
              C_STRIP = L_CNT + 96, C_FULL = L_CNT + 112, C_CFULL = L_CNT + 116, C_HOK = L_CNT + 120,
              C_G1 = L_CNT + 128, C_FLUSH = L_CNT + 144, L_TAG = L_CNT + 160;
constexpr u32 L_GATE = L_CNT + 256;
constexpr u32 L_DSK = L_A0 + 3 * STG;
constexpr u32 L_GW = L_DSK + 320;
constexpr u32 L_RAW = ring(5);
constexpr u32 L_TAB = L_RAW + 10240;
constexpr u32 L_ERAW = L_A0;
constexpr u32 L_WS = L_A0 + 5120;
constexpr u32 L_HP = L_A0 + 6400;
constexpr u32 L_PT = L_A0 + 7936;
constexpr u32 L_HT = L_A0 + 13056;
constexpr u32 L_BST = L_A0;
constexpr u32 L_IMG0 = L_A0 + 10240, L_IMG1 = L_A0 + 30720;
constexpr u32 UCOPY = 4640, UPAD = 384, KOFF = 4 * UCOPY, KCOPY = 4224, KPAD = 112;
constexpr u32 KC_OFF = 16640;
constexpr u32 TROW = 288;

constexpr int SPIN_CAP = 1 << 16;

static __device__ const __attribute__((aligned(64))) u32 seq_tab[16] = {1, 2, 3, 4, 5, 6, 7, 8, 9, 10, 11, 12, 13, 14, 15, 16};

template <bool B> struct BoolC { static constexpr bool value = B; };

struct Params {
  const u16 *x, *Lre, *Lim, *Bre, *Bim, *Cre, *Cim, *ldtA, *DA;
  const u16 *Are, *Aim, *sBre, *sBim, *sCre, *sCim, *ldtB, *DB, *gw;
  float* out;
  char* ws;
};

DEV bool wave_all(bool p) { return __builtin_amdgcn_ballot_w64(!p) == 0; }
DEV float bf2f(u32 v) { return __builtin_bit_cast(float, v << 16); }
DEV float bflo(u32 v) { return __builtin_bit_cast(float, v << 16); }
DEV float bfhi(u32 v) { return __builtin_bit_cast(float, v & 0xffff0000u); }
DEV u32 pk(float a, float b) { bf16x2_t v = {(__bf16)a, (__bf16)b}; return __builtin_bit_cast(u32, v); }
DEV float rbf(float a) { return bflo(pk(a, a)); }
DEV u32 f2u(float a) { return __builtin_bit_cast(u32, a); }
DEV float u2f(u32 a) { return __builtin_bit_cast(float, a); }
DEV float swp(float x) { return u2f(__builtin_amdgcn_update_dpp(0, f2u(x), 0xB1, 0xf, 0xf, true)); }
DEV f32x2 rot(f32x2 z) { return f32x2{-z.y, z.x}; }
DEV f32x2 cmul(f32x2 c, f32x2 z, f32x2 zr) { return __builtin_elementwise_fma(f32x2{c.y, c.y}, zr, f32x2{c.x, c.x} * z); }
DEV f32x2 cmul(f32x2 c, f32x2 z) { return cmul(c, z, rot(z)); }
DEV f32x2 cfma(f32x2 c, f32x2 z, f32x2 e) {
  return f32x2{__builtin_fmaf(-c.y, z.y, __builtin_fmaf(c.x, z.x, e.x)), __builtin_fmaf(c.y, z.x, __builtin_fmaf(c.x, z.y, e.y))};
}
DEV f32x2 cpowk(float a, float th, float k) {
  const float L2E = 1.44269504088896f, IH = 0.15915494f, IL = 6.4206382e-9f;
  float m = __builtin_amdgcn_exp2f(k * a * L2E);
  float x = k * th;
  float hi = x * IH;
  float lo = __builtin_fmaf(x, IL, __builtin_fmaf(x, IH, -hi));
  float r = __builtin_amdgcn_fractf(hi) + lo;
  return f32x2{m * __builtin_amdgcn_cosf(r), m * __builtin_amdgcn_sinf(r)};
}
template <typename T> DEV T lds_ld(u32 a) { typedef T __attribute__((may_alias)) TA; return *(const LAS TA*)(size_t)a; }
template <typename T> DEV void lds_st(u32 a, T v) { typedef T __attribute__((may_alias)) TA; *(LAS TA*)(size_t)a = v; }
DEV s16x4 lds_tr(u32 a) { return __builtin_amdgcn_ds_read_tr16_b64_v4i16((LAS s16x4*)(size_t)a); }
DEV f32x4 mfma(u32x4 a, u32x4 b, f32x4 c) {
  return __builtin_amdgcn_mfma_f32_16x16x32_bf16(__builtin_bit_cast(bf16x8, a), __builtin_bit_cast(bf16x8, b), c, 0, 0, 0);
}
DEV rsrc_t mksrd(const void* p, u32 n) { return __builtin_amdgcn_make_buffer_rsrc((void*)p, (short)0, (int)n, 0x00020000); }
DEV void dma16(rsrc_t rs, u32 voff, u32 soff, u32 lds) {
  asm volatile("s_mov_b32 m0, %0\n\ts_nop 0\n\tbuffer_load_dwordx4 %1, %2, %3 offen lds"
               :: "s"(lds), "v"(voff), "s"(rs), "s"(soff) : "m0", "memory");
}
DEV void dma64v(rsrc_t rs, u32 v0, u32 v1, u32 v2, u32 v3, u32 so, u32 lds) {
  asm volatile("s_mov_b32 m0, %0\n\ts_nop 0\n\tbuffer_load_dwordx4 %1, %5, %6 offen lds\n\t"
               "buffer_load_dwordx4 %2, %5, %6 offen offset:1024 lds\n\tbuffer_load_dwordx4 %3, %5, %6 offen offset:2048 lds\n\t"
               "buffer_load_dwordx4 %4, %5, %6 offen offset:3072 lds"
               :: "s"(lds), "v"(v0), "v"(v1), "v"(v2), "v"(v3), "s"(rs), "s"(so) : "m0", "memory");
}
DEV void dma32v(rsrc_t rs, u32 v0, u32 v1, u32 so, u32 lds) {
  asm volatile("s_mov_b32 m0, %0\n\ts_nop 0\n\tbuffer_load_dwordx4 %1, %3, %4 offen lds\n\t"
               "buffer_load_dwordx4 %2, %3, %4 offen offset:1024 lds"
               :: "s"(lds), "v"(v0), "v"(v1), "s"(rs), "s"(so) : "m0", "memory");
}
template <int OFF> DEV void dmaq(rsrc_t r0, rsrc_t r1, u32 v0, u32 v1, u32 v2, u32 v3, u32 so, u32 slot) {
  asm volatile("s_add_u32 m0, %0, %8\n\ts_nop 0\n\tbuffer_load_dwordx4 %1, %5, %7 offen lds\n\t"
               "buffer_load_dwordx4 %2, %6, %7 offen offset:1024 lds\n\tbuffer_load_dwordx4 %3, %6, %7 offen offset:2048 lds\n\t"
               "buffer_load_dwordx4 %4, %6, %7 offen offset:3072 lds"
               :: "s"(slot), "v"(v0), "v"(v1), "v"(v2), "v"(v3), "s"(r0), "s"(r1), "s"(so), "n"(OFF) : "m0", "scc", "memory");
}
template <int OFF> DEV void dmad(rsrc_t r0, u32 v0, u32 v1, u32 so, u32 slot) {
  asm volatile("s_add_u32 m0, %0, %5\n\ts_nop 0\n\tbuffer_load_dwordx4 %1, %3, %4 offen lds\n\t"
               "buffer_load_dwordx4 %2, %3, %4 offen offset:1024 lds"
               :: "s"(slot), "v"(v0), "v"(v1), "s"(r0), "s"(so), "n"(OFF) : "m0", "scc", "memory");
}
DEV void dma_mark(rsrc_t rs, u32 so, u32 lds) {
  u64 keep;
  asm volatile("s_mov_b32 m0, %1\n\ts_mov_b64 %0, exec\n\ts_mov_b64 exec, 1\n\tbuffer_load_dword off, %2, %3 lds\n\ts_mov_b64 exec, %0"
               : "=&s"(keep) : "s"(lds), "s"(rs), "s"(so) : "m0", "memory");
}
DEV u32 opaque(u32 v) { asm volatile("" : "+v"(v)); return v; }
#define SB() __builtin_amdgcn_sched_barrier(0)
#define RARE(x) __builtin_expect(!!(x), 0)
template <int N> DEV void waitv() { asm volatile("s_waitcnt vmcnt(%0)" :: "n"(N) : "memory"); }
DEV void waitl() { asm volatile("s_waitcnt lgkmcnt(0)" ::: "memory"); }
DEV void hwbar() { waitl(); __builtin_amdgcn_s_barrier(); asm volatile("" ::: "memory"); }
DEV void post(u32 a, u32 v) {
  asm volatile("" ::: "memory");
  lds_st<u32>(a, v);
  asm volatile("" ::: "memory");
}
DEV u32 min4(u32x4 v) { return (u32)__builtin_amdgcn_readfirstlane(min(min(v.x, v.y), min(v.z, v.w))); }
DEV u32x4 peek4(u32 a) { asm volatile("" ::: "memory"); return lds_ld<u32x4>(a); }
DEV void sw_wait4(u32 a, u32 target) {
#pragma unroll 1
  for (u32 it = 0; it < (1u << 22) && min4(peek4(a)) < target; ++it) {}
  asm volatile("" ::: "memory");
}
DEV void sw_wait(u32 a, u32 target) {
  u32 v, sv, cnt = 1u << 22;
  asm volatile("1:\n\tds_read_b32 %0, %4\n\ts_sub_u32 %2, %2, 1\n\ts_cselect_b32 %3, 0, %3\n\ts_waitcnt lgkmcnt(0)\n\t"
               "v_readfirstlane_b32 %1, %0\n\ts_cmp_lt_u32 %1, %3\n\ts_cbranch_scc1 1b"
               : "=&v"(v), "=&s"(sv), "+s"(cnt), "+s"(target) : "v"(a) : "scc", "memory");
}
DEV u32 peek(u32 a) { return __hip_atomic_load((LAS u32*)(size_t)a, __ATOMIC_RELAXED, __HIP_MEMORY_SCOPE_WORKGROUP); }
DEV u32x4 ld16(rsrc_t rs, u32 off) { return __builtin_amdgcn_raw_buffer_load_b128(rs, off, 0, 16); }
DEV u32x2 ld8(rsrc_t rs, u32 off) { return __builtin_amdgcn_raw_buffer_load_b64(rs, off, 0, 16); }
DEV void st16(rsrc_t rs, u32 off, u32x4 v) { __builtin_amdgcn_raw_buffer_store_b128(v, rs, off, 0, 16); }
DEV void st8(rsrc_t rs, u32 off, u32x2 v) { __builtin_amdgcn_raw_buffer_store_b64(v, rs, off, 0, 16); }
constexpr u32 OOB = 0x7ff00000u;
constexpr u32 FMIX = 0x9E3779B9u;
DEV bool flag_ok(u32x2 v, u32 off, u32 tag) { return wave_all((off == OOB) | ((v.x == tag) & (v.y == (tag ^ FMIX)))); }
DEV void poll_flags(rsrc_t rs, u32 off, u32 tag) {
#pragma unroll 1
  for (int it = 0; it < SPIN_CAP; ++it) {
    asm volatile("" ::: "memory");
    if (flag_ok(ld8(rs, off), off, tag)) break;
    __builtin_amdgcn_s_sleep(1);
  }
  asm volatile("" ::: "memory");
}

#define TR(k) do {} while (0)
#define DUMP() do {} while (0)
#define STAMP(k) do {} while (0)
#define CSTAMP(k) do {} while (0)

#define SCAN_STEP(o, v, h)                                                                                             \
  asm("v_fma_f32 %0, %15, %10, %5\n\tv_fma_f32 %1, %16, %11, %6\n\tv_fma_f32 %2, %17, %12, %7\n\t"                   \
      "v_fma_f32 %3, %18, %13, %8\n\tv_fma_f32 %4, %19, %14, %9\n\t"                                                   \
      "v_fmac_f32_dpp %0, %10, %20 quad_perm:[1,0,3,2] row_mask:0xf bank_mask:0xf\n\t"                                 \
      "v_fmac_f32_dpp %1, %11, %21 quad_perm:[1,0,3,2] row_mask:0xf bank_mask:0xf\n\t"                                 \
      "v_fmac_f32_dpp %2, %12, %22 quad_perm:[1,0,3,2] row_mask:0xf bank_mask:0xf\n\t"                                 \
      "v_fmac_f32_dpp %3, %13, %23 quad_perm:[1,0,3,2] row_mask:0xf bank_mask:0xf\n\t"                                 \
      "v_fmac_f32_dpp %4, %14, %24 quad_perm:[1,0,3,2] row_mask:0xf bank_mask:0xf"                                     \
      : "=&v"(o[0]), "=&v"(o[1]), "=&v"(o[2]), "=&v"(o[3]), "=&v"(o[4])                                                \
      : "v"(v[0]), "v"(v[1]), "v"(v[2]), "v"(v[3]), "v"(v[4]), "v"(h[0]), "v"(h[1]), "v"(h[2]), "v"(h[3]), "v"(h[4]), \
        "v"(ma[0]), "v"(ma[1]), "v"(ma[2]), "v"(ma[3]), "v"(ma[4]), "v"(mb[0]), "v"(mb[1]), "v"(mb[2]), "v"(mb[3]),   \
        "v"(mb[4]))
DEV void scan_step(float (&o)[5], const float (&v)[5], const float (&h)[5], const float (&ma)[5], const float (&mb)[5]) {
  float t[5];
  SCAN_STEP(t, v, h);
#pragma unroll
  for (int i = 0; i < 5; ++i) o[i] = t[i];
}

struct Frag { u32x4 a0, a1, b0, b1, b2, b3, b4; };
#define MF(d, a, b) "v_mfma_f32_16x16x32_bf16 %" #d ", %" #a ", %" #b ", %" #d "\n\t"
#define RA(d, o) "ds_read_b128 %" #d ", %24 offset:" #o "\n\t"
#define RB(d, o) "ds_read_b128 %" #d ", %25 offset:" #o "\n\t"
#define WL(n) "s_waitcnt lgkmcnt(" #n ")\n\t"
#define G1_BODY(w1, w2, w3, w5, w7, w9)                                                                    \
  w1 MF(0, 17, 19) RA(10, 0) w2 MF(5, 18, 19) RB(12, 0) w3 MF(1, 17, 20) RA(11, 2048) MF(6, 18, 20) RB(13, 1024) \
  w5 MF(2, 17, 21) RB(14, 2048) MF(7, 18, 21) RB(15, 3072) w7 MF(3, 17, 22) RB(16, 4096) MF(8, 18, 22)    \
  w9 MF(4, 17, 23) MF(9, 18, 23)
#define G1_ACC(A) "+v"(A[0][0]), "+v"(A[0][1]), "+v"(A[0][2]), "+v"(A[0][3]), "+v"(A[0][4]), "+v"(A[1][0]), "+v"(A[1][1]), "+v"(A[1][2]), "+v"(A[1][3]), "+v"(A[1][4])
#define G1_OUT(N) "=&v"(N.a0), "=&v"(N.a1), "=&v"(N.b0), "=&v"(N.b1), "=&v"(N.b2), "=&v"(N.b3), "=&v"(N.b4)
#define G1_IN(C) "v"(C.a0), "v"(C.a1), "v"(C.b0), "v"(C.b1), "v"(C.b2), "v"(C.b3), "v"(C.b4)
template <bool WAITS> DEV void g1_half(f32x4 (&A)[2][5], Frag& N, const Frag& C, u32 pa, u32 pb) {
  if constexpr (WAITS)
    asm volatile(G1_BODY(WL(5), WL(5), WL(5), WL(6), WL(7), WL(7)) : G1_ACC(A), G1_OUT(N) : G1_IN(C), "v"(pa), "v"(pb) : "memory");
  else
    asm volatile(G1_BODY("", "", "", "", "", "") : G1_ACC(A), G1_OUT(N) : G1_IN(C), "v"(pa), "v"(pb) : "memory");
}
DEV u32 g1_half_peek(f32x4 (&A)[2][5], Frag& N, const Frag& C, u32 pa, u32 pb, u32 pf) {
  u32 t = pf;
  asm volatile("ds_read_b32 %0, %0" : "+v"(t) :: "memory");
  asm volatile(G1_BODY(WL(6), WL(6), WL(6), WL(7), WL(8), WL(8)) WL(7) : G1_ACC(A), G1_OUT(N) : G1_IN(C), "v"(pa), "v"(pb) : "memory");
  u32 r;
  asm volatile("v_readfirstlane_b32 %0, %1" : "=s"(r) : "v"(t) : "memory");
  return r;
}
DEV void g1_last(f32x4 (&A)[2][5], const Frag& C) {
  asm volatile(WL(5) MF(0, 10, 12) WL(4) MF(5, 11, 12) WL(3) MF(1, 10, 13) MF(6, 11, 13) WL(2) MF(2, 10, 14) MF(7, 11, 14)
               WL(1) MF(3, 10, 15) MF(8, 11, 15) WL(0) MF(4, 10, 16) MF(9, 11, 16) "s_nop 7"
               : G1_ACC(A) : G1_IN(C) : "memory");
}

struct CF { u32x4 f[5]; };
DEV u32 peek_issue(u32 a) { asm volatile("ds_read_b32 %0, %0" : "+v"(a) :: "memory"); return a; }
DEV u32 peek_get(u32 t) { u32 r; asm volatile("s_waitcnt lgkmcnt(5)\n\tv_readfirstlane_b32 %0, %1" : "=s"(r) : "v"(t) : "memory"); return r; }
#define M2(d, a) "v_mfma_f32_16x16x32_bf16 %" #d ", %" #a ", %15, %" #d "\n\t"
#define R2(d, o) "ds_read_b128 %" #d ", %16 offset:%17+" #o "\n\t"
template <int OFF, int WT> DEV void g2_half(f32x4 (&y)[5], CF& N, const CF& Cc, u32x4 h, u32 pa) {
  asm volatile("s_waitcnt lgkmcnt(%18)\n\t" M2(0, 10) R2(5, 0) "s_waitcnt lgkmcnt(%18)\n\t" M2(1, 11) R2(6, 2048) "s_waitcnt lgkmcnt(%18)\n\t" M2(2, 12) R2(7, 4096)
               "s_waitcnt lgkmcnt(%18)\n\t" M2(3, 13) R2(8, 6144) "s_waitcnt lgkmcnt(%18)\n\t" M2(4, 14) R2(9, 8192)
               : "+v"(y[0]), "+v"(y[1]), "+v"(y[2]), "+v"(y[3]), "+v"(y[4]), "=&v"(N.f[0]), "=&v"(N.f[1]), "=&v"(N.f[2]), "=&v"(N.f[3]), "=&v"(N.f[4])
               : "v"(Cc.f[0]), "v"(Cc.f[1]), "v"(Cc.f[2]), "v"(Cc.f[3]), "v"(Cc.f[4]), "v"(h), "v"(pa), "n"(OFF), "n"(WT) : "memory");
}
#define M3(d, a) "v_mfma_f32_16x16x32_bf16 %" #d ", %" #a ", %10, %" #d "\n\t"
template <bool LAST> DEV void g2_half0(f32x4 (&y)[5], const CF& Cc, u32x4 h) {
  if constexpr (LAST)
    asm volatile(WL(4) M3(0, 5) WL(3) M3(1, 6) WL(2) M3(2, 7) WL(1) M3(3, 8) WL(0) M3(4, 9) "s_nop 7"
                 : "+v"(y[0]), "+v"(y[1]), "+v"(y[2]), "+v"(y[3]), "+v"(y[4])
                 : "v"(Cc.f[0]), "v"(Cc.f[1]), "v"(Cc.f[2]), "v"(Cc.f[3]), "v"(Cc.f[4]), "v"(h) : "memory");
  else
    asm volatile(M3(0, 5) M3(1, 6) M3(2, 7) M3(3, 8) M3(4, 9)
                 : "+v"(y[0]), "+v"(y[1]), "+v"(y[2]), "+v"(y[3]), "+v"(y[4])
                 : "v"(Cc.f[0]), "v"(Cc.f[1]), "v"(Cc.f[2]), "v"(Cc.f[3]), "v"(Cc.f[4]), "v"(h) : "memory");
}

NOPK __global__ void __launch_bounds__(512, 2) fused_kernel(Params) {
  __shared__ __attribute__((aligned(16))) char smem[163840];
  asm volatile("" :: "v"((u32)(uintptr_t)(LAS char*)smem));
  constexpr u32 L0 = 0;
  const u64 kap = (u64)__builtin_amdgcn_kernarg_segment_ptr();
  u64x8 k0, k1; u64x4 k2;
  asm volatile("s_load_dwordx16 %0, %3, 0x0\n\ts_load_dwordx16 %1, %3, 0x40\n\ts_load_dwordx8 %2, %3, 0x80"
               : "=&s"(k0), "=&s"(k1), "=&s"(k2) : "s"(kap));
  lds_st<u32>(L0 + L_CNT + min(threadIdx.x & 63, 39u) * 4, 0u);
  asm volatile("s_waitcnt lgkmcnt(0)\n\ts_barrier" : "+s"(k0), "+s"(k1), "+s"(k2) :: "memory");
  struct { const u16 *x, *Lre, *Lim, *Bre, *Bim, *Cre, *Cim, *ldtA, *DA, *Are, *Aim, *sBre, *sBim, *sCre, *sCim, *ldtB, *DB, *gw; float* out; char* ws; }
  const P = {(const u16*)k0[0], (const u16*)k0[1], (const u16*)k0[2], (const u16*)k0[3], (const u16*)k0[4], (const u16*)k0[5], (const u16*)k0[6],
             (const u16*)k0[7], (const u16*)k1[0], (const u16*)k1[1], (const u16*)k1[2], (const u16*)k1[3], (const u16*)k1[4], (const u16*)k1[5],
             (const u16*)k1[6], (const u16*)k1[7], (const u16*)k2[0], (const u16*)k2[1], (float*)k2[2], (char*)k2[3]};
  const int tid = threadIdx.x, lane = tid & 63;
  const int w = __builtin_amdgcn_readfirstlane(tid >> 6);
  const int b = blockIdx.x, xcd = b & 7, r = b >> 3;
  const int c = 4 * (xcd >> 1) + (r >> 3), j = 8 * (xcd & 1) + (r & 7);
  const int ti = 4 * xcd + (r >> 3), dj = r & 7;
  const int i16 = lane & 15, g = lane >> 4;
  const rsrc_t rws = mksrd(P.ws, WS_END);
  const rsrc_t rx = mksrd(P.x, 2048 * 1280);
  const rsrc_t rCre = mksrd(P.Cre, 640 * 1280), rCim = mksrd(P.Cim, 640 * 1280);
  const rsrc_t rka = mksrd(seq_tab, 64);
  constexpr u32 SEQ = 0;
  const int kq = w & 3, kh = w >> 2;
  const int rot10 = ((r & 7) + 2 * (r >> 3) + 3 * xcd) % 10;
  u32 tag;
  STAMP(0);
  TR(0);
  const u32 cf0 = i16 * 128 + ((g ^ ((i16 >> 1) & 7)) << 4);
  const u32 ccf = L0 + C_CFULL;
  const u32 ho = WS_H + (u32)((4 * ti + kq) * 40 + 20 * kh) * 1024 + lane * 16;
  const u32 ima = L0 + (kh ? L_IMG1 : L_IMG0) + (16 * kq + i16) * 320 + (g ^ ((i16 >> 1) & 3)) * 16;
#define G2_REGS(name, n, at)                                                                                \
  CF name[2 * n];                                                                                           \
  { const u32 a0 = opaque(L0 + (at) + cf0), a1 = opaque(L0 + (at) + (cf0 ^ 64));                            \
    _Pragma("unroll") for (int k = 0; k < 2 * n; ++k)                                                       \
    _Pragma("unroll") for (int nt = 0; nt < 5; ++nt) name[k].f[nt] = lds_ld<u32x4>(((k & 1) ? a1 : a0) + (k >> 1) * 10240 + nt * 2048); \
    _Pragma("unroll") for (int k = 0; k < 2 * n; ++k)                                                       \
      asm volatile("s_waitcnt lgkmcnt(0)" : "+v"(name[k].f[0]), "+v"(name[k].f[1]), "+v"(name[k].f[2]), "+v"(name[k].f[3]), "+v"(name[k].f[4]) :: "memory"); }
#define G2_LOADH()                                                                                          \
  u32x4 hf_[20];                                                                                            \
  _Pragma("unroll") for (int ks = 0; ks < 20; ++ks) hf_[ks] = ld16(rws, ho + ks * 1024);                    \
  SB();
#define G2_INIT(at)                                                                                         \
  f32x4 y[5];                                                                                               \
  _Pragma("unroll") for (int nt = 0; nt < 5; ++nt) y[nt] = f32x4{0.f, 0.f, 0.f, 0.f};                       \
  CF P0, P1;                                                                                                \
  const u32 cab[2][2] = {{opaque(L0 + (at) + cf0), opaque(L0 + (at) + (cf0 ^ 64))},                         \
                         {opaque(L0 + (at) + 40960 + cf0), opaque(L0 + (at) + 40960 + (cf0 ^ 64))}};        \
  asm volatile("s_nop 1" : "+v"(y[0]), "+v"(y[1]), "+v"(y[2]), "+v"(y[3]), "+v"(y[4]));
#define G2_U(uu, slot, need)                                                                                \
  { const u32 t_ = peek_issue(ccf);                                                                         \
    g2_half<((slot) & 3) * 10240, 5>(y, P1, P0, hf_[2 * (uu)], cab[(slot) >> 2][1]);                        \
    if (RARE(peek_get(t_) < (need))) sw_wait(ccf, need);                                                    \
    g2_half<(((slot) + 1) & 3) * 10240, 4>(y, P0, P1, hf_[2 * (uu) + 1], cab[((slot) + 1) >> 2][0]); }
#define G2_UN(uu, slot)                                                                                     \
  g2_half<((slot) & 3) * 10240, 4>(y, P1, P0, hf_[2 * (uu)], cab[(slot) >> 2][1]);                          \
  g2_half<(((slot) + 1) & 3) * 10240, 4>(y, P0, P1, hf_[2 * (uu) + 1], cab[((slot) + 1) >> 2][0]);
#define G2_ULAST(uu, slot)                                                                                  \
  g2_half<((slot) & 3) * 10240, 4>(y, P1, P0, hf_[2 * (uu)], cab[(slot) >> 2][1]);                          \
  g2_half0<true>(y, P1, hf_[2 * (uu) + 1]);
#define G2_B()                                                                                              \
  G2_LOADH();                                                                                               \
  G2_INIT(L_B0);                                                                                            \
  _Pragma("unroll") for (int k = 0; k < 5; ++k) g2_half0<false>(y, cr[k], hf_[k]);                          \
  sw_wait4(L0 + C_FLUSH, 1);                                                                                \
  g2_half<0, 4>(y, P0, cr[5], hf_[5], cab[0][0]);                                                           \
  STAMP(9);                                                                                                 \
  G2_UN(3, 0); G2_UN(4, 1); G2_UN(5, 2); G2_UN(6, 3); G2_UN(7, 4); G2_UN(8, 5); G2_ULAST(9, 6);             \
  G2_B_IMAGE();
#define G2_B_IMAGE()                                                                                        \
  { STAMP(11);                                                                                              \
    sw_wait4(L0 + C_BAR, 4);                                                                                \
    const u32 ia = opaque(ima);                                                                             \
    _Pragma("unroll") for (int nt = 0; nt < 5; ++nt) lds_st<f32x4>(ia + nt * 64, y[nt]); }
  auto get_tag = [&]() INL { sw_wait(L0 + C_FULL, 1); tag = __builtin_amdgcn_readfirstlane(lds_ld<u32>(opaque(L0 + L_TAG))) + 1u; };

  const int pl = lane < 40 ? lane : 39;
  u32 pfv = 0;
  if (w == 5) {
    u64 cb; u32 cn;
    asm volatile("s_getpc_b64 %0\n.Lfk_head:\n\ts_max_i32 %1, .Lfk_tail-.Lfk_head, 0" : "=s"(cb), "=s"(cn) :: "scc");
    asm volatile("buffer_load_dword %0, %1, %2, 0 offen" : "+v"(pfv) : "v"(lane < 16 ? (u32)(r * 16 + lane) * 128 : OOB), "s"(mksrd((const void*)cb, cn)) : "memory");
  }
  u32 pLre, pLim, pLdt;
  if (w < 4) {
    pLre = ((const GAS u16*)P.Lre)[40 * j + pl]; pLim = ((const GAS u16*)P.Lim)[40 * j + pl]; pLdt = ((const GAS u16*)P.ldtA)[40 * j + pl];
    asm volatile("" ::: "memory");
#pragma unroll
    for (int k = 0; k < 3; ++k) {
      const int qq = 64 * k + lane, t = (qq * 6554) >> 16, cc = qq - 10 * t;
      if (k < 2 || lane < 32) dma16(rx, (u32)(64 * ti + 16 * w + t) * 1280 + dj * 160 + cc * 16, 0, L0 + L_RAW + w * 2560 + k * 1024);
    }
    dma_mark(rka, SEQ, L0 + C_STRIP + 4 * w);
  }
  if (__builtin_expect(w == 7, 1)) {
      __builtin_amdgcn_s_setprio(3);
      const rsrc_t rBre = mksrd(P.Bre, 640 * 1280), rBim = mksrd((const char*)P.Bim - 1024, 640 * 1280 + 1024);
      const int q8 = lane >> 3;
      u32 vx[4], vb[10];
      {
        const u32 x0 = (u32)(128 * c + 8 * (q8 >> 2) + (q8 & 3)) * 1280 + (((lane & 7) ^ (q8 >> 1)) << 4);
        vx[0] = x0; vx[1] = (x0 ^ 64) + 16 * 1280 - 1024; vx[2] = x0 + 4 * 1280 - 2048; vx[3] = (x0 ^ 64) + 20 * 1280 - 3072;
        const u32 b0 = (u32)(40 * j + q8) * 1280 + (((lane & 7) ^ (q8 & 6)) << 4), b1 = (b0 ^ 16) + 1024;
#pragma unroll
        for (int k = 0; k < 10; ++k) vb[k] = (k < 5 ? b0 : b1) + 10240 * (k % 5) - 1024 * (k & 3);
      }
      u32 so = rot10 * 128;
      {
        u64 keep;
        asm volatile("s_mov_b32 m0, %1\n\ts_mov_b64 %0, exec\n\ts_mov_b64 exec, 1\n\tbuffer_load_dword off, %2, 0 sc1 lds\n\ts_mov_b64 exec, %0"
                     : "=&s"(keep) : "s"(L0 + L_TAG), "s"(rws) : "m0", "memory");
      }
      auto head = [&](u32 sl) INL {
        dmaq<0>(rx, rx, vx[0], vx[1], vx[2], vx[3], so, sl);
        dmaq<4096>(rx, rx, vx[0], vx[1], vx[2], vx[3], so + 40960, sl);
        dmaq<8192>(rx, rx, vx[0], vx[1], vx[2], vx[3], so + 81920, sl);
        dmaq<12288>(rx, rx, vx[0], vx[1], vx[2], vx[3], so + 122880, sl);
        dmaq<16384>(rBre, rBre, vb[0], vb[1], vb[2], vb[3], so, sl);
      };
      auto tail = [&](u32 sl) INL {
        dmaq<20480>(rBre, rBim, vb[4], vb[5], vb[6], vb[7], so, sl);
        dmad<24576>(rBim, vb[8], vb[9], so, sl);
        so = so == 1152 ? 0 : so + 128;
      };
#pragma unroll
      for (int s = 0; s < 10; ++s) {
        const u32 sb = L0 + ring(s);
        if (s == 5) sw_wait4(L0 + C_PRO, 1);
        if (s == 6 || s == 8) sw_wait4(L0 + C_FREE, s - 4);
        TR(4 + 4 * s);
        if (s >= 2) waitv<34>();
        TR(5 + 4 * s);
        head(sb);
        tail(sb);
        TR(6 + 4 * s);
        if (s == 0 && lane < 10) {
          dma16(mksrd(P.DA, 1280), lane * 16, dj * 160, L0 + L_DSK);
          dma16(mksrd(P.DB, 1280), lane * 16, dj * 160, L0 + L_DSK + 160);
          dma16(mksrd(P.gw, 1280), lane * 16, rot10 * 128, L0 + L_GW);
        }
        dma_mark(rka, SEQ + 4 * s, L0 + C_FULL);
        if (s == 0) __builtin_amdgcn_s_setprio(1);
      }
      STAMP(2);
      u32 vc[4];
#pragma unroll
      for (int k = 0; k < 4; ++k) vc[k] = (u32)(80 * dj + q8) * 1280 + (((lane & 7) ^ (lane >> 4) ^ (4 * (k & 1))) << 4) + 9216 * k;
      auto stageC = [&](int u, u32 dst) INL {
        const rsrc_t rs = (u & 1) ? rCim : rCre;
        const u32 so = (u32)(u >> 1) * 128;
        dma64v(rs, vc[0], vc[1], vc[2], vc[3], so, dst);
        dma64v(rs, vc[0], vc[1], vc[2], vc[3], so + 40960, dst + 4096);
        dma32v(rs, vc[0], vc[1], so + 81920, dst + 8192);
      };
      {
        u32 t0 = 0;
#pragma unroll
        for (int k = 0; k < 7; ++k) {
          const u32 ln = min(r * 400 + k * 64 + lane, r * 400 + 399);
          asm volatile("buffer_load_dword %0, %1, %2, 0 offen" : "+v"(t0) : "v"((ln >= 6400 ? ln - 6400 : ln) * 128), "s"(r >= 16 ? rCim : rCre) : "memory");
        }
        get_tag();
        asm volatile("s_waitcnt vmcnt(0)" : "+v"(t0) :: "memory");
        post(L0 + C_BPREP + 12, 1u);
      }
      sw_wait4(L0 + C_BPREP, 1);
      sw_wait4(L0 + C_BAR, 2);
      stageC(10, L0 + L_A0 + 5 * 10240); stageC(11, L0 + L_A0 + 6 * 10240); stageC(12, L0 + L_A0 + 7 * 10240);
      stageC(0, L0 + L_A0 + 3 * 10240); stageC(1, L0 + L_A0 + 4 * 10240);
      dma_mark(rka, SEQ, ccf);
      stageC(4, L0 + L_A0 + 2 * 10240);
      sw_wait(ccf, 1);
      G2_REGS(cr, 3, L_A0 + 5 * 10240);
      post(L0 + C_BDONE + 12, 1u);
      sw_wait4(L0 + C_BAR, 3);
      stageC(2, L0 + L_A0); stageC(3, L0 + L_A0 + 10240);
      dma_mark(rka, SEQ + 4, ccf);
      STAMP(3);
      sw_wait4(L0 + C_C0, 1);
      waitv<30>();
      stageC(5, L0 + L_A0 + 3 * 10240); stageC(6, L0 + L_A0 + 4 * 10240);
      dma_mark(rka, SEQ + 8, ccf);
      poll_flags(rws, lane < 32 ? WS_FH + (u32)(32 * ti + lane) * 128 : OOB, tag);
      post(L0 + C_HOK, 1u);
      __builtin_amdgcn_s_setprio(0);
      STAMP(8);
      G2_B();
  } else if (__builtin_expect(w < 4, 1)) {
    __builtin_amdgcn_s_setprio(2);
    f32x4 acc[2][5], accg[2] = {f32x4{0.f, 0.f, 0.f, 0.f}, f32x4{0.f, 0.f, 0.f, 0.f}};
#pragma unroll
    for (int m = 0; m < 2; ++m)
#pragma unroll
      for (int nt = 0; nt < 5; ++nt) acc[m][nt] = f32x4{0.f, 0.f, 0.f, 0.f};
    asm volatile("" : G1_ACC(acc));
    const u32 ax0 = opaque(L0 + (32 * w + i16) * 128 + ((g ^ ((i16 >> 1) & 7)) << 4));
    const u32 ab0 = opaque(L0 + 16384 + (40 * (i16 & 1) + (i16 >> 1)) * 128 + ((g ^ (2 * (i16 >> 2) + (i16 & 1))) << 4));
    const u32 ax1 = opaque(ax0 ^ 64), ab1 = opaque(ab0 ^ 64);
    const u32 cfull = opaque(L0 + C_FULL), cfree = opaque(L0 + C_FREE), myfree = opaque(L0 + C_FREE + 4 * w);
    const u32 xra = opaque(L0 + L_RAW + (32 * (g >> 1) + 8 * (lane & 3) + ((lane >> 2) & 3)) * 160 + 40 * w + 8 * (g & 1));
    const u32 xrq = opaque(L0 + L_RAW + (16 * w + i16) * 160 + 8 * g);
    const u32 xo = opaque(WS_XT + (u32)(20 * dj + 5 * w + (g & 1)) * 16384 + (8 * ti + 4 * (g >> 1)) * 64 + i16 * 16);
    const u32 xo2 = opaque((g & 1) ? OOB : xo + 65536);
    const u32 fxo_ = opaque(lane == 0 ? WS_FX + (u32)((ti * 8 + dj) * 4 + w) * 128 : OOB);
    const u32 tb = opaque(L0 + L_TAB + w * 3200 + pl * 16), tp = opaque(L0 + L_TAB + w * 3200 + (i16 >> 1) * 16), tg = opaque(tp + g * 640);
    const u32 pa0 = opaque(ax0 + L_A0), pb0 = opaque(ab0 + L_A0), pa1 = opaque(ax1 + L_A0);
    asm volatile("" : "+v"(pLre), "+v"(pLim), "+v"(pLdt));
    const float lre = bf2f(pLre), lim = bf2f(pLim);
    const float dtA = rbf(expf(bf2f(pLdt)));
    const float aA = dtA * lre, tA = dtA * lim;
    const f32x2 lam = cpowk(aA, tA, 1.f), l8 = cpowk(aA, tA, 8.f);
    f32x2 c2;
    {
      const float nr = lam.x - 1.f, ni = lam.y, den = 2.f * __builtin_amdgcn_rcpf(lre * lre + lim * lim);
      c2 = f32x2{(nr * lre + ni * lim) * den, (ni * lre - nr * lim) * den};
    }
    f32x2 zs[4], zh[4];
    zs[3] = cpowk(aA, tA, (float)(96 - 32 * w));
    zh[0] = cmul(cpowk(aA, tA, (float)(32 * w)), c2);
#pragma unroll
    for (int k = 1; k < 4; ++k) { zs[3 - k] = cmul(zs[4 - k], l8); zh[k] = cmul(zh[k - 1], l8); }
    const int sig = 4 * w + g;
    float lr[5], ls[5], sa[5], sb[5], ha[5], hb[5], ca[5], cb[5];
    {
#pragma unroll
      for (int k = 0; k < 4; ++k) lds_st<f32x4>(tb + k * 640, f32x4{zs[k].x, zs[k].y, zh[k].x, -zh[k].y});
      lds_st<f32x4>(tb + 4 * 640, f32x4{lam.x, lam.y, c2.x, -c2.y});
      const float sg = (lane & 1) ? 1.f : -1.f;
#pragma unroll
      for (int nt = 0; nt < 5; ++nt) {
        const f32x4 q1 = lds_ld<f32x4>(tp + 4 * 640 + nt * 128), q0 = lds_ld<f32x4>(tg + nt * 128);
        sa[nt] = q0.x; sb[nt] = sg * q0.y; ha[nt] = -sg * q0.z; hb[nt] = q0.w;
        lr[nt] = q1.x; ls[nt] = sg * q1.y; ca[nt] = -sg * q1.z; cb[nt] = q1.w;
        asm volatile("" : "+v"(sb[nt]), "+v"(ha[nt]), "+v"(ls[nt]), "+v"(ca[nt]));
      }
    }
    u32x2 xq[5];
    {
        sw_wait4(L0 + C_STRIP, 1);
        u32x2 lo[3], hi[3];
#pragma unroll
        for (int k = 0; k < 3; ++k) { lo[k] = __builtin_bit_cast(u32x2, lds_tr(xra + k * 16)); hi[k] = __builtin_bit_cast(u32x2, lds_tr(xra + k * 16 + 640)); }
#pragma unroll
        for (int nt = 0; nt < 5; ++nt) xq[nt] = lds_ld<u32x2>(xrq + 32 * nt);
        post(L0 + C_PRO + 4 * w, 1u);
#pragma unroll
        for (int k = 0; k < 3; ++k) st16(rws, k < 2 ? xo + k * 32768 : xo2, u32x4{lo[k].x, lo[k].y, hi[k].x, hi[k].y});
        asm volatile("s_waitcnt lgkmcnt(0)" : "+v"(xq[0]), "+v"(xq[1]), "+v"(xq[2]), "+v"(xq[3]), "+v"(xq[4]) :: "memory");
      }
    STAMP(1);
    Frag X, Y;
    u32x2 eda[5], edb[5];
    sw_wait(cfull, 1);
    X.a0 = lds_ld<u32x4>(pa0); X.b0 = lds_ld<u32x4>(pb0); X.a1 = lds_ld<u32x4>(pa0 + 2048); X.b1 = lds_ld<u32x4>(pb0 + 1024);
    X.b2 = lds_ld<u32x4>(pb0 + 2048); X.b3 = lds_ld<u32x4>(pb0 + 3072); X.b4 = lds_ld<u32x4>(pb0 + 4096);
    const u32 seed = lds_ld<u32>(opaque(L0 + L_TAG));
    const u32 dk = opaque(L0 + L_DSK + 8 * g);
#pragma unroll
    for (int nt = 0; nt < 5; ++nt) { eda[nt] = lds_ld<u32x2>(dk + 32 * nt); edb[nt] = lds_ld<u32x2>(dk + 160 + 32 * nt); asm volatile("" : "+v"(eda[nt]), "+v"(edb[nt])); }
    const u32 gwa = opaque(L0 + L_GW + g * 16);
    const u32x4 gw0 = lds_ld<u32x4>(gwa), gw1 = lds_ld<u32x4>(gwa + 64);
    accg[0] = mfma(lds_ld<u32x4>(pa1), gw1, mfma(X.a0, gw0, accg[0]));
    accg[1] = mfma(lds_ld<u32x4>(pa1 + 2048), gw1, mfma(X.a1, gw0, accg[1]));
    asm volatile("s_waitcnt lgkmcnt(0)" : "+v"(X.a0), "+v"(X.a1), "+v"(X.b0), "+v"(X.b1), "+v"(X.b2), "+v"(X.b3), "+v"(X.b4));
    tag = __builtin_amdgcn_readfirstlane(seed) + 1u;
    const u32x2 fxv = {tag, tag ^ FMIX};
    u32 so = L_A0;
#pragma unroll
    for (int s = 0; s < 4; s += 2) {
      u32 have = g1_half_peek(acc, Y, X, ax1 + so, ab1 + so, cfull);
      so = s == 2 ? 0 : so + STG;
      TR(4 + 4 * s);
      if (RARE(have < (u32)(s + 2))) sw_wait(cfull, s + 2);
      g1_half<true>(acc, X, Y, ax0 + so, ab0 + so);
      have = g1_half_peek(acc, Y, X, ax1 + so, ab1 + so, cfull);
      so += STG;
      post(myfree, s + 2);
      TR(5 + 4 * s);
      if (RARE(have < (u32)(s + 3))) sw_wait(cfull, s + 3);
      g1_half<true>(acc, X, Y, ax0 + so, ab0 + so);
      TR(6 + 4 * s);
    }
    waitv<0>();
    st8(rws, fxo_, fxv);
    {
      u32 have = g1_half_peek(acc, Y, X, ax1 + ring(4), ab1 + ring(4), cfull);
      if (RARE(have < 6u)) sw_wait(cfull, 6);
      g1_half<true>(acc, X, Y, ax0 + ring(5), ab0 + ring(5));
      have = g1_half_peek(acc, Y, X, ax1 + ring(5), ab1 + ring(5), cfull);
      if (RARE(have < 7u)) sw_wait(cfull, 7);
      g1_half<true>(acc, X, Y, ax0 + ring(6), ab0 + ring(6));
      TR(28);
      have = g1_half_peek(acc, Y, X, ax1 + ring(6), ab1 + ring(6), cfull);
      if (RARE(have < 8u)) sw_wait(cfull, 8);
      g1_half<true>(acc, X, Y, ax0 + ring(7), ab0 + ring(7));
      have = g1_half_peek(acc, Y, X, ax1 + ring(7), ab1 + ring(7), cfull);
      if (RARE(have < 9u)) sw_wait(cfull, 9);
      g1_half<true>(acc, X, Y, ax0 + ring(8), ab0 + ring(8));
      TR(30);
      have = g1_half_peek(acc, Y, X, ax1 + ring(8), ab1 + ring(8), cfull);
      if (RARE(have < 10u)) sw_wait(cfull, 10);
      TR(32);
      g1_half<true>(acc, X, Y, ax0 + ring(9), ab0 + ring(9));
      g1_half<true>(acc, Y, X, ax1 + ring(9), ab1 + ring(9));
      g1_last(acc, Y);
      post(L0 + C_G1 + 4 * w, 1u);
    }
    const u32x4 fr10 = peek4(L0 + C_G1);
    STAMP(2);
    TR(44);
    float v[8][5];
#pragma unroll
    for (int jj = 0; jj < 8; ++jj)
#pragma unroll
      for (int nt = 0; nt < 5; ++nt) v[jj][nt] = acc[jj >> 2][nt][jj & 3];
    float e[5];
#pragma unroll
    for (int nt = 0; nt < 5; ++nt) e[nt] = v[0][nt];
#pragma unroll
    for (int jj = 1; jj < 8; ++jj) scan_step(e, v[jj], e, lr, ls);
    {
      float z[5] = {0.f, 0.f, 0.f, 0.f, 0.f}, we[5];
      scan_step(we, z, e, sa, sb);
      auto s01 = __builtin_amdgcn_permlane32_swap(f2u(we[0]), f2u(we[1]), false, false);
      auto s23 = __builtin_amdgcn_permlane32_swap(f2u(we[2]), f2u(we[3]), false, false);
      auto s44 = __builtin_amdgcn_permlane32_swap(f2u(we[4]), f2u(we[4]), false, false);
      const float y01 = u2f(s01[0]) + u2f(s01[1]), y23 = u2f(s23[0]) + u2f(s23[1]), y4 = u2f(s44[0]) + u2f(s44[1]);
      auto q03 = __builtin_amdgcn_permlane16_swap(f2u(y01), f2u(y23), false, false);
      auto q44 = __builtin_amdgcn_permlane16_swap(f2u(y4), f2u(y4), false, false);
      if (RARE(min4(fr10) < 1u)) sw_wait4(L0 + C_G1, 1);
      const u32 wa = opaque(L0 + L_WS + w * 320 + i16 * 4);
      lds_st<float>(wa + (2 * (g & 1) + (g >> 1)) * 64, u2f(q03[0]) + u2f(q03[1]));
      lds_st<float>(wa + 256, u2f(q44[0]) + u2f(q44[1]));
      const u32 ra = opaque(L0 + L_ERAW + sig * 320 + i16 * 4);
#pragma unroll
      for (int nt = 0; nt < 5; ++nt) lds_st<float>(ra + nt * 64, e[nt]);
    }
    post(L0 + C_BAR + 4 * w, 1u);
    const u32 ap = opaque(L0 + L_WS + pl * 8), sgo = opaque(lane < 40 ? WS_SG + (u32)(c * 640 + 40 * j + lane) * 16 : OOB);
    sw_wait4(L0 + C_BAR, 1);
    if (w == 0) {
      const f32x2 se = (lds_ld<f32x2>(ap) + lds_ld<f32x2>(ap + 320)) + (lds_ld<f32x2>(ap + 640) + lds_ld<f32x2>(ap + 960));
      st16(rws, sgo, u32x4{f2u(se.x), tag, f2u(se.y), tag});
    }
    STAMP(3);
    const int nvalid = (c - w + 3) >> 2;
    const f32x2 lam4 = cpowk(aA, tA, 512.f);
    f32x2 lame = cpowk(aA, tA, (float)(128 * ((c - 1 - w) & 3)));
    asm volatile("" : "+v"(lame));
    if ((xcd & 1) ? ((r & 7) >= 2 && (r & 7) < 7) : (r & 7) < 5) {
      float gl = accg[0][0];
#pragma unroll
      for (int k = 1; k < 8; ++k) gl = (i16 == k) ? accg[k >> 2][k & 3] : gl;
      st8(rws, i16 < 8 ? WS_GG + (u32)(rot10 * 2048 + 128 * c + 32 * w + 8 * g + i16) * 8 : OOB, u32x2{f2u(gl), tag});
    }
    float pf[5];
    {
      u32 era = opaque(L0 + L_ERAW + pl * 8);
      f32x2 pp = {0.f, 0.f};
#pragma unroll 1
      for (int s2 = 0; s2 < w; ++s2, era += 1280) {
        const f32x2 e0 = lds_ld<f32x2>(era), e1 = lds_ld<f32x2>(era + 320), e2 = lds_ld<f32x2>(era + 640), e3 = lds_ld<f32x2>(era + 960);
        pp = cfma(cfma(cfma(cfma(pp, l8, e0), l8, e1), l8, e2), l8, e3);
      }
      const f32x2 e0 = lds_ld<f32x2>(era), e1 = lds_ld<f32x2>(era + 320), e2 = lds_ld<f32x2>(era + 640);
      const u32 ptb = L0 + L_PT + w * 1280;
      const u32 pw = opaque(ptb + pl * 8);
#pragma unroll
      for (int k = 0; k < 4; ++k) {
        const f32x2 q = cmul(pp, c2);
        lds_st<f32x2>(pw + k * 320, f32x2{q.x, -q.y});
        if (k < 3) pp = cfma(pp, l8, k == 0 ? e0 : k == 1 ? e1 : e2);
      }
      const u32 pr_ = opaque(ptb + g * 320 + i16 * 4);
#pragma unroll
      for (int nt = 0; nt < 5; ++nt) pf[nt] = lds_ld<float>(pr_ + nt * 64);
    }
    u32 hst[5];
    const u32 go = opaque(WS_GG + (u32)(g * 2048 + 64 * ti + 16 * kq + i16) * 8), go2 = opaque(g < 2 ? go + 8 * 2048 * 8 : OOB);
    const u32 hpr = opaque(L0 + L_HP + i16 * 16), htw = opaque(L0 + L_HT + w * 1280 + i16 * 80 + g * 16),
              htr = opaque(L0 + L_HT + w * 1280 + (2 * ((lane >> 2) & 3) + (g & 1)) * 80 + (g >> 1) * 32 + (lane & 3) * 8);
    {
      const u32 hb0 = WS_H + (u32)(8 * c + 2 * w + (g >> 1)) * 40960 + (g & 1) * 2048 + i16 * 16;
#pragma unroll
      for (int nt = 0; nt < 5; ++nt) {
        const int o = 5 * j + nt;
        hst[nt] = opaque(hb0 + (u32)((o >> 3) * 4096 + ((o >> 2) & 1) * 1024 + (o & 3) * 256));
      }
      u32 off[4];
      f32x2 mk[3];
#pragma unroll
      for (int k = 0; k < 4; ++k) off[k] = opaque((k < nvalid && lane < 40) ? WS_SG + (u32)((w + 4 * k) * 640 + 40 * j + lane) * 16 : OOB);
#pragma unroll
      for (int k = 0; k < 3; ++k) { mk[k] = k + 1 < nvalid ? lam4 : f32x2{1.f, 0.f}; asm volatile("" : "+v"(mk[k])); }
      const u32 hp = opaque(L0 + L_HP + min(lane, 40) * 32 + w * 4);
      u32x4 sv[4];
#pragma unroll
      for (int k = 0; k < 4; ++k) sv[k] = ld16(rws, off[k]);
      SB();
      const float z[5] = {0.f, 0.f, 0.f, 0.f, 0.f};
#pragma unroll
      for (int jj = 0; jj < 8; ++jj) scan_step(v[jj], z, v[jj], ca, cb);
#pragma unroll
      for (int nt = 0; nt < 5; ++nt) ls[nt] = -ls[nt];
      asm volatile("" : "+v"(v[7][4]), "+v"(ls[4]));
      SB();
#pragma unroll 1
      for (int it = 0; it < SPIN_CAP; ++it) {
        bool ok = true;
#pragma unroll
        for (int k = 0; k < 4; ++k) ok &= (off[k] == OOB) | ((sv[k].y == tag) & (sv[k].w == tag));
        if (wave_all(ok)) break;
        asm volatile("" ::: "memory");
#pragma unroll
        for (int k = 0; k < 4; ++k) sv[k] = ld16(rws, off[k]);
      }
      asm volatile("" ::: "memory");
      STAMP(4);
      f32x2 hsum = {u2f(sv[0].x), u2f(sv[0].z)};
#pragma unroll
      for (int k = 1; k < 4; ++k) hsum = cfma(hsum, mk[k - 1], f32x2{u2f(sv[k].x), u2f(sv[k].z)});
      hsum = cmul(hsum, lame);
      lds_st<float>(hp, hsum.x);
      lds_st<float>(hp + 16, hsum.y);
    }
    post(L0 + C_BAR + 4 * w, 2u);
    u32x2 g0 = ld8(rws, go), g1 = ld8(rws, go + 4 * 2048 * 8), g2 = ld8(rws, go2);
    SB();
    sw_wait4(L0 + C_BAR, 2);
    {
      float hin[5], h0[5];
#pragma unroll
      for (int nt = 0; nt < 5; ++nt) {
        const f32x4 q = lds_ld<f32x4>(hpr + nt * 256);
        hin[nt] = (q.x + q.y) + (q.z + q.w);
      }
      scan_step(h0, pf, hin, ha, hb);
      scan_step(v[0], v[0], h0, lr, ls);
#pragma unroll
      for (int jj = 1; jj < 8; ++jj) scan_step(v[jj], v[jj], v[jj - 1], lr, ls);
    }
    {
      const u32 wa = htw, ra = htr;
      u32x2 lo[5], hi[5];
#pragma unroll
      for (int nt = 0; nt < 5; ++nt) {
        lds_st<u32x4>(wa, u32x4{pk(v[0][nt], v[1][nt]), pk(v[2][nt], v[3][nt]), pk(v[4][nt], v[5][nt]), pk(v[6][nt], v[7][nt])});
        lo[nt] = __builtin_bit_cast(u32x2, lds_tr(ra));
        hi[nt] = __builtin_bit_cast(u32x2, lds_tr(ra + 640));
      }
      SB();
#pragma unroll
      for (int nt = 0; nt < 5; ++nt) st16(rws, hst[nt], u32x4{lo[nt].x, lo[nt].y, hi[nt].x, hi[nt].y});
    }
    post(L0 + C_BAR + 4 * w, 3u);
    STAMP(5);
#pragma unroll 1
    for (int it = 0; it < SPIN_CAP && RARE(!wave_all((g0.y == tag) & (g1.y == tag) & ((g2.y == tag) | (g >= 2)))); ++it) {
      __builtin_amdgcn_s_sleep(1);
      asm volatile("" ::: "memory");
      g0 = ld8(rws, go); g1 = ld8(rws, go + 4 * 2048 * 8); g2 = ld8(rws, go2);
    }
    float gate, ogate;
    {
      const float s3 = (u2f(g0.x) + u2f(g1.x)) + u2f(g2.x);
      auto sa_ = __builtin_amdgcn_permlane32_swap(f2u(s3), f2u(s3), false, false);
      const float s2 = u2f(sa_[0]) + u2f(sa_[1]);
      auto sb_ = __builtin_amdgcn_permlane16_swap(f2u(s2), f2u(s2), false, false);
      gate = rbf(__builtin_amdgcn_rcpf(1.f + __builtin_amdgcn_exp2f(-1.44269504088896f * rbf(u2f(sb_[0]) + u2f(sb_[1])))));
      ogate = rbf(1.f - gate);
      asm volatile("" : "+v"(gate), "+v"(ogate));
    }
    f32x4 base[5];
#pragma unroll
    for (int nt = 0; nt < 5; ++nt) {
#pragma unroll
      for (int e = 0; e < 2; ++e) {
        base[nt][2 * e] = bflo(xq[nt][e]) * __builtin_fmaf(gate, bflo(eda[nt][e]), ogate * bflo(edb[nt][e]));
        base[nt][2 * e + 1] = bfhi(xq[nt][e]) * __builtin_fmaf(gate, bfhi(eda[nt][e]), ogate * bfhi(edb[nt][e]));
      }
      asm volatile("" : "+v"(base[nt]));
    }
    waitv<0>();
    st8(rws, lane == 0 ? WS_FH + (u32)((c * 4 + w) * 16 + j) * 128 : OOB, u32x2{tag, tag ^ FMIX});
    STAMP(14);
    __builtin_amdgcn_s_setprio(0);
    sw_wait(ccf, 1);
    G2_REGS(cr, 2, L_A0 + 3 * 10240);
    post(L0 + C_C0 + 4 * w, 1u);
    u32x4 held[5][5];
    {
      const u32 v0 = (u32)(80 * dj + 40 * (w & 1) + (lane >> 3)) * 1280 + (((lane & 7) ^ (lane >> 4) ^ (4 * (w & 1))) << 4), v1 = v0 ^ 64;
#pragma unroll
      for (int pr = 0; pr < 5; ++pr) {
        const int u = (w >> 1) ? (pr < 3 ? 14 + 2 * pr : pr == 3 ? 7 : 9) : (pr < 4 ? 13 + 2 * pr : 8);
#pragma unroll
        for (int k = 0; k < 5; ++k)
          held[pr][k] = __builtin_amdgcn_raw_buffer_load_b128((u & 1) ? rCim : rCre, (k & 1) ? v1 : v0, (u >> 1) * 128 + k * 10240, 0);
      }
      SB();
    }
    STAMP(15);
    sw_wait4(L0 + C_BDONE, 1);
    {
      const u32 fb = opaque(L0 + L_B0 + (w >> 1) * 10240 + (w & 1) * 5120 + lane * 16);
      const u32 f3 = opaque(L0 + ((w >> 1) ? L_A0 + 5 * 10240 : L_B0 + 6 * 10240) + (w & 1) * 5120 + lane * 16);
      const u32 f4 = opaque(L0 + L_A0 + (6 + (w >> 1)) * 10240 + (w & 1) * 5120 + lane * 16);
#pragma unroll
      for (int pr = 0; pr < 5; ++pr)
#pragma unroll
        for (int k = 0; k < 5; ++k) lds_st<u32x4>((pr < 3 ? fb + pr * 20480 : pr == 3 ? f3 : f4) + k * 1024, held[pr][k]);
      post(L0 + C_FLUSH + 4 * w, 1u);
    }
    sw_wait(L0 + C_HOK, 1);
    STAMP(6);
    G2_LOADH();
    STAMP(7);
    G2_INIT(L_A0);

    g2_half0<false>(y, cr[0], hf_[0]); g2_half0<false>(y, cr[1], hf_[1]); g2_half0<false>(y, cr[2], hf_[2]);
    sw_wait(ccf, 3);
    sw_wait4(L0 + C_FLUSH, 1);
    g2_half<0, 4>(y, P0, cr[3], hf_[3], cab[0][0]);
    STAMP(8);
    G2_UN(2, 0); G2_UN(3, 1); G2_UN(4, 2); G2_UN(5, 3);
    STAMP(9);
    G2_UN(6, 4);
    lds_st<float>(opaque(L0 + L_GATE + (16 * w + i16) * 4), gate);
    post(L0 + C_BAR + 4 * w, 4u);
    waitl();
    const u32 bo = WS_BT + (u32)(80 * dj + (lane >> 2)) * 8192 + (64 * ti + 16 * w) * 4 + (lane & 3) * 16;
    u32x4 bv[5];
#pragma unroll
    for (int k = 0; k < 5; ++k) bv[k] = ld16(rws, bo + k * 16 * 8192);
    SB();
    G2_UN(7, 5);
    waitl();
    STAMP(10);
#pragma unroll 1
    for (int it = 0; it < SPIN_CAP; ++it) {
      bool ok = true;
#pragma unroll
      for (int k = 0; k < 5; ++k) ok &= (bv[k].y == tag) & (bv[k].w == tag);
      if (!RARE(!wave_all(ok))) break;
      __builtin_amdgcn_s_sleep(2);
      asm volatile("" ::: "memory");
#pragma unroll
      for (int k = 0; k < 5; ++k) bv[k] = ld16(rws, bo + k * 16 * 8192);
    }
    STAMP(11);
    sw_wait4(L0 + C_BAR, 4);
    {
      const u32 ba = opaque(L0 + L_BST + w * 2560 + lane * 8);
#pragma unroll
      for (int k = 0; k < 5; ++k) lds_st<u32x2>(ba + k * 512, u32x2{bv[k].x, bv[k].z});
      u32x2 yb[5];
#pragma unroll
      for (int nt = 0; nt < 5; ++nt) yb[nt] = __builtin_bit_cast(u32x2, lds_tr(ba + nt * 512));
      SB();
#pragma unroll
      for (int nt = 0; nt < 5; ++nt) {
        base[nt] += ogate * f32x4{bflo(yb[nt].x), bfhi(yb[nt].x), bflo(yb[nt].y), bfhi(yb[nt].y)};
        asm volatile("" : "+v"(base[nt]));
      }
    }
    G2_UN(8, 6); G2_ULAST(9, 7);
    {
      const u32 ia = opaque(ima);
#pragma unroll
      for (int nt = 0; nt < 5; ++nt) lds_st<f32x4>(ia + nt * 64, gate * y[nt] + base[nt]);
    }
  } else {
    const int pr = xcd * 16 + (r >> 1);
    const int nch = (r & 1) ? 2 : 3, ch = 5 * pr + ((r & 1) ? 3 : 0) + kq;
    const u32 slot = opaque(L0 + L_B0 + kq * BSLOT) ;
    f32x4 ya[8];
    if (__builtin_expect(kq < nch, 1)) {
      const GAS u32* pa = (const GAS u32*)P.Are + ch * 64 + lane;
      const u32 wAre = *pa, wAim = ((const GAS u32*)P.Aim)[ch * 64 + lane];
      const u32 wBre = ((const GAS u32*)P.sBre)[ch * 64 + lane], wBim = ((const GAS u32*)P.sBim)[ch * 64 + lane];
      const u32 wCre = ((const GAS u32*)P.sCre)[ch * 64 + lane], wCim = ((const GAS u32*)P.sCim)[ch * 64 + lane];
      const u32 wdt = ((const GAS u16*)P.ldtB)[ch];
      asm volatile("s_waitcnt vmcnt(0)" : "+v"(pfv) :: "memory");
      const float dt = rbf(expf(bf2f(wdt)));
      f32x4 ka[3] = {f32x4{0.f, 0.f, 0.f, 0.f}, f32x4{0.f, 0.f, 0.f, 0.f}, f32x4{0.f, 0.f, 0.f, 0.f}};
      const u32 tr = opaque(slot + i16 * TROW + g * 16);
      const u32 fxo = lane < 32 ? WS_FX + (u32)((lane * 8 + ch / 80) * 4 + (ch % 80) / 20) * 128 : OOB;
      u32 tv[56], tu[56];
#pragma unroll 1
      for (int rd = 0; rd < 2; ++rd) {
#pragma unroll
        for (int k = 0; k < 56; ++k) tu[k] = tv[k];
        const u32 sh = rd ? 0u : 16u;
        const float a = dt * bfhi(wAre << sh), t = dt * bfhi(wAim << sh);
        const f32x2 db = {dt * bfhi(wBre << sh), dt * bfhi(wBim << sh)}, cc = {bfhi(wCre << sh), bfhi(wCim << sh)};
        const f32x2 W = cmul(cc, db);
        const f32x2 z1 = cpowk(a, t, 1.f), z8 = cpowk(a, t, 8.f), z128 = cpowk(a, t, 128.f);
        {
          f32x2 cv = z8;
          tv[41] = pk(cv.x, cv.y);
#pragma unroll
          for (int jj = 2; jj < 16; ++jj) { cv = cmul(cv, z8); tv[40 + jj] = pk(cv.x, cv.y); }
        }
        {
          const f32x2 y1 = {z1.x, -z1.y}, yh = {z128.x, -z128.y};
          f32x2 R[8];
          R[0] = f32x2{W.x, -W.y};
#pragma unroll
          for (int k = 1; k < 8; ++k) R[k] = cmul(R[k - 1], y1);
#pragma unroll
          for (int k = 0; k < 8; ++k) tv[k] = pk(R[k].x, R[k].y);
#pragma unroll
          for (int k = 0; k < 8; ++k) { R[k] = cmul(R[k], yh); tv[8 + k] = pk(R[k].x, R[k].y); }
#pragma unroll
          for (int hh = 0; hh < 2; ++hh)
#pragma unroll
            for (int k = 0; k < 4; ++k) { R[2 * k] = cmul(R[2 * k], yh); tv[16 + 4 * hh + k] = pk(R[2 * k].x, R[2 * k].y); }
#pragma unroll
          for (int hh = 0; hh < 4; ++hh)
#pragma unroll
            for (int k = 0; k < 2; ++k) { R[4 * k] = cmul(R[4 * k], yh); tv[24 + 2 * hh + k] = pk(R[4 * k].x, R[4 * k].y); }
#pragma unroll
          for (int hh = 0; hh < 9; ++hh) { R[0] = cmul(R[0], yh); tv[32 + hh] = pk(R[0].x, R[0].y); }
        }
      }
      STAMP(1);
      get_tag();
      const u32 kc = slot + KC_OFF;
      const bool r1 = lane < 32;
      const u32 xa = opaque(kc - 4 + (r1 ? 16 * lane : 256 + 8 * lane)), xb = opaque(xa + (r1 ? 12 : 8));
      const u32 sA = opaque(r1 ? 0x05040706u : 0x03020504u), sB = opaque(r1 ? 0x05040706u : 0x05040504u), sC = opaque(r1 ? 0x03020504u : 0x01000302u),
                sD = opaque(r1 ? 0x03020504u : 0x05040504u), sE = opaque(lane <= 32 ? 0x03020504u : 0x05040504u);
      const u32 ka0 = opaque(kc + (128 * (g >> 1) + 8 * i16 + 4 * (g & 1)) * 2);
      const u32 ka1 = opaque(kc + (g < 2 ? 256 + 64 * g + 4 * i16 : 256 + 64 * g + 2 * i16) * 2), ka1b = opaque(ka1 + (g < 2 ? 4 : 64));
      const u32 ka2 = opaque(kc + (512 + 64 * min(g, 2) + i16) * 2);
      const u32 sbase = __builtin_amdgcn_readfirstlane(slot);
      const u32 uo = opaque(WS_XT + (u32)(ch >> 2) * 16384 + (ch & 3) * 16 + lane * 64), uw = opaque(slot + UPAD + lane * 16);
      sw_wait(L0 + C_FULL, 10);
      const u32x2 fx = ld8(rws, fxo);
      sw_wait4(L0 + C_G1, 1);
      STAMP(6);
#define TW(v, row) asm volatile("ds_write_addtid_b32 %0 offset:%1" :: "v"(v), "n"((row) * TROW) : "memory")
      asm volatile("s_mov_b32 m0, %0\n\ts_nop 0" :: "s"(sbase) : "m0", "memory");
      TW(0x00003f80u, 41);
      u32x4 uv[4];
#pragma unroll
      for (int rd = 0; rd < 2; ++rd) {
#pragma unroll
        for (int k = 0; k < 56; ++k) TW(rd ? tv[k] : tu[k], k < 41 ? k : k + 1);
        u32x4 fr[4][4];
#pragma unroll
        for (int ks = 0; ks < 4; ++ks)
#pragma unroll
          for (int m = 0; m < 4; ++m) fr[ks][m] = lds_ld<u32x4>(tr + (m == 0 ? 41 : 16 * (m - 1)) * TROW + ks * 64);
        SB();
#pragma unroll
        for (int ks = 0; ks < 4; ++ks)
#pragma unroll
          for (int m = 0; m < 3; ++m) ka[m] = mfma(fr[ks][m + 1], fr[ks][0], ka[m]);
        SB();
      }
      if (RARE(!flag_ok(fx, fxo, tag))) poll_flags(rws, fxo, tag);
      asm volatile("" ::: "memory");
#pragma unroll
      for (int k = 0; k < 4; ++k) uv[k] = ld16(rws, uo + k * 4096);
      SB();
#define UW(v, off) asm volatile("ds_write_addtid_b32 %0 offset:%1" :: "v"(v), "n"(off) : "memory")
#pragma unroll
      for (int cc = 0; cc < 4; ++cc) { UW(0u, cc * UCOPY); UW(0u, cc * UCOPY + 144); }
      STAMP(5);
      {
        const u32 q0 = pk(ka[2].x, ka[2].y), q1 = pk(ka[2].z, ka[2].w);
        if (g < 3) { lds_st<u16>(ka2, (u16)q0); lds_st<u16>(ka2 + 32, (u16)(q0 >> 16)); lds_st<u16>(ka2 + 64, (u16)q1); lds_st<u16>(ka2 + 96, (u16)(q1 >> 16)); }
        lds_st<u32x2>(ka0, u32x2{pk(ka[0].x, ka[0].y), pk(ka[0].z, ka[0].w)});
        lds_st<u32>(ka1, pk(ka[1].x, ka[1].y));
        lds_st<u32>(ka1b, pk(ka[1].z, ka[1].w));
        lds_st<u32>(kc - 4, 0u);
      }
#define PERM(hi, lo, sel) __builtin_amdgcn_perm(hi, lo, sel)
      {
        const u32 R0 = lds_ld<u32>(xa), R1 = lds_ld<u32>(xa + 4), R2 = lds_ld<u32>(xa + 8), R3 = lds_ld<u32>(xb), R4 = lds_ld<u32>(xb + 4);
        const u32 ea_ = opaque(kc + 768 + 4 * lane), eb_ = opaque(kc + 1024 + 2 * lane);
        const u32 E0 = lds_ld<u32>(ea_), E1 = lds_ld<u32>(ea_ + 4);
        const u32 f0 = lds_ld<u16>(eb_), f1 = lds_ld<u16>(eb_ + 2), f2 = lds_ld<u16>(eb_ + 128), f3 = lds_ld<u16>(eb_ + 130);
        UW(0u, KOFF); UW(0u, KOFF + KCOPY);
        const u32 k0 = opaque(slot + KOFF + KPAD + lane * 16);
        lds_st<u32x4>(k0 + KCOPY, u32x4{PERM(R4, R3, sA), PERM(R3, R2, sB), PERM(R2, R1, sA), PERM(R1, R0, sB)});
        lds_st<u32x4>(k0, u32x4{PERM(R4, R3, sC), PERM(R3, R2, sD), PERM(R2, R1, sC), PERM(R1, R0, sE)});
        const u32 e00 = PERM(E0, E0, 0x01000100u), e11 = PERM(E0, E0, 0x03020302u);
        lds_st<u32x4>(k0 + KCOPY + 1024, u32x4{PERM(E1, E1, 0x01000100u), e11, PERM(E0, E0, 0x01000302u), e00});
        lds_st<u32x4>(k0 + 1024, u32x4{PERM(E0, E1, 0x07060100u), e11, e00, e00});
#pragma unroll
        for (int rr = 0; rr < 2; ++rr) {
          const u32 e0 = rr ? f2 : f0, e1 = rr ? f3 : f1, t4 = (lane & 1) ? e1 : e0;
          const u32 c00 = e0 | (e0 << 16), c11 = e1 | (e1 << 16);
          lds_st<u32x4>(k0 + KCOPY + 2048 + 1024 * rr, u32x4{c11, e1 | (t4 << 16), c00, c00});
          lds_st<u32x4>(k0 + 2048 + 1024 * rr, u32x4{c11, t4 | (e0 << 16), c00, c00});
        }
      }
#pragma unroll
      for (int k = 0; k < 4; ++k) {
        lds_st<u32x4>(uw + k * 1024, uv[k]);
#pragma unroll
        for (int e = 0; e < 4; ++e) { lds_st<u32>(uw + k * 1024 + UCOPY + 4 + 4 * e, uv[k][e]); lds_st<u32>(uw + k * 1024 + 3 * UCOPY + 12 + 4 * e, uv[k][e]); }
        lds_st<u32x2>(uw + k * 1024 + 2 * UCOPY + 8, u32x2{uv[k].x, uv[k].y});
        lds_st<u32x2>(uw + k * 1024 + 2 * UCOPY + 16, u32x2{uv[k].z, uv[k].w});
      }
      post(L0 + C_BPREP + 4 * kq, 1u);
      STAMP(2);
#pragma unroll
      for (int A = 0; A < 8; ++A) ya[A] = f32x4{0.f, 0.f, 0.f, 0.f};
      const u32 ta = opaque(slot + KOFF + KPAD + (i16 & 1) * KCOPY + ((i16 >> 1) - g) * 16 - 64);
      const u32 ua = opaque(slot + (i16 & 3) * UCOPY + (i16 >> 2) * 128 + g * 16);
      u32x4 T[2][8], U[2][8];
#define CV_READ(S, st)                                                                                        \
      _Pragma("unroll") for (int D = 0; D < 8; ++D) {                                                         \
        T[S][D] = lds_ld<u32x4>(ta + ((st) >> 1) * 128 + D * 512 + (1 - ((st) & 1)) * 64);                   \
        U[S][D] = lds_ld<u32x4>(ua + UPAD - ((st) >> 1) * 128 + D * 512 + ((st) & 1) * 64);                  \
      }
      CV_READ(0, 0);
      __builtin_amdgcn_s_setprio(2);
#pragma unroll
      for (int st = 0; st < 8; ++st) {
        SB();
        if (st < 7) { CV_READ((st + 1) & 1, st + 1); }
#pragma unroll
        for (int S = 0; S < 8; ++S)
#pragma unroll
          for (int D = 0; D <= S; ++D) ya[S] = mfma(T[st & 1][D], U[st & 1][S - D], ya[S]);
        if (st < 7) {
#pragma unroll
          for (int k = 0; k < 16; ++k) {
            __builtin_amdgcn_sched_group_barrier(0x8, 2, 0);
            __builtin_amdgcn_sched_group_barrier(0x100, 1, 0);
          }
          __builtin_amdgcn_sched_group_barrier(0x8, 4, 0);
        }
      }
      SB();
      STAMP(3);
    } else {
      get_tag();
      sw_wait4(L0 + C_G1, 1);
      post(L0 + C_BPREP + 4 * kq, 1u);
#pragma unroll
      for (int A = 0; A < 8; ++A) ya[A] = f32x4{0.f, 0.f, 0.f, 0.f};
    }
    sw_wait(ccf, 1);
    G2_REGS(cr, 3, L_A0 + 5 * 10240);
    post(L0 + C_BDONE + 4 * kq, 1u);
    {
      const bool odd = lane & 1;
      const u32 bo = kq < nch ? WS_BT + (u32)ch * 8192 + (i16 >> 2) * 256 + g * 64 + (odd ? 0 : 32) + ((lane & 2) ? 0 : 16) : OOB;
#pragma unroll
      for (int A = 0; A < 8; ++A) {
        const u32 x0 = pk(ya[A].x, ya[A].y), x1 = pk(ya[A].z, ya[A].w);
        const u32 rv = __builtin_amdgcn_update_dpp(0u, odd ? x1 : x0, 0xB1, 0xf, 0xf, true);
        st16(rws, bo + A * 1024, u32x4{odd ? x0 : rv, tag, odd ? rv : x1, tag});
      }
    }
    sw_wait(L0 + C_HOK, 1);
    __builtin_amdgcn_s_setprio(0);
    STAMP(4);
    G2_B();
    asm volatile(".Lfk_tail:");
  }

  hwbar();
  STAMP(12);
  if (b == 255 && tid == 0) __hip_atomic_store((GAS u32*)P.ws, tag, __ATOMIC_RELAXED, __HIP_MEMORY_SCOPE_AGENT);
  {
    const int et = tid >> 3, es = tid & 7;
    const u32 ia = opaque(L0 + L_IMG0 + et * 320 + ((es & 4) | ((es ^ (et >> 1)) & 3)) * 16);
    const float gt = lds_ld<float>(opaque(L0 + L_GATE + et * 4));
    const rsrc_t ro = mksrd(P.out, 2048 * 2560);
    const u32 oo = (64 * ti + et) * 2560 + dj * 320 + es * 16;
    const f32x4 o0 = lds_ld<f32x4>(ia) + gt * lds_ld<f32x4>(ia + (L_IMG1 - L_IMG0));
    const f32x4 o1 = lds_ld<f32x4>(ia + 128) + gt * lds_ld<f32x4>(ia + 128 + (L_IMG1 - L_IMG0));
    const f32x4 o2 = lds_ld<f32x4>(ia + 256) + gt * lds_ld<f32x4>(ia + 256 + (L_IMG1 - L_IMG0));
    st16(ro, oo, __builtin_bit_cast(u32x4, o0));
    st16(ro, oo + 128, __builtin_bit_cast(u32x4, o1));
    st16(ro, es < 4 ? oo + 256 : OOB, __builtin_bit_cast(u32x4, o2));
  }
  DUMP();
  STAMP(13);
}

extern "C" void kernel_launch(void* const* d_in, const int* in_sizes, int n_in, void* d_out, int out_size, void* d_ws,
                              size_t ws_size, hipStream_t stream) {
  Params p;
  p.x = (const u16*)d_in[0];
  p.Lre = (const u16*)d_in[1];
  p.Lim = (const u16*)d_in[2];
  p.Bre = (const u16*)d_in[3];
  p.Bim = (const u16*)d_in[4];
  p.Cre = (const u16*)d_in[5];
  p.Cim = (const u16*)d_in[6];
  p.ldtA = (const u16*)d_in[7];
  p.DA = (const u16*)d_in[8];
  p.Are = (const u16*)d_in[10];
  p.Aim = (const u16*)d_in[11];
  p.sBre = (const u16*)d_in[12];
  p.sBim = (const u16*)d_in[13];
  p.sCre = (const u16*)d_in[14];
  p.sCim = (const u16*)d_in[15];
  p.ldtB = (const u16*)d_in[16];
  p.DB = (const u16*)d_in[17];
  p.gw = (const u16*)d_in[18];
  p.out = (float*)d_out;
  p.ws = (char*)d_ws;
  fused_kernel<<<256, 512, 0, stream>>>(p);
}
```
